```python
import torch

def _softmax(x, axis=-1):
    return torch.softmax(x, dim=axis)
def _gelu(x):
    return torch.nn.functional.gelu(x, approximate="tanh")
def _silu(x):
    return torch.nn.functional.silu(x)
def _sigmoid(x):
    return torch.sigmoid(x)
def _layernorm(x, axis=-1, eps=1e-5):
    m = x.mean(dim=axis, keepdim=True)
    v = x.var(dim=axis, keepdim=True, unbiased=False)
    return (x - m) / torch.sqrt(v + eps)
def _scatter_add(tgt, idx, src, axis=0):
    return tgt.clone().index_add_(axis, idx, src)
def _segment_sum(src, seg, n):
    out = torch.zeros((n,) + src.shape[1:], dtype=src.dtype)
    return out.index_add_(0, seg, src)
def _pad(x, pw):
    flat = tuple(v for pair in reversed(pw) for v in pair)
    return torch.nn.functional.pad(x, flat)

kernel_name = 'standalone__softmax_full_causal__prompt'

def setup_inputs():

    _w = (lambda a: (lambda t: t.to(torch.bfloat16) if t.is_floating_point() else t)(torch.as_tensor(a)))
    B, H, S, D = 1, 32, 2048, 128
    return {
        "q": _w((lambda *s: torch.randn(*s, dtype=torch.bfloat16) * 0.8)(B, H, S, D)),
        "k": _w((lambda *s: torch.randn(*s, dtype=torch.bfloat16) * 0.8)(B, H, S, D)),
        "v": _w((lambda *s: torch.randn(*s, dtype=torch.bfloat16) * 0.05)(B, H, S, D)),
    }

def reference(q, k, v):
    B, H, S, D = q.shape[0], q.shape[1], q.shape[2], q.shape[3]
    scale = D ** -0.5
    scores = torch.einsum('bhqd,bhkd->bhqk', q, k) * scale
    qi = torch.reshape(torch.arange(S), (1, 1, S, 1))
    kj = torch.reshape(torch.arange(S), (1, 1, 1, S))
    causal_bias = (lambda x, dt: x.to(dt))(kj > qi, scores.dtype) * (-1e9)
    attn = _softmax(scores + causal_bias, axis=-1)
    return torch.einsum('bhqk,bhkd->bhqd', attn, v)
```

```cpp
#include <hip/hip_runtime.h>
#include <hip/hip_bf16.h>
#include <cstdio>
#include <cstdint>

enum { ORDER_NATURAL = 0, ORDER_REVERSED = 1, ORDER_PAIRED = 2, ORDER_XCD = 4 };
using TIN  = __hip_bfloat16;
using TOUT = __hip_bfloat16;
constexpr int B = 1, H = 32, HKV = 32, SQ = 2048, SKV = 2048, D = 128;
constexpr int QOFF = 0;
constexpr int WINDOW = SKV;
constexpr float THR = 8.f;
constexpr int ORDER = ORDER_PAIRED | ORDER_XCD;
constexpr int GRID_WGS = 0;
constexpr bool WSKIP = WINDOW <= 512;
constexpr float SCALE = 0.08838834764831845f;
constexpr int NW = 8, QBLK = 32, KVBLK = 64, QB = NW * QBLK;
constexpr int SHM_V = KVBLK * D * 2, SHM_K = KVBLK * D * 2;
constexpr int LDS_BYTES = 2 * SHM_V + 2 * SHM_K + NW * 64 * 4;
static_assert(D == 128 && SQ % QB == 0 && SKV % KVBLK == 0 && H % HKV == 0 && QOFF >= 0 && QOFF + SQ <= SKV && WINDOW >= 1, "geometry");

using bf16 = __hip_bfloat16;
typedef short bf16x8 __attribute__((ext_vector_type(8)));
typedef short s16x4 __attribute__((ext_vector_type(4)));
typedef float f32x16 __attribute__((ext_vector_type(16)));
typedef float f32x4 __attribute__((ext_vector_type(4)));
typedef unsigned u32x4 __attribute__((ext_vector_type(4)));
template <class A, class Bt> struct same_t { static constexpr bool v = false; };
template <class A> struct same_t<A, A> { static constexpr bool v = true; };

#define KSWZ(row, colB) ((row) * 256 + ((colB) ^ (((row) & 7) << 4)))
#define SBAR() __builtin_amdgcn_sched_barrier(0)
__device__ __forceinline__ int v_st(int k, int c) { const int kk = (k & ~0xC) | ((k & 4) << 1) | ((k & 8) >> 1); return ((kk >> 3) * 4 + (c >> 5)) * 512 + ((kk & 7) * 32 + (c & 31)) * 2; }
__device__ __forceinline__ int v_rd_base(int lane) { return ((lane & 3) << 3) | (((lane >> 2) & 3) << 6) | (((lane >> 4) & 1) << 5) | (((lane >> 5) & 1) << 8); }
constexpr int v_rd_off(int d0, int ks, int half) { return d0 * 512 + ks * 4096 + half * 2048; }
__device__ __forceinline__ int crow(int r, int hi) { return (r & 3) + 8 * (r >> 2) + 4 * hi; }
__device__ __forceinline__ unsigned cvtpk(float lo, float hi) {
    unsigned r; asm volatile("v_cvt_pk_bf16_f32 %0, %1, %2" : "=v"(r) : "v"(lo), "v"(hi)); return r;
}
__device__ __forceinline__ bf16x8 pack8(f32x4 a, f32x4 b) {
    u32x4 w = {cvtpk(a[0], a[1]), cvtpk(a[2], a[3]), cvtpk(b[0], b[1]), cvtpk(b[2], b[3])};
    return *reinterpret_cast<bf16x8*>(&w);
}
template <class T> __device__ __forceinline__ bf16x8 load8(const T* p) {
    if constexpr (same_t<T, float>::v) { return pack8(*(const f32x4*)p, *(const f32x4*)(p + 4)); }
    else { return *reinterpret_cast<const bf16x8*>(p); }
}
__device__ __forceinline__ void mask_tile(f32x16& p0, f32x16& p1, int dq, unsigned W) {
    const float NEG = -__builtin_inff();
#pragma unroll
    for (int r = 0; r < 16; ++r) {
        const int c = (r & 3) + 8 * (r >> 2);
        if ((unsigned)(dq - c) >= W) p0[r] = NEG;
        if ((unsigned)(dq - c - 32) >= W) p1[r] = NEG;
    }
}
__device__ __forceinline__ void partialSM(f32x16& p0, f32x16& p1, float& m_reg, float& mn, float& alpha) {
    float pmax = p0[0]; for (int r = 1; r < 16; ++r) pmax = fmaxf(pmax, p0[r]); for (int r = 0; r < 16; ++r) pmax = fmaxf(pmax, p1[r]);
    { auto rr = __builtin_amdgcn_permlane32_swap(__float_as_uint(pmax), __float_as_uint(pmax), false, false);
      pmax = fmaxf(__uint_as_float(rr[0]), __uint_as_float(rr[1])); }
    constexpr float C2 = 1.4426950408889634f * SCALE;
    if (__builtin_expect(__all((pmax - m_reg) * SCALE <= THR), 1)) { mn = m_reg; alpha = 1.f; }
    else { mn = fmaxf(m_reg, pmax); alpha = __builtin_amdgcn_exp2f((m_reg - mn) * C2); m_reg = mn; }
    const float mnL = -mn * C2;
    for (int r = 0; r < 16; ++r) p0[r] = fmaf(p0[r], C2, mnL); for (int r = 0; r < 16; ++r) p1[r] = fmaf(p1[r], C2, mnL);
    for (int r = 0; r < 16; ++r) p0[r] = __builtin_amdgcn_exp2f(p0[r]);
}
__device__ __forceinline__ void finishSM(f32x16& p0, f32x16& p1, float alpha, float& l_reg, bf16x8& pa0, bf16x8& pa1, bf16x8& pa2, bf16x8& pa3) {
    for (int r = 0; r < 16; ++r) p1[r] = __builtin_amdgcn_exp2f(p1[r]);
    float ps = 0; for (int r = 0; r < 16; ++r) ps += p0[r]; for (int r = 0; r < 16; ++r) ps += p1[r];
    { auto rr = __builtin_amdgcn_permlane32_swap(__float_as_uint(ps), __float_as_uint(ps), false, false);
      ps = __uint_as_float(rr[0]) + __uint_as_float(rr[1]); }
    l_reg = l_reg * alpha + ps;
#define PK4(P, B_, OUT) do { unsigned a0 = cvtpk(P[B_+0], P[B_+1]), a1 = cvtpk(P[B_+2], P[B_+3]);                          \
        unsigned b0 = cvtpk(P[B_+4], P[B_+5]), b1 = cvtpk(P[B_+6], P[B_+7]);                                             \
        auto r0 = __builtin_amdgcn_permlane32_swap(a0, b0, false, false); auto r1 = __builtin_amdgcn_permlane32_swap(a1, b1, false, false); \
        u32x4 w = {r0[0], r1[0], r0[1], r1[1]}; OUT = *reinterpret_cast<bf16x8*>(&w); } while (0)
    PK4(p0, 0, pa0); PK4(p0, 8, pa1); PK4(p1, 0, pa2); PK4(p1, 8, pa3);
#undef PK4
}
template <int KB, bool SK>
__device__ __forceinline__ void qkt(f32x16& p0, f32x16& p1, const char* K_lds, int r32, int hi, const bf16x8* qr, bool act) {
    if (SK && !act) { const float NEG = -__builtin_inff();
#pragma unroll
        for (int r = 0; r < 16; ++r) { p0[r] = NEG; p1[r] = NEG; } return; }
    p0 = f32x16{}; p1 = f32x16{};
    const char* kb[4];
#pragma unroll
    for (int dd = 0; dd < 4; ++dd) kb[dd] = K_lds + KB * SHM_K + KSWZ(r32, (dd * 16 + hi * 8) * 2);
#pragma unroll
    for (int d0 = 0; d0 < 8; ++d0) { const char* a = kb[d0 & 3] + (d0 >> 2) * 128;
        bf16x8 b0 = *reinterpret_cast<const bf16x8*>(a);
        bf16x8 b1 = *reinterpret_cast<const bf16x8*>(a + 32 * 256);
        p0 = __builtin_amdgcn_mfma_f32_32x32x16_bf16(b0, qr[d0], p0, 0, 0, 0);
        p1 = __builtin_amdgcn_mfma_f32_32x32x16_bf16(b1, qr[d0], p1, 0, 0, 0); }
}
template <int VB, bool SK>
__device__ __forceinline__ void pv_tile(f32x16* o, int vb0, bf16x8 pa0, bf16x8 pa1, bf16x8 pa2, bf16x8 pa3, bool act) {
    if (SK && !act) return;
#define TRRD(dst, off) asm volatile("ds_read_b64_tr_b16 %0, %1 offset:%2" : "=&v"(dst) : "v"(vb0), "i"(off) : "memory")
#define PV_D0(d0) do { s16x4 l0, l1, l2, l3, h0, h1, h2, h3; constexpr int b_ = VB * SHM_V + v_rd_off(d0, 0, 0);     \
        TRRD(l0, b_); TRRD(h0, b_ + 2048); TRRD(l1, b_ + 4096); TRRD(h1, b_ + 6144); TRRD(l2, b_ + 8192); TRRD(h2, b_ + 10240); TRRD(l3, b_ + 12288); TRRD(h3, b_ + 14336); \
        asm volatile("s_waitcnt lgkmcnt(0)" ::: "memory"); SBAR();                 \
        o[d0] = __builtin_amdgcn_mfma_f32_32x32x16_bf16(pa0, (bf16x8){l0[0], l0[1], l0[2], l0[3], h0[0], h0[1], h0[2], h0[3]}, o[d0], 0, 0, 0);   \
        o[d0] = __builtin_amdgcn_mfma_f32_32x32x16_bf16(pa1, (bf16x8){l1[0], l1[1], l1[2], l1[3], h1[0], h1[1], h1[2], h1[3]}, o[d0], 0, 0, 0);   \
        o[d0] = __builtin_amdgcn_mfma_f32_32x32x16_bf16(pa2, (bf16x8){l2[0], l2[1], l2[2], l2[3], h2[0], h2[1], h2[2], h2[3]}, o[d0], 0, 0, 0);   \
        o[d0] = __builtin_amdgcn_mfma_f32_32x32x16_bf16(pa3, (bf16x8){l3[0], l3[1], l3[2], l3[3], h3[0], h3[1], h3[2], h3[3]}, o[d0], 0, 0, 0); } while (0)
    PV_D0(0); PV_D0(1); PV_D0(2); PV_D0(3);
#undef PV_D0
#undef TRRD
}

template <class TIn, class TOut> struct BlockRef { const TIn* Q; const TIn* K; const TIn* V; TOut* O; int P0; };
template <class TIn> struct Seam {
    bf16x8 qr[8];
    bf16x8 st_v0, st_v1, st_k0, st_k1; f32x4 sf0, sf1, sf2, sf3;
    f32x4 tq[16];
};
__device__ __forceinline__ int swa_jlo(int P0, int W) { const int lowk = P0 - W + 1; return lowk > 0 ? lowk / KVBLK : 0; }
#define ROW(p, k0, rr) ((p) + (size_t)((k0) + (rr)) * D + sc)
#define VMW() asm volatile("s_waitcnt vmcnt(0)" ::: "memory")
#define VMWN(n) asm volatile("s_waitcnt vmcnt(%0)" :: "i"(n) : "memory")
#define SLOAD_H(Kp, Vp, k0) do { S.st_v0 = load8<TIn>(ROW(Vp, k0, sr)); S.st_v1 = load8<TIn>(ROW(Vp, k0, 32 + sr));              \
                         S.st_k0 = load8<TIn>(ROW(Kp, k0, sr)); S.st_k1 = load8<TIn>(ROW(Kp, k0, 32 + sr)); } while (0)
#define SWRITE_HK(bf) do { *(bf16x8*)(K_lds + (bf) * SHM_K + kws) = S.st_k0; *(bf16x8*)(K_lds + (bf) * SHM_K + kws + 32 * 256) = S.st_k1; } while (0)
#define SWRITE_HV(bf) do { *(bf16x8*)(V_lds + (bf) * SHM_V + vst0) = S.st_v0; *(bf16x8*)(V_lds + (bf) * SHM_V + vst1) = S.st_v1; } while (0)
#define SWRITE_H(bf) do { SWRITE_HV(bf); SWRITE_HK(bf); } while (0)
#define SLOAD_F(p, k0) do { S.sf0 = *(const f32x4*)ROW(p, k0, sr); S.sf1 = *(const f32x4*)(ROW(p, k0, sr) + 4);                \
                            S.sf2 = *(const f32x4*)ROW(p, k0, 32 + sr); S.sf3 = *(const f32x4*)(ROW(p, k0, 32 + sr) + 4); } while (0)
#define SWRITE_KF(bf) do { *(bf16x8*)(K_lds + (bf) * SHM_K + kws) = pack8(S.sf0, S.sf1); *(bf16x8*)(K_lds + (bf) * SHM_K + kws + 32 * 256) = pack8(S.sf2, S.sf3); } while (0)
#define SWRITE_VF(bf) do { *(bf16x8*)(V_lds + (bf) * SHM_V + vst0) = pack8(S.sf0, S.sf1); *(bf16x8*)(V_lds + (bf) * SHM_V + vst1) = pack8(S.sf2, S.sf3); } while (0)
template <class TIn, class TOut>
__device__ __forceinline__ void causal_swa_prime(const BlockRef<TIn, TOut>& cur, int W, char* lds, Seam<TIn>& S) {
    constexpr bool F32 = same_t<TIn, float>::v;
    const int tid = threadIdx.x, wid = __builtin_amdgcn_readfirstlane(tid >> 6), lane = tid & 63, r32 = lane & 31, hi = lane >> 5;
    const int sr = tid >> 4, sc = (tid & 15) * 8, kws = KSWZ(sr, sc * 2); char* K_lds = lds + 2 * SHM_V;
    const int kb0 = swa_jlo(cur.P0, W) * KVBLK;
    for (int d0 = 0; d0 < 8; ++d0) S.qr[d0] = load8<TIn>(cur.Q + (size_t)(wid * QBLK + r32) * D + d0 * 16 + hi * 8);
    if constexpr (F32) { SLOAD_F((const float*)cur.K, kb0); VMW(); SWRITE_KF(0); SBAR(); SLOAD_F((const float*)cur.V, kb0); }
    else { SLOAD_H(cur.K, cur.V, kb0); VMW(); SWRITE_HK(0); }
    __syncthreads();
}
template <class TIn, class TOut>
__device__ __forceinline__ void causal_swa_block(const BlockRef<TIn, TOut>& cur, const BlockRef<TIn, TOut>& nxt, int skv, int W, char* lds, Seam<TIn>& S) {
    constexpr bool F32 = same_t<TIn, float>::v;
    const int tid = threadIdx.x, wid = __builtin_amdgcn_readfirstlane(tid >> 6), lane = tid & 63, r32 = lane & 31, hi = lane >> 5;
    const int j_lo = swa_jlo(cur.P0, W);
    int j_hi = (cur.P0 + QB - 1) / KVBLK + 1; if (j_hi > skv / KVBLK) j_hi = skv / KVBLK;
    const int NT = j_hi - j_lo;
    const int kbn = swa_jlo(nxt.P0, W) * KVBLK;
    const int qlo = cur.P0 + wid * QBLK, qm = qlo + r32 - 4 * hi;
    char* V_lds = lds; char* K_lds = lds + 2 * SHM_V;
    float* ws = (float*)(lds + 2 * SHM_V + 2 * SHM_K) + wid * 64; float* li_l = ws, * al_l = ws + 32;
    float m_reg = -1e30f, l_reg = 0; f32x16 o[4] = {};
    const int sr = tid >> 4, sc = (tid & 15) * 8, vst0 = v_st(sr, sc), vst1 = v_st(32 + sr, sc), kws = KSWZ(sr, sc * 2);
    const int vb0 = (int)(uintptr_t)V_lds + v_rd_base(lane);
    const TIn* Kh = cur.K; const TIn* Vh = cur.V;
#define RESC(a) do { if (__any((a) < 1.f)) { if (hi == 0) al_l[r32] = (a); asm volatile("s_waitcnt lgkmcnt(0)" ::: "memory");              \
                     for (int d_ = 0; d_ < 4; ++d_) for (int r = 0; r < 16; ++r) o[d_][r] *= al_l[crow(r, hi)]; } } while (0)
#define KBASE(t) ((j_lo + (t)) * KVBLK)
#define ACT(t) (KBASE(t) <= qlo + QBLK - 1 && KBASE(t) + KVBLK - 1 >= qlo - W + 1)
#define MASKT(P0_, P1_, t) do { const int kb_ = KBASE(t); if ((!SK || ACT(t)) && (kb_ + KVBLK - 1 > qlo || kb_ <= qlo + QBLK - 1 - W)) mask_tile(P0_, P1_, qm - kb_, (unsigned)W); } while (0)
    constexpr int NQL = F32 ? 16 : 8;
    constexpr bool SK = WSKIP && !F32;
#define SEAM_K0() do { VMWN(NQL); if constexpr (F32) { SWRITE_KF(0); SBAR(); SLOAD_F((const float*)nxt.V, kbn); } else { SWRITE_HK(0); } SBAR(); } while (0)
    f32x16 pA0, pA1, pB0, pB1; float mnA, mnB, alA, alB; bf16x8 pa0, pa1, pa2, pa3;
    if constexpr (F32) { VMW(); SWRITE_VF(0); SBAR(); } else { SWRITE_HV(0); SBAR(); }
    if (NT > 1) { if constexpr (F32) SLOAD_F((const float*)Kh, KBASE(1)); else SLOAD_H(Kh, Vh, KBASE(1)); }
    SBAR(); qkt<0, SK>(pA0, pA1, K_lds, r32, hi, S.qr, ACT(0));
    if constexpr (F32) { if (NT > 1) { VMW(); SWRITE_KF(1); SBAR(); SLOAD_F((const float*)Vh, KBASE(1)); } }
    MASKT(pA0, pA1, 0); partialSM(pA0, pA1, m_reg, mnA, alA);
    if (NT > 1) { VMW(); if constexpr (F32) { SWRITE_VF(1); SBAR(); if (NT > 2) SLOAD_F((const float*)Kh, KBASE(2)); } else SWRITE_H(1); }
    __syncthreads();
#define HALF_STEP(PX0, PX1, mnX, alX, PY0, PY1, alY, t, KB, VB, SB) do {                                                      \
        SBAR(); qkt<KB, SK>(PX0, PX1, K_lds, r32, hi, S.qr, ACT(t));                                             \
        finishSM(PY0, PY1, alY, l_reg, pa0, pa1, pa2, pa3); SBAR();                                                           \
        if ((t) + 1 < NT) { if constexpr (F32) { VMW(); SWRITE_KF(SB); SBAR(); SLOAD_F((const float*)Vh, KBASE((t) + 1)); }  \
                            else { SLOAD_H(Kh, Vh, KBASE((t) + 1)); } SBAR(); }                                               \
        pv_tile<VB, SK>(o, vb0, pa0, pa1, pa2, pa3, ACT((t) - 1)); MASKT(PX0, PX1, (t)); partialSM(PX0, PX1, m_reg, mnX, alX);                                        \
        __syncthreads();                                                                                                      \
        if ((t) + 1 < NT) { VMW(); if constexpr (F32) { SWRITE_VF(SB); SBAR(); if ((t) + 2 < NT) SLOAD_F((const float*)Kh, KBASE((t) + 2)); } \
                            else { SWRITE_H(SB); } }                                                                          \
        RESC(alX); __syncthreads(); } while (0)
    for (int t = 1; t + 1 < NT; t += 2) {
        HALF_STEP(pB0, pB1, mnB, alB, pA0, pA1, alA, t, 1, 0, 0);
        HALF_STEP(pA0, pA1, mnA, alA, pB0, pB1, alB, t + 1, 0, 1, 1);
    }
    const bool even = (NT & 1) == 0;
    if (even) { SBAR(); qkt<1, SK>(pB0, pB1, K_lds, r32, hi, S.qr, ACT(NT - 1)); SBAR(); }
#define QROW(e) (nxt.Q + (size_t)(wid * QBLK + r32) * D + ((e) >> 1) * 16 + hi * 8 + ((e) & 1) * 4)
    if constexpr (F32) { SLOAD_F((const float*)nxt.K, kbn); SBAR();
#pragma unroll
        for (int e = 0; e < 8; ++e) S.tq[e] = *(const f32x4*)QROW(e); }
    else { SLOAD_H(nxt.K, nxt.V, kbn); SBAR();
#pragma unroll
        for (int d0 = 0; d0 < 8; ++d0) S.qr[d0] = load8<TIn>(nxt.Q + (size_t)(wid * QBLK + r32) * D + d0 * 16 + hi * 8); }
    SBAR();
    finishSM(pA0, pA1, alA, l_reg, pa0, pa1, pa2, pa3); SBAR();
    if constexpr (F32) {
#pragma unroll
        for (int e = 8; e < 16; ++e) S.tq[e] = *(const f32x4*)QROW(e); SBAR(); }
#undef QROW
    pv_tile<0, SK>(o, vb0, pa0, pa1, pa2, pa3, ACT(even ? NT - 2 : NT - 1));
    if (even) { MASKT(pB0, pB1, NT - 1); partialSM(pB0, pB1, m_reg, mnB, alB); __syncthreads(); RESC(alB);
        finishSM(pB0, pB1, alB, l_reg, pa0, pa1, pa2, pa3); SBAR(); pv_tile<1, SK>(o, vb0, pa0, pa1, pa2, pa3, ACT(NT - 1)); }
    SBAR(); SEAM_K0();
    if (hi == 0) li_l[r32] = l_reg; asm volatile("s_waitcnt lgkmcnt(0)" ::: "memory");
    float rli[16];
#pragma unroll
    for (int r = 0; r < 16; ++r) rli[r] = __builtin_amdgcn_rcpf(li_l[crow(r, hi)]);
    TOut* Ow = cur.O + (size_t)(wid * QBLK) * D;
#pragma unroll
    for (int r = 0; r < 16; ++r) { const int orow = crow(r, hi);
#pragma unroll
        for (int d0 = 0; d0 < 4; ++d0) { const float v = o[d0][r] * rli[r];
            if constexpr (same_t<TOut, float>::v) { Ow[(size_t)orow * D + d0 * 32 + r32] = v; }
            else { const float vn = __shfl_xor(v, 1);
                   if ((r32 & 1) == 0) *(unsigned*)(Ow + (size_t)orow * D + d0 * 32 + r32) = cvtpk(v, vn); } } }
    if constexpr (F32) {
#pragma unroll
        for (int d0 = 0; d0 < 8; ++d0) S.qr[d0] = pack8(S.tq[2 * d0], S.tq[2 * d0 + 1]); }
    __syncthreads();
#undef RESC
#undef KBASE
#undef ACT
#undef MASKT
#undef SEAM_K0
#undef HALF_STEP
}
#undef ROW
#undef VMW
#undef VMWN
#undef SLOAD_H
#undef SWRITE_HK
#undef SWRITE_HV
#undef SWRITE_H
#undef SLOAD_F
#undef SWRITE_KF
#undef SWRITE_VF

__host__ __device__ inline int swa_nramp(int nqb, int W, int qoff) { const int t = W - 1 - qoff; const int n = t < 0 ? 0 : t / QB + 1; return n > nqb ? nqb : n; }
__host__ __device__ inline int swa_nx(int nqb, int nramp, int order) { return (order & ORDER_PAIRED) ? (nramp + 1) / 2 + (nqb - nramp) : nqb; }
struct SwaItem { int bh, qb0, qb1; };
__device__ __forceinline__ SwaItem swa_decode(int L, int nb, int nh, int nhkv, int nqb, int nx, int nramp, int order) {
    const int G = nh / nhkv; SwaItem it; int x;
    if ((order & ORDER_XCD) && (nb * nhkv) % 8 == 0) { const int xcd = L & 7, k = L >> 3, per = G * nx, gi = k / per, r = k - gi * per;
        it.bh = (gi * 8 + xcd) * G + r / nx; x = r % nx; }
    else { it.bh = L / nx; x = L - it.bh * nx; }
    if (order & ORDER_PAIRED) { const int ns = nqb - nramp;
        if (x < ns) { it.qb0 = it.qb1 = nqb - 1 - x; } else { it.qb0 = x - ns; it.qb1 = nramp - 1 - it.qb0; } }
    else { it.qb0 = it.qb1 = ((order & 3) == ORDER_REVERSED) ? nqb - 1 - x : x; }
    return it;
}
template <class TIn, class TOut>
__device__ __forceinline__ BlockRef<TIn, TOut> swa_ref(const SwaItem& it, int pass, const TIn* Q, const TIn* K, const TIn* V, TOut* O,
                                                    int nh, int nhkv, int sq, int skv, int qoff) {
    const int qb = pass ? it.qb1 : it.qb0, kvh = it.bh / (nh / nhkv);
    BlockRef<TIn, TOut> r;
    r.Q = Q + ((size_t)it.bh * sq + (size_t)qb * QB) * D; r.O = O + ((size_t)it.bh * sq + (size_t)qb * QB) * D;
    r.K = K + (size_t)kvh * skv * D; r.V = V + (size_t)kvh * skv * D; r.P0 = qoff + qb * QB;
    return r;
}
template <class TIn, class TOut>
__global__ __launch_bounds__(64 * NW, 1)
void attn_fwd_causal_swa(const TIn* __restrict__ Q, const TIn* __restrict__ K, const TIn* __restrict__ V, TOut* __restrict__ O,
                         const int* __restrict__ Wdev, int nb, int nh, int nhkv, int sq, int skv, int W, int qoff, int order, int nramp) {
    extern __shared__ __attribute__((aligned(16))) char lds[];
    if (Wdev) W = *Wdev;
    const int nqb = sq / QB, nx = swa_nx(nqb, nramp, order), total = nx * nb * nh, stride = gridDim.x;
    int L = blockIdx.x; if (L >= total) return;
    SwaItem it = swa_decode(L, nb, nh, nhkv, nqb, nx, nramp, order); int pass = 0;
    BlockRef<TIn, TOut> cur = swa_ref<TIn, TOut>(it, 0, Q, K, V, O, nh, nhkv, sq, skv, qoff);
    Seam<TIn> S;
    causal_swa_prime<TIn, TOut>(cur, W, lds, S);
    for (;;) {
        const bool more_pass = pass == 0 && it.qb1 != it.qb0, more_item = L + stride < total, last = !more_pass && !more_item;
        SwaItem itn = it; int passn = pass + 1, Ln = L;
        if (!more_pass) { passn = 0; Ln = more_item ? L + stride : L; itn = swa_decode(Ln, nb, nh, nhkv, nqb, nx, nramp, order); }
        const BlockRef<TIn, TOut> nxt = last ? cur : swa_ref<TIn, TOut>(itn, passn, Q, K, V, O, nh, nhkv, sq, skv, qoff);
        causal_swa_block<TIn, TOut>(cur, nxt, skv, W, lds, S);
        if (last) break;
        cur = nxt; it = itn; pass = passn; L = Ln;
    }
}
template <class TIn, class TOut>
static void causal_swa_launch(const TIn* q, const TIn* k, const TIn* v, TOut* o, const int* Wdev  ,
                              int nb, int nh, int nhkv, int sq, int skv, int W  ,
                              int qoff, int order, hipStream_t stream) {
    bool attr_done = false; int grid_wgs = 256;
    if (!attr_done) { attr_done = true;
        hipError_t e = hipFuncSetAttribute((const void*)attn_fwd_causal_swa<TIn, TOut>, hipFuncAttributeMaxDynamicSharedMemorySize, LDS_BYTES);
        if (e != hipSuccess) fprintf(stderr, "attn_fwd_causal_swa: hipFuncSetAttribute(%d B LDS): %s\n", LDS_BYTES, hipGetErrorString(e));
        if (grid_wgs <= 0) { int dev = 0, cus = 0; (void)hipGetDevice(&dev);
            if (hipDeviceGetAttribute(&cus, hipDeviceAttributeMultiprocessorCount, dev) != hipSuccess || cus <= 0) cus = 256; grid_wgs = cus; } }
    const int nqb = sq / QB, nramp = swa_nramp(nqb, W, qoff), nx = swa_nx(nqb, nramp, order), total = nx * nb * nh;
    const int grid = total < grid_wgs ? total : grid_wgs;
    attn_fwd_causal_swa<TIn, TOut><<<dim3(grid), 64 * NW, LDS_BYTES, stream>>>(q, k, v, o, Wdev, nb, nh, nhkv, sq, skv, W, qoff, order, nramp);
}

extern "C" void kernel_launch(void* const* d_in, const int* in_sizes, int n_in, void* d_out, int out_size, void* d_ws,
                              size_t ws_size, hipStream_t stream) {
    (void)d_ws; (void)ws_size;
    constexpr long NQ = (long)B * H * SQ * D, NKV = (long)B * HKV * SKV * D;
    if ((n_in != 3 && n_in != 4) || in_sizes[0] != NQ || in_sizes[1] != NKV || in_sizes[2] != NKV || (n_in == 4 && in_sizes[3] != 1) || out_size != NQ) {
        fprintf(stderr, "attn_fwd_causal_swa: shape mismatch (n_in=%d in_sizes=%d,%d,%d,%d out=%d; want 3 or 4 / %ld,%ld,%ld[,1] / %ld)\n",
                n_in, n_in > 0 ? in_sizes[0] : -1, n_in > 1 ? in_sizes[1] : -1, n_in > 2 ? in_sizes[2] : -1, n_in > 3 ? in_sizes[3] : -1, out_size, NQ, NKV, NKV, NQ);
        return;
    }
    const int* Wdev = n_in == 4 ? (const int*)d_in[3] : nullptr;
    causal_swa_launch<TIN, TOUT>((const TIN*)d_in[0], (const TIN*)d_in[1], (const TIN*)d_in[2], (TOUT*)d_out, Wdev, B, H, HKV, SQ, SKV, WINDOW, QOFF, ORDER, stream);
}
```

```cpp
#include <hip/hip_runtime.h>
#include <stdint.h>
extern "C" __global__ __launch_bounds__(256, 1) void attn_causal_kernel(const char* q, const char* k, const char* v, char* o) {
  __shared__ __attribute__((aligned(16))) char lds[163840];
  const unsigned w = __builtin_amdgcn_readfirstlane(threadIdx.x >> 6);
  asm volatile(
    "L_begin:\n"
    "s_load_dwordx8 s[64:71], %0, 0x0\n"
    "s_and_b32 s38, %1, 3\n"
    "s_bfe_u32 s39, %1, 0x30005\n"
    "s_mov_b32 s22, %1\n"
    "v_mbcnt_lo_u32_b32 v232, -1, 0\n"
    "v_mbcnt_hi_u32_b32 v232, -1, v232\n"
    "s_getpc_b64 s[72:73]\n"
    "L_pc:\n"
    "s_and_b32 s73, s73, 0xffff\n"
    "s_mov_b32 s74, L_hot_end-L_pc\n"
    "s_mov_b32 s75, 0x20000\n"
    "v_lshlrev_b32 v233, 7, v232\n"
    "s_and_b32 s62, s22, 0x303\n"
    "s_lshl_b32 s90, s39, 13\n"
    "s_cmp_eq_u32 s62, 3\n"
    "s_cselect_b32 s62, s90, 0x7fff0000\n"
    "buffer_load_dword a244, v233, s[72:75], s62 offen\n"
    "s_bfe_u32 s92, s22, 0x30002\n"
    "s_lshl_b32 s92, s92, 2\n"
    "s_lshr_b32 s62, s22, 8\n"
    "s_add_u32 s92, s92, s62\n"
    "s_lshl_b32 s92, s92, 19\n"
    "s_lshl_b32 s88, s39, 15\n"
    "s_sub_u32 s42, 524288, s88\n"
    "s_mov_b32 s50, 524288\n"
    "s_mov_b32 s43, 0x20000\n"
    "s_mov_b32 s51, 0x20000\n"
    "v_bfe_u32 v234, v232, 2, 2\n"
    "v_lshlrev_b32 v233, 8, v234\n"
    "v_lshrrev_b32 v235, 4, v232\n"
    "v_lshl_add_u32 v233, v235, 6, v233\n"
    "v_and_b32 v253, 3, v232\n"
    "v_lshl_add_u32 v251, v253, 4, v233\n"
    "v_xor_b32 v252, 32, v251\n"
    "s_lshl_b32 s93, s38, 11\n"
    "s_lshl_b32 s58, s38, 12\n"
    "s_lshl_b32 s89, s38, 13\n"
    "s_sub_u32 s98, 491520, s88\n"
    "s_add_u32 s98, s98, s58\n"
    "s_add_u32 s99, s88, s58\n"
    "s_sub_u32 s23, 28672, s89\n"
    "s_add_u32 s91, s98, s23\n"
    "s_add_u32 s56, s58, 0x40000\n"
    "s_waitcnt lgkmcnt(0)\n"
    "s_add_u32 s48, s64, s92\n"
    "s_addc_u32 s49, s65, 0\n"
    "s_and_b32 s49, s49, 0xffff\n"
    "s_add_u32 s40, s66, s92\n"
    "s_addc_u32 s41, s67, 0\n"
    "s_and_b32 s41, s41, 0xffff\n"
    "s_add_u32 m0, s58, 0\n"
    "s_nop 0\n"
    "buffer_load_dwordx4 v251, s[40:43], s56 offen offset:0 lds\n"
    "buffer_load_dwordx4 v252, s[40:43], s56 offen offset:1024 lds\n"
    "buffer_load_dwordx4 v251, s[40:43], s56 offen offset:2048 lds\n"
    "buffer_load_dwordx4 v252, s[40:43], s56 offen offset:3072 lds\n"
    "s_add_u32 m0, s89, 135168\n"
    "s_nop 0\n"
    "buffer_load_dwordx4 v251, s[48:51], s91 offen offset:0 lds\n"
    "buffer_load_dwordx4 v252, s[48:51], s91 offen offset:1024 lds\n"
    "buffer_load_dwordx4 v251, s[48:51], s91 offen offset:2048 lds\n"
    "buffer_load_dwordx4 v252, s[48:51], s91 offen offset:3072 lds\n"
    "s_add_u32 m0, s89, 131072\n"
    "s_nop 0\n"
    "buffer_load_dwordx4 v251, s[48:51], s98 offen offset:0 lds\n"
    "buffer_load_dwordx4 v252, s[48:51], s98 offen offset:1024 lds\n"
    "buffer_load_dwordx4 v251, s[48:51], s98 offen offset:2048 lds\n"
    "buffer_load_dwordx4 v252, s[48:51], s98 offen offset:3072 lds\n"
    "s_mov_b32 s46, s42\n"
    "s_mov_b32 s54, 524288\n"
    "s_mov_b32 s47, 0x20000\n"
    "s_mov_b32 s55, 0x20000\n"
    "s_add_u32 s44, s68, s92\n"
    "s_addc_u32 s45, s69, 0\n"
    "s_and_b32 s45, s45, 0xffff\n"
    "s_add_u32 s52, s70, s92\n"
    "s_addc_u32 s53, s71, 0\n"
    "s_and_b32 s53, s53, 0xffff\n"
    "v_mov_b32 v224, 0\n"
    "v_mov_b32 v225, 0\n"
    "v_mov_b32 v226, 0\n"
    "v_mov_b32 v227, 0\n"
    "v_mov_b32 v228, 0\n"
    "v_mov_b32 v229, 0\n"
    "v_mov_b32 v230, 0\n"
    "v_mov_b32 v231, 0\n"
    "s_nop 1\n"
    "v_mfma_f32_32x32x16_bf16 v[0:15], v[224:227], v[224:227], 0\n"
    "v_mfma_f32_32x32x16_bf16 v[16:31], v[224:227], v[224:227], 0\n"
    "v_mfma_f32_32x32x16_bf16 v[32:47], v[224:227], v[224:227], 0\n"
    "v_mfma_f32_32x32x16_bf16 v[48:63], v[224:227], v[224:227], 0\n"
    "v_mfma_f32_32x32x16_bf16 v[64:79], v[224:227], v[224:227], 0\n"
    "v_mfma_f32_32x32x16_bf16 v[80:95], v[224:227], v[224:227], 0\n"
    "v_mfma_f32_32x32x16_bf16 v[96:111], v[224:227], v[224:227], 0\n"
    "v_mfma_f32_32x32x16_bf16 v[112:127], v[224:227], v[224:227], 0\n"
    "v_mfma_f32_32x32x16_bf16 a[176:191], v[224:227], v[224:227], 0\n"
    "v_mfma_f32_32x32x16_bf16 a[192:207], v[224:227], v[224:227], 0\n"
    "v_mfma_f32_32x32x16_bf16 a[208:223], v[224:227], v[224:227], 0\n"
    "v_mfma_f32_32x32x16_bf16 a[224:239], v[224:227], v[224:227], 0\n"
    "s_mov_b32 s36, 0x3e0293ee\n"
    "s_mov_b32 s37, 0x4e800000\n"
    "s_mov_b32 s35, 0xff800000\n"
    "v_mov_b32 v254, s35\n"
    "s_mov_b32 s64, 0x000f0000\n"
    "s_mov_b32 s65, 0x0fff00ff\n"
    "s_mov_b32 s66, 0x001f0001\n"
    "s_mov_b32 s67, 0x1fff01ff\n"
    "s_mov_b32 s68, 0x003f0003\n"
    "s_mov_b32 s69, 0x3fff03ff\n"
    "s_mov_b32 s70, 0x007f0007\n"
    "s_mov_b32 s71, 0x7fff07ff\n"
    "v_and_b32 v233, 1, v234\n"
    "v_lshlrev_b32 v233, 1, v233\n"
    "v_xor_b32 v233, v233, v235\n"
    "v_lshlrev_b32 v248, 10, v234\n"
    "v_lshl_add_u32 v248, v253, 6, v248\n"
    "v_lshl_add_u32 v248, v233, 4, v248\n"
    "v_lshlrev_b32 v249, 10, v235\n"
    "v_lshl_add_u32 v249, v234, 6, v249\n"
    "v_lshl_add_u32 v249, v253, 3, v249\n"
    "v_and_b32 v233, 1, v235\n"
    "v_lshl_add_u32 v249, v233, 5, v249\n"
    "v_add_u32 v249, 65536, v249\n"
    "s_lshr_b32 s92, s38, 1\n"
    "s_lshl_b32 s63, s92, 13\n"
    "s_lshl_b32 s92, s92, 1\n"
    "s_xor_b32 s96, s38, 2\n"
    "s_sub_i32 s19, 3, s38\n"
    "s_sub_i32 s19, s19, s92\n"
    "s_sub_i32 s97, 3, s96\n"
    "s_sub_i32 s97, s97, s92\n"
    "s_sub_i32 s95, s38, s92\n"
    "s_sub_i32 s96, s96, s92\n"
    "s_sub_i32 s20, 6, s39\n"
    "s_max_i32 s20, s20, 0\n"
    "s_min_i32 s20, s20, 6\n"
    "s_add_u32 s16, s20, 1\n"
    "s_lshl_b32 s16, s16, 15\n"
    "s_add_u32 s17, s16, 0x40000\n"
    "s_sub_u32 s18, 0, s16\n"
    "s_cmp_ge_u32 s20, 2\n"
    "s_cselect_b32 s15, 2, -1\n"
    "v_lshlrev_b32 v237, 4, v232\n"
    "v_accvgpr_write_b32 a246, v237\n"
    "v_and_b32 v237, 15, v232\n"
    "v_lshrrev_b32 v238, 5, v232\n"
    "v_and_b32 v239, 1, v235\n"
    "v_lshlrev_b32 v233, 8, v237\n"
    "v_lshl_add_u32 v233, v239, 5, v233\n"
    "v_lshl_add_u32 v233, v238, 4, v233\n"
    "v_accvgpr_write_b32 a245, v233\n"
    "s_add_u32 s90, s89, 65536\n"
    "v_xor_b32 v238, v238, v237\n"
    "v_lshlrev_b32 v238, 4, v238\n"
    "v_lshl_add_u32 v238, v237, 8, v238\n"
    "v_lshl_add_u32 v238, v239, 3, v238\n"
    "v_add_u32 v238, s90, v238\n"
    "v_accvgpr_write_b32 a247, v238\n"
    "v_xor_b32 v237, v237, v235\n"
    "v_lshlrev_b32 v237, 4, v237\n"
    "v_lshl_add_u32 v237, v235, 8, v237\n"
    "v_add_u32 v237, s90, v237\n"
    "v_accvgpr_write_b32 a248, v237\n"
    "v_mov_b32 v233, 0x3f803f80\n"
    "v_cmp_eq_u32 vcc, 0, v253\n"
    "s_nop 1\n"
    "v_cndmask_b32 v234, 0, v233, vcc\n"
    "v_accvgpr_write_b32 a160, v234\n"
    "v_accvgpr_write_b32 a161, v234\n"
    "v_accvgpr_write_b32 a162, v234\n"
    "v_accvgpr_write_b32 a163, v234\n"
    "v_cmp_eq_u32 vcc, 1, v253\n"
    "s_nop 1\n"
    "v_cndmask_b32 v234, 0, v233, vcc\n"
    "v_accvgpr_write_b32 a164, v234\n"
    "v_accvgpr_write_b32 a165, v234\n"
    "v_accvgpr_write_b32 a166, v234\n"
    "v_accvgpr_write_b32 a167, v234\n"
    "v_cmp_eq_u32 vcc, 2, v253\n"
    "s_nop 1\n"
    "v_cndmask_b32 v234, 0, v233, vcc\n"
    "v_accvgpr_write_b32 a168, v234\n"
    "v_accvgpr_write_b32 a169, v234\n"
    "v_accvgpr_write_b32 a170, v234\n"
    "v_accvgpr_write_b32 a171, v234\n"
    "v_cmp_eq_u32 vcc, 3, v253\n"
    "s_nop 1\n"
    "v_cndmask_b32 v234, 0, v233, vcc\n"
    "v_accvgpr_write_b32 a172, v234\n"
    "v_accvgpr_write_b32 a173, v234\n"
    "v_accvgpr_write_b32 a174, v234\n"
    "v_accvgpr_write_b32 a175, v234\n"
    "s_add_u32 s90, s89, 131072\n"
    "v_add_u32 v253, s90, v248\n"
    "v_xor_b32 v234, 16384, v253\n"
    "v_lshlrev_b32 v235, 4, v232\n"
    "v_add_u32 v235, s90, v235\n"
    "v_add_u32 v246, s63, v248\n"
    "s_mov_b32 s61, s63\n"
    "v_add_u32 v247, s63, v249\n"
    "v_xor_b32 v250, 32, v247\n"
    "v_mov_b32 v236, s36\n"
    "s_mov_b32 s57, s56\n"
    "s_add_u32 s56, s56, 16384\n"
    "s_add_u32 m0, s58, 16384\n"
    "s_nop 0\n"
    "buffer_load_dwordx4 v251, s[40:43], s56 offen offset:0 lds\n"
    "buffer_load_dwordx4 v252, s[40:43], s56 offen offset:1024 lds\n"
    "buffer_load_dwordx4 v251, s[40:43], s56 offen offset:2048 lds\n"
    "buffer_load_dwordx4 v252, s[40:43], s56 offen offset:3072 lds\n"
    "s_add_u32 m0, s58, 65536\n"
    "s_nop 0\n"
    "buffer_load_dwordx4 v251, s[44:47], s57 offen offset:0 lds\n"
    "buffer_load_dwordx4 v252, s[44:47], s57 offen offset:1024 lds\n"
    "buffer_load_dwordx4 v251, s[44:47], s57 offen offset:2048 lds\n"
    "buffer_load_dwordx4 v252, s[44:47], s57 offen offset:3072 lds\n"
    "s_add_u32 s59, s58, 16384\n"
    "s_add_u32 s58, s58, 32768\n"
    "s_cmp_lt_u32 s58, s17\n"
    "s_cselect_b32 s56, s18, 0\n"
    "s_cmp_lt_u32 s58, s16\n"
    "s_cselect_b32 s56, 0x40000, s56\n"
    "s_add_u32 s56, s56, s58\n"
    "s_cmp_lt_u32 s59, s17\n"
    "s_cselect_b32 s57, s18, 0\n"
    "s_cmp_lt_u32 s59, s16\n"
    "s_cselect_b32 s57, 0x40000, s57\n"
    "s_add_u32 s57, s57, s59\n"
    "s_waitcnt vmcnt(12) lgkmcnt(0)\n"
    "s_barrier\n"
    "ds_read_b128 a[96:99], v246 offset:0\n"
    "ds_read_b128 a[100:103], v246 offset:256\n"
    "ds_read_b128 a[104:107], v246 offset:512\n"
    "ds_read_b128 a[108:111], v246 offset:768\n"
    "ds_read_b128 a[16:19], v253 offset:4096\n"
    "ds_read_b128 a[20:23], v253 offset:4352\n"
    "ds_read_b128 a[24:27], v253 offset:4608\n"
    "ds_read_b128 a[28:31], v253 offset:4864\n"
    "ds_read_b128 a[112:115], v246 offset:4096\n"
    "ds_read_b128 a[116:119], v246 offset:4352\n"
    "ds_read_b128 a[120:123], v246 offset:4608\n"
    "ds_read_b128 a[124:127], v246 offset:4864\n"
    "ds_read_b128 a[80:83], v234 offset:4096\n"
    "ds_read_b128 a[84:87], v234 offset:4352\n"
    "ds_read_b128 a[88:91], v234 offset:4608\n"
    "ds_read_b128 a[92:95], v234 offset:4864\n"
    "s_waitcnt lgkmcnt(11)\n"
    "v_mfma_f32_16x16x32_bf16 v[136:139], a[96:99], a[16:19], 0\n"
    "s_waitcnt lgkmcnt(10)\n"
    "v_mfma_f32_16x16x32_bf16 v[136:139], a[100:103], a[20:23], v[136:139]\n"
    "s_waitcnt lgkmcnt(9)\n"
    "v_mfma_f32_16x16x32_bf16 v[136:139], a[104:107], a[24:27], v[136:139]\n"
    "s_waitcnt lgkmcnt(8)\n"
    "v_mfma_f32_16x16x32_bf16 v[136:139], a[108:111], a[28:31], v[136:139]\n"
    "s_waitcnt lgkmcnt(7)\n"
    "v_mfma_f32_16x16x32_bf16 v[140:143], a[112:115], a[16:19], 0\n"
    "s_waitcnt lgkmcnt(6)\n"
    "v_mfma_f32_16x16x32_bf16 v[140:143], a[116:119], a[20:23], v[140:143]\n"
    "s_waitcnt lgkmcnt(5)\n"
    "v_mfma_f32_16x16x32_bf16 v[140:143], a[120:123], a[24:27], v[140:143]\n"
    "s_waitcnt lgkmcnt(4)\n"
    "v_mfma_f32_16x16x32_bf16 v[140:143], a[124:127], a[28:31], v[140:143]\n"
    "s_waitcnt vmcnt(8) lgkmcnt(0)\n"
    "s_barrier\n"
    "ds_read_b128 a[0:3], v253 offset:0\n"
    "ds_read_b128 a[4:7], v253 offset:256\n"
    "ds_read_b128 a[8:11], v253 offset:512\n"
    "ds_read_b128 a[12:15], v253 offset:768\n"
    "ds_read_b128 a[64:67], v234 offset:0\n"
    "ds_read_b128 a[68:71], v234 offset:256\n"
    "ds_read_b128 a[72:75], v234 offset:512\n"
    "ds_read_b128 a[76:79], v234 offset:768\n"
    "v_mfma_f32_16x16x32_bf16 v[152:155], a[96:99], a[80:83], 0\n"
    "v_mfma_f32_16x16x32_bf16 v[152:155], a[100:103], a[84:87], v[152:155]\n"
    "s_and_b32 m0, s58, 0xffff\n"
    "v_mfma_f32_16x16x32_bf16 v[152:155], a[104:107], a[88:91], v[152:155]\n"
    "buffer_load_dwordx4 v251, s[40:43], s56 offen offset:0 lds\n"
    "v_mfma_f32_16x16x32_bf16 v[152:155], a[108:111], a[92:95], v[152:155]\n"
    "v_mfma_f32_16x16x32_bf16 v[156:159], a[112:115], a[80:83], 0\n"
    "buffer_load_dwordx4 v252, s[40:43], s56 offen offset:1024 lds\n"
    "v_mfma_f32_16x16x32_bf16 v[156:159], a[116:119], a[84:87], v[156:159]\n"
    "v_mfma_f32_16x16x32_bf16 v[156:159], a[120:123], a[88:91], v[156:159]\n"
    "buffer_load_dwordx4 v251, s[40:43], s56 offen offset:2048 lds\n"
    "v_mfma_f32_16x16x32_bf16 v[156:159], a[124:127], a[92:95], v[156:159]\n"
    "s_waitcnt lgkmcnt(7)\n"
    "v_mfma_f32_16x16x32_bf16 v[128:131], a[96:99], a[0:3], 0\n"
    "s_waitcnt lgkmcnt(6)\n"
    "buffer_load_dwordx4 v252, s[40:43], s56 offen offset:3072 lds\n"
    "v_mfma_f32_16x16x32_bf16 v[128:131], a[100:103], a[4:7], v[128:131]\n"
    "s_waitcnt lgkmcnt(5)\n"
    "s_add_u32 s58, s58, 16384\n"
    "s_cmp_lt_u32 s58, s17\n"
    "s_cselect_b32 s56, s18, 0\n"
    "s_cmp_lt_u32 s58, s16\n"
    "s_cselect_b32 s56, 0x40000, s56\n"
    "s_add_u32 s56, s56, s58\n"
    "s_and_b32 m0, s59, 0xffff\n"
    "v_mfma_f32_16x16x32_bf16 v[128:131], a[104:107], a[8:11], v[128:131]\n"
    "s_waitcnt lgkmcnt(0)\n"
    "s_bitset1_b32 m0, 16\n"
    "v_mfma_f32_16x16x32_bf16 v[128:131], a[108:111], a[12:15], v[128:131]\n"
    "buffer_load_dwordx4 v251, s[44:47], s57 offen offset:0 lds\n"
    "v_mfma_f32_16x16x32_bf16 v[132:135], a[112:115], a[0:3], 0\n"
    "v_mfma_f32_16x16x32_bf16 v[132:135], a[116:119], a[4:7], v[132:135]\n"
    "buffer_load_dwordx4 v252, s[44:47], s57 offen offset:1024 lds\n"
    "v_mfma_f32_16x16x32_bf16 v[132:135], a[120:123], a[8:11], v[132:135]\n"
    "v_mfma_f32_16x16x32_bf16 v[132:135], a[124:127], a[12:15], v[132:135]\n"
    "buffer_load_dwordx4 v251, s[44:47], s57 offen offset:2048 lds\n"
    "v_mfma_f32_16x16x32_bf16 v[144:147], a[96:99], a[64:67], 0\n"
    "v_mfma_f32_16x16x32_bf16 v[144:147], a[100:103], a[68:71], v[144:147]\n"
    "buffer_load_dwordx4 v252, s[44:47], s57 offen offset:3072 lds\n"
    "v_mfma_f32_16x16x32_bf16 v[144:147], a[104:107], a[72:75], v[144:147]\n"
    "s_add_u32 s59, s59, 16384\n"
    "s_cmp_lt_u32 s59, s17\n"
    "s_cselect_b32 s57, s18, 0\n"
    "s_cmp_lt_u32 s59, s16\n"
    "s_cselect_b32 s57, 0x40000, s57\n"
    "s_add_u32 s57, s57, s59\n"
    "v_mfma_f32_16x16x32_bf16 v[144:147], a[108:111], a[76:79], v[144:147]\n"
    "v_mfma_f32_16x16x32_bf16 v[148:151], a[112:115], a[64:67], 0\n"
    "v_mfma_f32_16x16x32_bf16 v[148:151], a[116:119], a[68:71], v[148:151]\n"
    "v_mfma_f32_16x16x32_bf16 v[148:151], a[120:123], a[72:75], v[148:151]\n"
    "v_mfma_f32_16x16x32_bf16 v[148:151], a[124:127], a[76:79], v[148:151]\n"
    "s_sub_u32 s21, 32768, s63\n"
    "s_mov_b32 s62, s20\n"
    "s_mov_b32 s14, 0\n"
    "s_mov_b32 s22, -1\n"
    "s_waitcnt lgkmcnt(0)\n"
    "s_waitcnt vmcnt(8)\n"
    "s_barrier\n"
    "ds_read_b128 a[96:99], v246 offset:16384\n"
    "ds_read_b128 a[100:103], v246 offset:16640\n"
    "ds_read_b128 a[104:107], v246 offset:16896\n"
    "ds_read_b128 a[108:111], v246 offset:17152\n"
    "ds_read_b128 a[112:115], v246 offset:20480\n"
    "ds_read_b128 a[116:119], v246 offset:20736\n"
    "ds_read_b128 a[120:123], v246 offset:20992\n"
    "ds_read_b128 a[124:127], v246 offset:21248\n"
    "s_cmp_eq_u32 s39, 7\n"
    "s_cbranch_scc1 L_mo_pro\n"
    "L_ms_pro:\n"
    "s_cmp_lt_u32 s20, 2\n"
    "s_cbranch_scc1 L_qs0\n"
    "L_noqs:\n"
    "v_max3_f32 v192, v136, v137, v138\n"
    "v_max3_f32 v193, v128, v129, v130\n"
    "v_max3_f32 v194, v144, v145, v146\n"
    "v_max3_f32 v195, v152, v153, v154\n"
    "v_max3_f32 v192, v192, v139, v140\n"
    "v_max3_f32 v193, v193, v131, v132\n"
    "v_max3_f32 v194, v194, v147, v148\n"
    "v_max3_f32 v195, v195, v155, v156\n"
    "v_max3_f32 v192, v192, v141, v142\n"
    "v_max3_f32 v193, v193, v133, v134\n"
    "v_max3_f32 v194, v194, v149, v150\n"
    "v_max3_f32 v195, v195, v157, v158\n"
    "v_max_f32 v192, v192, v143\n"
    "v_max_f32 v193, v193, v135\n"
    "v_max_f32 v194, v194, v151\n"
    "v_max_f32 v195, v195, v159\n"
    "v_mov_b32 v200, v192\n"
    "v_mov_b32 v201, v193\n"
    "v_mov_b32 v202, v194\n"
    "v_mov_b32 v203, v195\n"
    "s_waitcnt lgkmcnt(0)\n"
    "v_mfma_f32_16x16x32_bf16 v[168:171], a[96:99], a[16:19], 0\n"
    "ds_read_b64_tr_b16 a[128:129], v247 offset:0\n"
    "v_permlane16_swap_b32 v192, v200\n"
    "v_permlane16_swap_b32 v193, v201\n"
    "v_permlane16_swap_b32 v194, v202\n"
    "v_permlane16_swap_b32 v195, v203\n"
    "v_max_f32 v192, v192, v200\n"
    "v_max_f32 v193, v193, v201\n"
    "v_max_f32 v194, v194, v202\n"
    "v_mfma_f32_16x16x32_bf16 v[168:171], a[100:103], a[20:23], v[168:171]\n"
    "s_and_b32 m0, s58, 0xffff\n"
    "ds_read_b64_tr_b16 a[130:131], v247 offset:4096\n"
    "ds_read_b64_tr_b16 a[132:133], v250 offset:0\n"
    "ds_read_b64_tr_b16 a[134:135], v250 offset:4096\n"
    "v_max_f32 v195, v195, v203\n"
    "v_mov_b32 v200, v192\n"
    "v_mov_b32 v201, v193\n"
    "v_mov_b32 v202, v194\n"
    "v_mfma_f32_16x16x32_bf16 v[168:171], a[104:107], a[24:27], v[168:171]\n"
    "s_cmp_eq_u32 s39, 7\n"
    "buffer_load_dwordx4 v251, s[40:43], s56 offen offset:0 lds\n"
    "ds_read_b64_tr_b16 a[136:137], v247 offset:256\n"
    "ds_read_b64_tr_b16 a[138:139], v247 offset:4352\n"
    "v_mov_b32 v203, v195\n"
    "v_mfma_f32_16x16x32_bf16 v[168:171], a[108:111], a[28:31], v[168:171]\n"
    "s_cselect_b32 s92, s19, 64\n"
    "ds_read_b64_tr_b16 a[140:141], v250 offset:256\n"
    "v_permlane32_swap_b32 v192, v200\n"
    "v_permlane32_swap_b32 v193, v201\n"
    "v_permlane32_swap_b32 v194, v202\n"
    "v_permlane32_swap_b32 v195, v203\n"
    "v_max_f32 v192, v192, v200\n"
    "v_max_f32 v193, v193, v201\n"
    "v_mfma_f32_16x16x32_bf16 v[172:175], a[112:115], a[16:19], 0\n"
    "s_cmp_lt_i32 s92, 0\n"
    "buffer_load_dwordx4 v252, s[40:43], s56 offen offset:1024 lds\n"
    "v_max_f32 v194, v194, v202\n"
    "v_max_f32 v195, v195, v203\n"
    "v_mul_f32 v192, s36, v192\n"
    "v_mfma_f32_16x16x32_bf16 v[172:175], a[116:119], a[20:23], v[172:175]\n"
    "s_cselect_b64 s[84:85], -1, 0\n"
    "ds_read_b64_tr_b16 a[142:143], v250 offset:4352\n"
    "ds_read_b64_tr_b16 a[144:145], v247 offset:512\n"
    "v_mul_f32 v193, s36, v193\n"
    "v_mul_f32 v194, s36, v194\n"
    "v_mul_f32 v195, s36, v195\n"
    "v_max_f32 v192, 0xf149f2ca, v192\n"
    "v_max_f32 v193, 0xf149f2ca, v193\n"
    "v_mfma_f32_16x16x32_bf16 v[172:175], a[120:123], a[24:27], v[172:175]\n"
    "buffer_load_dwordx4 v251, s[40:43], s56 offen offset:2048 lds\n"
    "v_max_f32 v194, 0xf149f2ca, v194\n"
    "v_max_f32 v195, 0xf149f2ca, v195\n"
    "v_sub_f32 v212, 0, v192\n"
    "v_sub_f32 v208, 0, v193\n"
    "v_mfma_f32_16x16x32_bf16 v[172:175], a[124:127], a[28:31], v[172:175]\n"
    "v_sub_f32 v216, 0, v194\n"
    "v_sub_f32 v220, 0, v195\n"
    "v_fma_f32 v232, v136, s36, v212\n"
    "v_fma_f32 v233, v137, s36, v212\n"
    "v_fma_f32 v234, v138, s36, v212\n"
    "v_fma_f32 v235, v139, s36, v212\n"
    "v_fma_f32 v236, v140, s36, v212\n"
    "v_mfma_f32_16x16x32_bf16 v[160:163], a[96:99], a[0:3], 0\n"
    "buffer_load_dwordx4 v252, s[40:43], s56 offen offset:3072 lds\n"
    "v_exp_f32 v232, v232\n"
    "v_fma_f32 v237, v141, s36, v212\n"
    "v_mfma_f32_16x16x32_bf16 v[160:163], a[100:103], a[4:7], v[160:163]\n"
    "s_and_b32 m0, s59, 0xffff\n"
    "s_bitset1_b32 m0, 16\n"
    "ds_read_b64_tr_b16 a[146:147], v247 offset:4608\n"
    "v_exp_f32 v233, v233\n"
    "s_nop 0\n"
    "v_cvt_pk_bf16_f32 v196, v232, v233\n"
    "v_fma_f32 v238, v142, s36, v212\n"
    "v_fma_f32 v239, v143, s36, v212\n"
    "v_fma_f32 v240, v128, s36, v208\n"
    "v_mfma_f32_16x16x32_bf16 v[160:163], a[104:107], a[8:11], v[160:163]\n"
    "s_cmp_eq_i32 s92, 0\n"
    "buffer_load_dwordx4 v251, s[44:47], s57 offen offset:0 lds\n"
    "v_exp_f32 v234, v234\n"
    "v_exp_f32 v235, v235\n"
    "v_mfma_f32_16x16x32_bf16 v[160:163], a[108:111], a[12:15], v[160:163]\n"
    "s_cselect_b64 s[72:73], s[64:65], s[84:85]\n"
    "v_exp_f32 v236, v236\n"
    "v_exp_f32 v237, v237\n"
    "v_exp_f32 v238, v238\n"
    "v_exp_f32 v239, v239\n"
    "v_mfma_f32_16x16x32_bf16 v[164:167], a[112:115], a[0:3], 0\n"
    "s_cselect_b64 s[74:75], s[66:67], s[84:85]\n"
    "buffer_load_dwordx4 v252, s[44:47], s57 offen offset:1024 lds\n"
    "v_cvt_pk_bf16_f32 v197, v234, v235\n"
    "v_cvt_pk_bf16_f32 v198, v236, v237\n"
    "v_cvt_pk_bf16_f32 v199, v238, v239\n"
    "s_nop 1\n"
    "v_mfma_f32_16x16x32_bf16 v[224:227], a[164:167], v[196:199], v[228:231]\n"
    "s_cselect_b64 s[76:77], s[68:69], s[84:85]\n"
    "v_exp_f32 v240, v240\n"
    "v_fma_f32 v236, v129, s36, v208\n"
    "v_fma_f32 v237, v130, s36, v208\n"
    "v_fma_f32 v238, v131, s36, v208\n"
    "v_fma_f32 v239, v132, s36, v208\n"
    "v_fma_f32 v241, v133, s36, v208\n"
    "v_mfma_f32_16x16x32_bf16 v[164:167], a[116:119], a[4:7], v[164:167]\n"
    "s_cselect_b64 s[78:79], s[70:71], s[84:85]\n"
    "buffer_load_dwordx4 v251, s[44:47], s57 offen offset:2048 lds\n"
    "v_exp_f32 v236, v236\n"
    "s_nop 0\n"
    "v_cvt_pk_bf16_f32 v192, v240, v236\n"
    "v_fma_f32 v242, v134, s36, v208\n"
    "v_mfma_f32_16x16x32_bf16 v[164:167], a[120:123], a[8:11], v[164:167]\n"
    "s_cmp_lg_u32 s20, 0\n"
    "v_exp_f32 v237, v237\n"
    "v_exp_f32 v238, v238\n"
    "v_exp_f32 v239, v239\n"
    "v_fma_f32 v236, v135, s36, v208\n"
    "v_mfma_f32_16x16x32_bf16 v[164:167], a[124:127], a[12:15], v[164:167]\n"
    "s_cselect_b32 s60, s63, 0\n"
    "buffer_load_dwordx4 v252, s[44:47], s57 offen offset:3072 lds\n"
    "v_exp_f32 v241, v241\n"
    "v_fma_f32 v240, v144, s36, v216\n"
    "v_mfma_f32_16x16x32_bf16 v[176:179], a[96:99], a[64:67], 0\n"
    "s_add_u32 s58, s58, 16384\n"
    "s_cmp_lt_u32 s58, s17\n"
    "s_cselect_b32 s56, s18, 0\n"
    "s_cmp_lt_u32 s58, s16\n"
    "s_cselect_b32 s56, 0x40000, s56\n"
    "s_add_u32 s56, s56, s58\n"
    "s_and_b32 m0, s58, 0xffff\n"
    "ds_read_b64_tr_b16 a[148:149], v250 offset:512\n"
    "ds_read_b64_tr_b16 a[150:151], v250 offset:4608\n"
    "v_mfma_f32_16x16x32_bf16 v[176:179], a[100:103], a[68:71], v[176:179]\n"
    "s_add_u32 s60, s60, 32768\n"
    "buffer_load_dwordx4 v251, s[40:43], s56 offen offset:0 lds\n"
    "v_cvt_pk_bf16_f32 v193, v237, v238\n"
    "v_cvt_pk_bf16_f32 v194, v239, v241\n"
    "v_fma_f32 v243, v145, s36, v216\n"
    "v_mfma_f32_16x16x32_bf16 v[176:179], a[104:107], a[72:75], v[176:179]\n"
    "s_add_u32 s59, s59, 16384\n"
    "v_exp_f32 v242, v242\n"
    "v_exp_f32 v236, v236\n"
    "s_nop 0\n"
    "v_cvt_pk_bf16_f32 v195, v242, v236\n"
    "v_fma_f32 v237, v146, s36, v216\n"
    "v_fma_f32 v238, v147, s36, v216\n"
    "v_fma_f32 v239, v148, s36, v216\n"
    "v_mfma_f32_16x16x32_bf16 v[224:227], a[160:163], v[192:195], v[224:227]\n"
    "s_cmp_lt_u32 s59, s17\n"
    "buffer_load_dwordx4 v252, s[40:43], s56 offen offset:1024 lds\n"
    "v_exp_f32 v240, v240\n"
    "v_fma_f32 v236, v149, s36, v216\n"
    "v_mfma_f32_16x16x32_bf16 v[176:179], a[108:111], a[76:79], v[176:179]\n"
    "s_cselect_b32 s57, s18, 0\n"
    "v_exp_f32 v243, v243\n"
    "s_nop 0\n"
    "v_cvt_pk_bf16_f32 v200, v240, v243\n"
    "v_exp_f32 v237, v237\n"
    "v_exp_f32 v238, v238\n"
    "v_mfma_f32_16x16x32_bf16 v[180:183], a[112:115], a[64:67], 0\n"
    "s_cmp_lt_u32 s59, s16\n"
    "buffer_load_dwordx4 v251, s[40:43], s56 offen offset:2048 lds\n"
    "v_exp_f32 v239, v239\n"
    "v_fma_f32 v240, v150, s36, v216\n"
    "v_mfma_f32_16x16x32_bf16 v[180:183], a[116:119], a[68:71], v[180:183]\n"
    "s_cselect_b32 s57, 0x40000, s57\n"
    "ds_read_b64_tr_b16 a[152:153], v247 offset:768\n"
    "v_exp_f32 v236, v236\n"
    "v_exp_f32 v240, v240\n"
    "v_fma_f32 v241, v151, s36, v216\n"
    "v_fma_f32 v242, v152, s36, v220\n"
    "v_mfma_f32_16x16x32_bf16 v[180:183], a[120:123], a[72:75], v[180:183]\n"
    "s_add_u32 s57, s57, s59\n"
    "buffer_load_dwordx4 v252, s[40:43], s56 offen offset:3072 lds\n"
    "v_cvt_pk_bf16_f32 v201, v237, v238\n"
    "v_cvt_pk_bf16_f32 v202, v239, v236\n"
    "v_fma_f32 v243, v153, s36, v220\n"
    "v_mfma_f32_16x16x32_bf16 v[180:183], a[124:127], a[76:79], v[180:183]\n"
    "s_and_b32 m0, s59, 0xffff\n"
    "s_bitset1_b32 m0, 16\n"
    "v_cndmask_b32 v168, v168, v254, s[72:73]\n"
    "v_cndmask_b32 v169, v169, v254, s[74:75]\n"
    "v_exp_f32 v241, v241\n"
    "s_nop 0\n"
    "v_cvt_pk_bf16_f32 v203, v240, v241\n"
    "s_nop 1\n"
    "v_mfma_f32_16x16x32_bf16 v[224:227], a[168:171], v[200:203], v[224:227]\n"
    "s_add_u32 s58, s58, 16384\n"
    "buffer_load_dwordx4 v251, s[44:47], s57 offen offset:0 lds\n"
    "ds_read_b64_tr_b16 a[154:155], v247 offset:4864\n"
    "v_exp_f32 v242, v242\n"
    "v_mfma_f32_16x16x32_bf16 v[184:187], a[96:99], a[80:83], 0\n"
    "s_cmp_lt_u32 s58, s17\n"
    "v_cndmask_b32 v170, v170, v254, s[76:77]\n"
    "v_exp_f32 v243, v243\n"
    "s_nop 0\n"
    "v_cvt_pk_bf16_f32 v204, v242, v243\n"
    "v_fma_f32 v236, v154, s36, v220\n"
    "v_fma_f32 v237, v155, s36, v220\n"
    "v_fma_f32 v238, v156, s36, v220\n"
    "v_mfma_f32_16x16x32_bf16 v[184:187], a[100:103], a[84:87], v[184:187]\n"
    "s_cselect_b32 s56, s18, 0\n"
    "buffer_load_dwordx4 v252, s[44:47], s57 offen offset:1024 lds\n"
    "ds_read_b64_tr_b16 a[156:157], v250 offset:768\n"
    "ds_read_b64_tr_b16 a[158:159], v250 offset:4864\n"
    "v_mfma_f32_16x16x32_bf16 v[184:187], a[104:107], a[88:91], v[184:187]\n"
    "s_cmp_lt_u32 s58, s16\n"
    "v_exp_f32 v236, v236\n"
    "v_exp_f32 v237, v237\n"
    "v_fma_f32 v239, v157, s36, v220\n"
    "v_fma_f32 v240, v158, s36, v220\n"
    "v_fma_f32 v241, v159, s36, v220\n"
    "v_mfma_f32_16x16x32_bf16 v[184:187], a[108:111], a[92:95], v[184:187]\n"
    "s_cselect_b32 s56, 0x40000, s56\n"
    "buffer_load_dwordx4 v251, s[44:47], s57 offen offset:2048 lds\n"
    "v_cndmask_b32 v171, v171, v254, s[78:79]\n"
    "v_exp_f32 v238, v238\n"
    "v_mfma_f32_16x16x32_bf16 v[188:191], a[112:115], a[80:83], 0\n"
    "s_add_u32 s56, s56, s58\n"
    "v_exp_f32 v239, v239\n"
    "v_exp_f32 v240, v240\n"
    "v_exp_f32 v241, v241\n"
    "s_nop 0\n"
    "v_cvt_pk_bf16_f32 v207, v240, v241\n"
    "v_mfma_f32_16x16x32_bf16 v[188:191], a[116:119], a[84:87], v[188:191]\n"
    "buffer_load_dwordx4 v252, s[44:47], s57 offen offset:3072 lds\n"
    "v_cvt_pk_bf16_f32 v205, v236, v237\n"
    "v_cvt_pk_bf16_f32 v206, v238, v239\n"
    "v_fma_f32 v232, v168, s36, v212\n"
    "s_nop 0\n"
    "v_mfma_f32_16x16x32_bf16 v[224:227], a[172:175], v[204:207], v[224:227]\n"
    "s_add_u32 s59, s59, 16384\n"
    "s_cmp_lt_u32 s59, s17\n"
    "s_cselect_b32 s57, s18, 0\n"
    "s_cmp_lt_u32 s59, s16\n"
    "s_cselect_b32 s57, 0x40000, s57\n"
    "s_add_u32 s57, s57, s59\n"
    "v_add_u32 v246, s60, v248\n"
    "v_fma_f32 v233, v169, s36, v212\n"
    "v_mfma_f32_16x16x32_bf16 v[188:191], a[120:123], a[88:91], v[188:191]\n"
    "v_exp_f32 v232, v232\n"
    "v_exp_f32 v233, v233\n"
    "v_fma_f32 v234, v170, s36, v212\n"
    "v_exp_f32 v234, v234\n"
    "v_mfma_f32_16x16x32_bf16 v[188:191], a[124:127], a[92:95], v[188:191]\n"
    "v_fma_f32 v235, v171, s36, v212\n"
    "v_exp_f32 v235, v235\n"
    "v_max3_f32 v241, v224, v225, v226\n"
    "v_max_f32 v241, v241, v227\n"
    "v_cmp_nge_f32 vcc, s37, v241\n"
    "s_cbranch_vccnz L_rc_T0a\n"
    "L_rr_T0a:\n"
    "s_waitcnt lgkmcnt(0)\n"
    "s_nop 0\n"
    "s_cmp_eq_u32 s20, 0\n"
    "s_cbranch_scc1 L_T0b\n"
    ".p2align 6\n"
    "L_L2:\n"
    "v_mfma_f32_16x16x32_bf16 v[32:35], a[128:131], v[196:199], v[32:35]\n"
    "s_cmp_eq_u32 s62, s15\n"
    "v_fma_f32 v236, v172, s36, v212\n"
    "v_fma_f32 v237, v173, s36, v212\n"
    "v_mfma_f32_16x16x32_bf16 v[36:39], a[132:135], v[196:199], v[36:39]\n"
    "s_cbranch_scc1 L_qs\n"
    "L_qsr:\n"
    "v_fma_f32 v238, v174, s36, v212\n"
    "v_fma_f32 v239, v175, s36, v212\n"
    "v_mfma_f32_16x16x32_bf16 v[40:43], a[136:139], v[196:199], v[40:43]\n"
    "s_waitcnt vmcnt(16)\n"
    "v_exp_f32 v236, v236\n"
    "v_mfma_f32_16x16x32_bf16 v[44:47], a[140:143], v[196:199], v[44:47]\n"
    "s_barrier\n"
    "v_exp_f32 v237, v237\n"
    "v_fma_f32 v240, v160, s36, v208\n"
    "v_mfma_f32_16x16x32_bf16 v[48:51], a[144:147], v[196:199], v[48:51]\n"
    "ds_read_b128 a[96:99], v246 offset:0\n"
    "v_exp_f32 v238, v238\n"
    "v_mfma_f32_16x16x32_bf16 v[52:55], a[148:151], v[196:199], v[52:55]\n"
    "ds_read_b128 a[100:103], v246 offset:256\n"
    "v_fma_f32 v241, v161, s36, v208\n"
    "v_fma_f32 v242, v162, s36, v208\n"
    "v_mfma_f32_16x16x32_bf16 v[56:59], a[152:155], v[196:199], v[56:59]\n"
    "v_exp_f32 v239, v239\n"
    "v_mfma_f32_16x16x32_bf16 v[60:63], a[156:159], v[196:199], v[60:63]\n"
    "v_cvt_pk_bf16_f32 v197, v234, v235\n"
    "v_cvt_pk_bf16_f32 v198, v236, v237\n"
    "v_mfma_f32_16x16x32_bf16 v[0:3], a[128:131], v[192:195], v[0:3]\n"
    "v_cvt_pk_bf16_f32 v196, v232, v233\n"
    "v_exp_f32 v240, v240\n"
    "v_mfma_f32_16x16x32_bf16 v[4:7], a[132:135], v[192:195], v[4:7]\n"
    "v_exp_f32 v241, v241\n"
    "v_fma_f32 v236, v163, s36, v208\n"
    "v_mfma_f32_16x16x32_bf16 v[8:11], a[136:139], v[192:195], v[8:11]\n"
    "v_cvt_pk_bf16_f32 v199, v238, v239\n"
    "v_fma_f32 v237, v164, s36, v208\n"
    "v_fma_f32 v243, v165, s36, v208\n"
    "v_mfma_f32_16x16x32_bf16 v[12:15], a[140:143], v[192:195], v[12:15]\n"
    "v_exp_f32 v242, v242\n"
    "v_mfma_f32_16x16x32_bf16 v[228:231], a[164:167], v[196:199], v[224:227]\n"
    "v_exp_f32 v236, v236\n"
    "v_mfma_f32_16x16x32_bf16 v[16:19], a[144:147], v[192:195], v[16:19]\n"
    "s_and_b32 m0, s58, 0xffff\n"
    "v_exp_f32 v237, v237\n"
    "v_fma_f32 v238, v166, s36, v208\n"
    "v_mfma_f32_16x16x32_bf16 v[20:23], a[148:151], v[192:195], v[20:23]\n"
    "buffer_load_dwordx4 v251, s[40:43], s56 offen offset:0 lds\n"
    "v_mfma_f32_16x16x32_bf16 v[24:27], a[152:155], v[192:195], v[24:27]\n"
    "ds_read_b128 a[104:107], v246 offset:512\n"
    "ds_read_b128 a[108:111], v246 offset:768\n"
    "v_mfma_f32_16x16x32_bf16 v[28:31], a[156:159], v[192:195], v[28:31]\n"
    "v_cvt_pk_bf16_f32 v193, v242, v236\n"
    "v_exp_f32 v243, v243\n"
    "v_mfma_f32_16x16x32_bf16 a[176:179], a[128:131], v[200:203], a[176:179]\n"
    "v_fma_f32 v236, v167, s36, v208\n"
    "v_fma_f32 v239, v176, s36, v216\n"
    "v_mfma_f32_16x16x32_bf16 a[208:211], a[128:131], v[204:207], a[208:211]\n"
    "buffer_load_dwordx4 v252, s[40:43], s56 offen offset:1024 lds\n"
    "v_mfma_f32_16x16x32_bf16 a[180:183], a[132:135], v[200:203], a[180:183]\n"
    "ds_read_b64_tr_b16 a[128:129], v247 offset:16384\n"
    "v_exp_f32 v238, v238\n"
    "v_mfma_f32_16x16x32_bf16 a[212:215], a[132:135], v[204:207], a[212:215]\n"
    "v_cvt_pk_bf16_f32 v192, v240, v241\n"
    "v_exp_f32 v236, v236\n"
    "v_mfma_f32_16x16x32_bf16 a[184:187], a[136:139], v[200:203], a[184:187]\n"
    "v_fma_f32 v240, v177, s36, v216\n"
    "v_fma_f32 v241, v178, s36, v216\n"
    "v_mfma_f32_16x16x32_bf16 a[216:219], a[136:139], v[204:207], a[216:219]\n"
    "ds_read_b128 a[112:115], v246 offset:4096\n"
    "ds_read_b128 a[116:119], v246 offset:4352\n"
    "v_cvt_pk_bf16_f32 v194, v237, v243\n"
    "v_mfma_f32_16x16x32_bf16 a[188:191], a[140:143], v[200:203], a[188:191]\n"
    "buffer_load_dwordx4 v251, s[40:43], s56 offen offset:2048 lds\n"
    "v_mfma_f32_16x16x32_bf16 a[220:223], a[140:143], v[204:207], a[220:223]\n"
    "ds_read_b64_tr_b16 a[130:131], v247 offset:20480\n"
    "v_cvt_pk_bf16_f32 v195, v238, v236\n"
    "v_mfma_f32_16x16x32_bf16 a[192:195], a[144:147], v[200:203], a[192:195]\n"
    "ds_read_b128 a[120:123], v246 offset:4608\n"
    "v_exp_f32 v239, v239\n"
    "v_mfma_f32_16x16x32_bf16 a[224:227], a[144:147], v[204:207], a[224:227]\n"
    "ds_read_b128 a[124:127], v246 offset:4864\n"
    "v_fma_f32 v236, v179, s36, v216\n"
    "v_fma_f32 v237, v180, s36, v216\n"
    "v_mfma_f32_16x16x32_bf16 v[228:231], a[160:163], v[192:195], v[228:231]\n"
    "v_exp_f32 v240, v240\n"
    "v_fma_f32 v238, v181, s36, v216\n"
    "v_mfma_f32_16x16x32_bf16 a[196:199], a[148:151], v[200:203], a[196:199]\n"
    "buffer_load_dwordx4 v252, s[40:43], s56 offen offset:3072 lds\n"
    "v_mfma_f32_16x16x32_bf16 a[228:231], a[148:151], v[204:207], a[228:231]\n"
    "s_add_u32 s58, s58, 16384\n"
    "v_exp_f32 v241, v241\n"
    "v_mfma_f32_16x16x32_bf16 a[200:203], a[152:155], v[200:203], a[200:203]\n"
    "v_fma_f32 v242, v182, s36, v216\n"
    "v_fma_f32 v243, v183, s36, v216\n"
    "v_mfma_f32_16x16x32_bf16 a[232:235], a[152:155], v[204:207], a[232:235]\n"
    "s_and_b32 m0, s59, 0xffff\n"
    "v_exp_f32 v236, v236\n"
    "v_mfma_f32_16x16x32_bf16 a[204:207], a[156:159], v[200:203], a[204:207]\n"
    "s_bitset1_b32 m0, 16\n"
    "v_exp_f32 v237, v237\n"
    "v_mfma_f32_16x16x32_bf16 a[236:239], a[156:159], v[204:207], a[236:239]\n"
    "s_waitcnt lgkmcnt(2)\n"
    "s_cmp_lt_u32 s58, s17\n"
    "buffer_load_dwordx4 v251, s[44:47], s57 offen offset:0 lds\n"
    "v_mfma_f32_16x16x32_bf16 v[136:139], a[96:99], a[16:19], 0\n"
    "s_cselect_b32 s56, s18, 0\n"
    "ds_read_b64_tr_b16 a[132:133], v250 offset:16384\n"
    "v_cvt_pk_bf16_f32 v200, v239, v240\n"
    "v_cvt_pk_bf16_f32 v201, v241, v236\n"
    "v_mfma_f32_16x16x32_bf16 v[136:139], a[100:103], a[20:23], v[136:139]\n"
    "s_cmp_lt_u32 s58, s16\n"
    "v_exp_f32 v238, v238\n"
    "v_mfma_f32_16x16x32_bf16 v[136:139], a[104:107], a[24:27], v[136:139]\n"
    "s_cselect_b32 s56, 0x40000, s56\n"
    "v_exp_f32 v242, v242\n"
    "v_fma_f32 v236, v184, s36, v220\n"
    "v_mfma_f32_16x16x32_bf16 v[136:139], a[108:111], a[28:31], v[136:139]\n"
    "s_add_u32 s56, s56, s58\n"
    "v_exp_f32 v243, v243\n"
    "v_fma_f32 v239, v185, s36, v220\n"
    "v_mfma_f32_16x16x32_bf16 v[140:143], a[112:115], a[16:19], 0\n"
    "buffer_load_dwordx4 v252, s[44:47], s57 offen offset:1024 lds\n"
    "v_mfma_f32_16x16x32_bf16 v[140:143], a[116:119], a[20:23], v[140:143]\n"
    "s_waitcnt lgkmcnt(1)\n"
    "v_cvt_pk_bf16_f32 v203, v242, v243\n"
    "v_fma_f32 v240, v186, s36, v220\n"
    "v_mfma_f32_16x16x32_bf16 v[140:143], a[120:123], a[24:27], v[140:143]\n"
    "v_cvt_pk_bf16_f32 v202, v237, v238\n"
    "v_exp_f32 v236, v236\n"
    "v_mfma_f32_16x16x32_bf16 v[140:143], a[124:127], a[28:31], v[140:143]\n"
    "v_exp_f32 v239, v239\n"
    "v_mfma_f32_16x16x32_bf16 v[128:131], a[96:99], a[0:3], 0\n"
    "v_fma_f32 v237, v187, s36, v220\n"
    "v_fma_f32 v238, v188, s36, v220\n"
    "v_mfma_f32_16x16x32_bf16 v[228:231], a[168:171], v[200:203], v[228:231]\n"
    "buffer_load_dwordx4 v251, s[44:47], s57 offen offset:2048 lds\n"
    "v_mfma_f32_16x16x32_bf16 v[128:131], a[100:103], a[4:7], v[128:131]\n"
    "ds_read_b64_tr_b16 a[134:135], v250 offset:20480\n"
    "ds_read_b64_tr_b16 a[136:137], v247 offset:16640\n"
    "v_mfma_f32_16x16x32_bf16 v[128:131], a[104:107], a[8:11], v[128:131]\n"
    "v_exp_f32 v240, v240\n"
    "v_fma_f32 v241, v189, s36, v220\n"
    "v_mfma_f32_16x16x32_bf16 v[128:131], a[108:111], a[12:15], v[128:131]\n"
    "v_exp_f32 v237, v237\n"
    "v_mfma_f32_16x16x32_bf16 v[132:135], a[112:115], a[0:3], 0\n"
    "ds_read_b64_tr_b16 a[138:139], v247 offset:20736\n"
    "ds_read_b64_tr_b16 a[140:141], v250 offset:16640\n"
    "v_mfma_f32_16x16x32_bf16 v[132:135], a[116:119], a[4:7], v[132:135]\n"
    "buffer_load_dwordx4 v252, s[44:47], s57 offen offset:3072 lds\n"
    "v_mfma_f32_16x16x32_bf16 v[132:135], a[120:123], a[8:11], v[132:135]\n"
    "s_add_u32 s59, s59, 16384\n"
    "v_exp_f32 v238, v238\n"
    "v_mfma_f32_16x16x32_bf16 v[132:135], a[124:127], a[12:15], v[132:135]\n"
    "s_cmp_lt_u32 s59, s17\n"
    "ds_read_b64_tr_b16 a[142:143], v250 offset:20736\n"
    "ds_read_b64_tr_b16 a[144:145], v247 offset:16896\n"
    "v_mfma_f32_16x16x32_bf16 v[144:147], a[96:99], a[64:67], 0\n"
    "s_cselect_b32 s57, s18, 0\n"
    "v_exp_f32 v241, v241\n"
    "v_mfma_f32_16x16x32_bf16 v[144:147], a[100:103], a[68:71], v[144:147]\n"
    "s_cmp_lt_u32 s59, s16\n"
    "ds_read_b64_tr_b16 a[146:147], v247 offset:20992\n"
    "v_fma_f32 v242, v190, s36, v220\n"
    "v_mfma_f32_16x16x32_bf16 v[144:147], a[104:107], a[72:75], v[144:147]\n"
    "s_cselect_b32 s57, 0x40000, s57\n"
    "v_exp_f32 v242, v242\n"
    "v_mfma_f32_16x16x32_bf16 v[144:147], a[108:111], a[76:79], v[144:147]\n"
    "s_add_u32 s57, s57, s59\n"
    "v_cvt_pk_bf16_f32 v206, v238, v241\n"
    "v_fma_f32 v243, v191, s36, v220\n"
    "v_mfma_f32_16x16x32_bf16 v[148:151], a[112:115], a[64:67], 0\n"
    "v_exp_f32 v243, v243\n"
    "v_mfma_f32_16x16x32_bf16 v[148:151], a[116:119], a[68:71], v[148:151]\n"
    "v_cvt_pk_bf16_f32 v204, v236, v239\n"
    "v_cvt_pk_bf16_f32 v205, v240, v237\n"
    "v_mfma_f32_16x16x32_bf16 v[148:151], a[120:123], a[72:75], v[148:151]\n"
    "ds_read_b64_tr_b16 a[148:149], v250 offset:16896\n"
    "v_fma_f32 v232, v136, s36, v212\n"
    "v_mfma_f32_16x16x32_bf16 v[148:151], a[124:127], a[76:79], v[148:151]\n"
    "ds_read_b64_tr_b16 a[150:151], v250 offset:20992\n"
    "v_cvt_pk_bf16_f32 v207, v242, v243\n"
    "v_mfma_f32_16x16x32_bf16 v[152:155], a[96:99], a[80:83], 0\n"
    "v_exp_f32 v232, v232\n"
    "v_mfma_f32_16x16x32_bf16 v[228:231], a[172:175], v[204:207], v[228:231]\n"
    "ds_read_b64_tr_b16 a[152:153], v247 offset:17152\n"
    "ds_read_b64_tr_b16 a[154:155], v247 offset:21248\n"
    "v_mfma_f32_16x16x32_bf16 v[152:155], a[100:103], a[84:87], v[152:155]\n"
    "ds_read_b64_tr_b16 a[156:157], v250 offset:17152\n"
    "ds_read_b64_tr_b16 a[158:159], v250 offset:21248\n"
    "v_mfma_f32_16x16x32_bf16 v[152:155], a[104:107], a[88:91], v[152:155]\n"
    "v_fma_f32 v233, v137, s36, v212\n"
    "v_fma_f32 v234, v138, s36, v212\n"
    "v_mfma_f32_16x16x32_bf16 v[152:155], a[108:111], a[92:95], v[152:155]\n"
    "v_exp_f32 v233, v233\n"
    "v_mfma_f32_16x16x32_bf16 v[156:159], a[112:115], a[80:83], 0\n"
    "v_exp_f32 v234, v234\n"
    "v_mfma_f32_16x16x32_bf16 v[156:159], a[116:119], a[84:87], v[156:159]\n"
    "s_nop 0\n"
    "v_fma_f32 v235, v139, s36, v212\n"
    "v_max3_f32 v241, v228, v229, v230\n"
    "v_mfma_f32_16x16x32_bf16 v[156:159], a[120:123], a[88:91], v[156:159]\n"
    "s_cmp_eq_u32 s62, s22\n"
    "v_max_f32 v241, v241, v231\n"
    "v_cmp_nge_f32 vcc, s37, v241\n"
    "v_mfma_f32_16x16x32_bf16 v[156:159], a[124:127], a[92:95], v[156:159]\n"
    "s_waitcnt lgkmcnt(0)\n"
    "v_exp_f32 v235, v235\n"
    "s_cbranch_vccnz L_rc_L2a\n"
    "L_rr_L2a:\n"
    "s_cbranch_scc1 L_T2b\n"
    "v_mfma_f32_16x16x32_bf16 v[32:35], a[128:131], v[196:199], v[32:35]\n"
    "v_fma_f32 v236, v140, s36, v212\n"
    "v_fma_f32 v237, v141, s36, v212\n"
    "v_mfma_f32_16x16x32_bf16 v[36:39], a[132:135], v[196:199], v[36:39]\n"
    "v_fma_f32 v238, v142, s36, v212\n"
    "v_fma_f32 v239, v143, s36, v212\n"
    "v_fma_f32 v240, v128, s36, v208\n"
    "v_mfma_f32_16x16x32_bf16 v[40:43], a[136:139], v[196:199], v[40:43]\n"
    "s_waitcnt vmcnt(16)\n"
    "v_exp_f32 v236, v236\n"
    "v_mfma_f32_16x16x32_bf16 v[44:47], a[140:143], v[196:199], v[44:47]\n"
    "s_barrier\n"
    "v_exp_f32 v237, v237\n"
    "v_fma_f32 v241, v129, s36, v208\n"
    "v_mfma_f32_16x16x32_bf16 v[48:51], a[144:147], v[196:199], v[48:51]\n"
    "s_add_u32 s61, s61, 32768\n"
    "v_exp_f32 v238, v238\n"
    "v_fma_f32 v242, v130, s36, v208\n"
    "v_mfma_f32_16x16x32_bf16 v[52:55], a[148:151], v[196:199], v[52:55]\n"
    "s_and_b32 s61, s61, 0xffff\n"
    "v_exp_f32 v239, v239\n"
    "v_mfma_f32_16x16x32_bf16 v[56:59], a[152:155], v[196:199], v[56:59]\n"
    "s_cmp_eq_u32 s62, 1\n"
    "v_exp_f32 v240, v240\n"
    "v_fma_f32 v243, v131, s36, v208\n"
    "v_mfma_f32_16x16x32_bf16 v[60:63], a[156:159], v[196:199], v[60:63]\n"
    "s_cselect_b32 s94, s21, 32768\n"
    "ds_read_b128 a[96:99], v246 offset:16384\n"
    "v_cvt_pk_bf16_f32 v198, v236, v237\n"
    "v_mfma_f32_16x16x32_bf16 v[0:3], a[128:131], v[192:195], v[0:3]\n"
    "s_add_u32 s60, s60, s94\n"
    "v_add_u32 v247, s61, v249\n"
    "v_cvt_pk_bf16_f32 v197, v234, v235\n"
    "v_cvt_pk_bf16_f32 v199, v238, v239\n"
    "v_mfma_f32_16x16x32_bf16 v[4:7], a[132:135], v[192:195], v[4:7]\n"
    "s_and_b32 s60, s60, 0xffff\n"
    "ds_read_b128 a[100:103], v246 offset:16640\n"
    "v_fma_f32 v236, v132, s36, v208\n"
    "v_fma_f32 v237, v133, s36, v208\n"
    "v_mfma_f32_16x16x32_bf16 v[8:11], a[136:139], v[192:195], v[8:11]\n"
    "v_cvt_pk_bf16_f32 v196, v232, v233\n"
    "v_exp_f32 v241, v241\n"
    "v_mfma_f32_16x16x32_bf16 v[12:15], a[140:143], v[192:195], v[12:15]\n"
    "ds_read_b128 a[104:107], v246 offset:16896\n"
    "v_xor_b32 v250, 32, v247\n"
    "v_fma_f32 v238, v134, s36, v208\n"
    "v_mfma_f32_16x16x32_bf16 v[224:227], a[164:167], v[196:199], v[228:231]\n"
    "v_exp_f32 v242, v242\n"
    "v_mfma_f32_16x16x32_bf16 v[16:19], a[144:147], v[192:195], v[16:19]\n"
    "s_and_b32 m0, s58, 0xffff\n"
    "v_exp_f32 v243, v243\n"
    "v_mfma_f32_16x16x32_bf16 v[20:23], a[148:151], v[192:195], v[20:23]\n"
    "buffer_load_dwordx4 v251, s[40:43], s56 offen offset:0 lds\n"
    "v_mfma_f32_16x16x32_bf16 v[24:27], a[152:155], v[192:195], v[24:27]\n"
    "v_exp_f32 v236, v236\n"
    "v_mfma_f32_16x16x32_bf16 v[28:31], a[156:159], v[192:195], v[28:31]\n"
    "ds_read_b128 a[108:111], v246 offset:17152\n"
    "v_exp_f32 v237, v237\n"
    "v_mfma_f32_16x16x32_bf16 a[176:179], a[128:131], v[200:203], a[176:179]\n"
    "v_cvt_pk_bf16_f32 v194, v236, v237\n"
    "v_exp_f32 v238, v238\n"
    "v_mfma_f32_16x16x32_bf16 a[208:211], a[128:131], v[204:207], a[208:211]\n"
    "buffer_load_dwordx4 v252, s[40:43], s56 offen offset:1024 lds\n"
    "v_mfma_f32_16x16x32_bf16 a[180:183], a[132:135], v[200:203], a[180:183]\n"
    "v_fma_f32 v236, v135, s36, v208\n"
    "v_fma_f32 v237, v144, s36, v216\n"
    "v_fma_f32 v239, v145, s36, v216\n"
    "v_mfma_f32_16x16x32_bf16 a[212:215], a[132:135], v[204:207], a[212:215]\n"
    "v_cvt_pk_bf16_f32 v192, v240, v241\n"
    "v_exp_f32 v236, v236\n"
    "v_mfma_f32_16x16x32_bf16 a[184:187], a[136:139], v[200:203], a[184:187]\n"
    "v_cvt_pk_bf16_f32 v193, v242, v243\n"
    "v_exp_f32 v237, v237\n"
    "v_mfma_f32_16x16x32_bf16 a[216:219], a[136:139], v[204:207], a[216:219]\n"
    "ds_read_b64_tr_b16 a[128:129], v247 offset:0\n"
    "v_fma_f32 v240, v146, s36, v216\n"
    "v_mfma_f32_16x16x32_bf16 a[188:191], a[140:143], v[200:203], a[188:191]\n"
    "buffer_load_dwordx4 v251, s[40:43], s56 offen offset:2048 lds\n"
    "v_mfma_f32_16x16x32_bf16 a[220:223], a[140:143], v[204:207], a[220:223]\n"
    "v_exp_f32 v239, v239\n"
    "v_mfma_f32_16x16x32_bf16 a[192:195], a[144:147], v[200:203], a[192:195]\n"
    "ds_read_b64_tr_b16 a[130:131], v247 offset:4096\n"
    "v_cvt_pk_bf16_f32 v195, v238, v236\n"
    "v_fma_f32 v241, v147, s36, v216\n"
    "v_mfma_f32_16x16x32_bf16 a[224:227], a[144:147], v[204:207], a[224:227]\n"
    "ds_read_b128 a[112:115], v246 offset:20480\n"
    "v_fma_f32 v236, v148, s36, v216\n"
    "v_fma_f32 v238, v149, s36, v216\n"
    "v_mfma_f32_16x16x32_bf16 v[224:227], a[160:163], v[192:195], v[224:227]\n"
    "v_exp_f32 v240, v240\n"
    "v_mfma_f32_16x16x32_bf16 a[196:199], a[148:151], v[200:203], a[196:199]\n"
    "buffer_load_dwordx4 v252, s[40:43], s56 offen offset:3072 lds\n"
    "v_mfma_f32_16x16x32_bf16 a[228:231], a[148:151], v[204:207], a[228:231]\n"
    "s_add_u32 s58, s58, 16384\n"
    "ds_read_b128 a[116:119], v246 offset:20736\n"
    "v_exp_f32 v241, v241\n"
    "v_mfma_f32_16x16x32_bf16 a[200:203], a[152:155], v[200:203], a[200:203]\n"
    "v_exp_f32 v236, v236\n"
    "v_fma_f32 v242, v150, s36, v216\n"
    "v_mfma_f32_16x16x32_bf16 a[232:235], a[152:155], v[204:207], a[232:235]\n"
    "s_and_b32 m0, s59, 0xffff\n"
    "ds_read_b64_tr_b16 a[132:133], v250 offset:0\n"
    "ds_read_b64_tr_b16 a[134:135], v250 offset:4096\n"
    "v_mfma_f32_16x16x32_bf16 a[204:207], a[156:159], v[200:203], a[204:207]\n"
    "s_bitset1_b32 m0, 16\n"
    "ds_read_b128 a[120:123], v246 offset:20992\n"
    "v_exp_f32 v238, v238\n"
    "v_mfma_f32_16x16x32_bf16 a[236:239], a[156:159], v[204:207], a[236:239]\n"
    "s_waitcnt lgkmcnt(5)\n"
    "s_cmp_lt_u32 s58, s17\n"
    "buffer_load_dwordx4 v251, s[44:47], s57 offen offset:0 lds\n"
    "v_mfma_f32_16x16x32_bf16 v[168:171], a[96:99], a[16:19], 0\n"
    "s_cselect_b32 s56, s18, 0\n"
    "ds_read_b128 a[124:127], v246 offset:21248\n"
    "ds_read_b64_tr_b16 a[136:137], v247 offset:256\n"
    "v_mfma_f32_16x16x32_bf16 v[168:171], a[100:103], a[20:23], v[168:171]\n"
    "s_cmp_lt_u32 s58, s16\n"
    "v_exp_f32 v242, v242\n"
    "v_mfma_f32_16x16x32_bf16 v[168:171], a[104:107], a[24:27], v[168:171]\n"
    "s_cselect_b32 s56, 0x40000, s56\n"
    "v_cvt_pk_bf16_f32 v202, v236, v238\n"
    "v_fma_f32 v243, v151, s36, v216\n"
    "v_mfma_f32_16x16x32_bf16 v[168:171], a[108:111], a[28:31], v[168:171]\n"
    "s_waitcnt lgkmcnt(5)\n"
    "s_add_u32 s56, s56, s58\n"
    "v_exp_f32 v243, v243\n"
    "v_mfma_f32_16x16x32_bf16 v[172:175], a[112:115], a[16:19], 0\n"
    "buffer_load_dwordx4 v252, s[44:47], s57 offen offset:1024 lds\n"
    "v_mfma_f32_16x16x32_bf16 v[172:175], a[116:119], a[20:23], v[172:175]\n"
    "s_waitcnt lgkmcnt(2)\n"
    "ds_read_b64_tr_b16 a[138:139], v247 offset:4352\n"
    "v_fma_f32 v236, v152, s36, v220\n"
    "v_mfma_f32_16x16x32_bf16 v[172:175], a[120:123], a[24:27], v[172:175]\n"
    "s_waitcnt lgkmcnt(2)\n"
    "v_cvt_pk_bf16_f32 v200, v237, v239\n"
    "v_cvt_pk_bf16_f32 v203, v242, v243\n"
    "v_fma_f32 v238, v153, s36, v220\n"
    "v_mfma_f32_16x16x32_bf16 v[172:175], a[124:127], a[28:31], v[172:175]\n"
    "ds_read_b64_tr_b16 a[140:141], v250 offset:256\n"
    "v_cvt_pk_bf16_f32 v201, v240, v241\n"
    "v_mfma_f32_16x16x32_bf16 v[160:163], a[96:99], a[0:3], 0\n"
    "v_fma_f32 v237, v154, s36, v220\n"
    "v_fma_f32 v239, v155, s36, v220\n"
    "v_mfma_f32_16x16x32_bf16 v[224:227], a[168:171], v[200:203], v[224:227]\n"
    "buffer_load_dwordx4 v251, s[44:47], s57 offen offset:2048 lds\n"
    "v_mfma_f32_16x16x32_bf16 v[160:163], a[100:103], a[4:7], v[160:163]\n"
    "ds_read_b64_tr_b16 a[142:143], v250 offset:4352\n"
    "v_exp_f32 v236, v236\n"
    "v_mfma_f32_16x16x32_bf16 v[160:163], a[104:107], a[8:11], v[160:163]\n"
    "v_exp_f32 v238, v238\n"
    "v_mfma_f32_16x16x32_bf16 v[160:163], a[108:111], a[12:15], v[160:163]\n"
    "v_exp_f32 v237, v237\n"
    "v_mfma_f32_16x16x32_bf16 v[164:167], a[112:115], a[0:3], 0\n"
    "v_fma_f32 v240, v156, s36, v220\n"
    "v_fma_f32 v241, v157, s36, v220\n"
    "v_mfma_f32_16x16x32_bf16 v[164:167], a[116:119], a[4:7], v[164:167]\n"
    "buffer_load_dwordx4 v252, s[44:47], s57 offen offset:3072 lds\n"
    "v_mfma_f32_16x16x32_bf16 v[164:167], a[120:123], a[8:11], v[164:167]\n"
    "s_add_u32 s59, s59, 16384\n"
    "v_exp_f32 v239, v239\n"
    "v_mfma_f32_16x16x32_bf16 v[164:167], a[124:127], a[12:15], v[164:167]\n"
    "s_cmp_lt_u32 s59, s17\n"
    "v_exp_f32 v240, v240\n"
    "v_mfma_f32_16x16x32_bf16 v[176:179], a[96:99], a[64:67], 0\n"
    "s_cselect_b32 s57, s18, 0\n"
    "v_cvt_pk_bf16_f32 v205, v237, v239\n"
    "v_fma_f32 v242, v158, s36, v220\n"
    "v_mfma_f32_16x16x32_bf16 v[176:179], a[100:103], a[68:71], v[176:179]\n"
    "s_cmp_lt_u32 s59, s16\n"
    "ds_read_b64_tr_b16 a[144:145], v247 offset:512\n"
    "ds_read_b64_tr_b16 a[146:147], v247 offset:4608\n"
    "v_mfma_f32_16x16x32_bf16 v[176:179], a[104:107], a[72:75], v[176:179]\n"
    "s_cselect_b32 s57, 0x40000, s57\n"
    "v_exp_f32 v241, v241\n"
    "v_mfma_f32_16x16x32_bf16 v[176:179], a[108:111], a[76:79], v[176:179]\n"
    "s_add_u32 s57, s57, s59\n"
    "v_exp_f32 v242, v242\n"
    "v_fma_f32 v237, v159, s36, v220\n"
    "v_mfma_f32_16x16x32_bf16 v[180:183], a[112:115], a[64:67], 0\n"
    "ds_read_b64_tr_b16 a[148:149], v250 offset:512\n"
    "v_fma_f32 v232, v168, s36, v212\n"
    "v_mfma_f32_16x16x32_bf16 v[180:183], a[116:119], a[68:71], v[180:183]\n"
    "v_exp_f32 v237, v237\n"
    "v_mfma_f32_16x16x32_bf16 v[180:183], a[120:123], a[72:75], v[180:183]\n"
    "v_cvt_pk_bf16_f32 v204, v236, v238\n"
    "v_cvt_pk_bf16_f32 v206, v240, v241\n"
    "v_mfma_f32_16x16x32_bf16 v[180:183], a[124:127], a[76:79], v[180:183]\n"
    "v_cvt_pk_bf16_f32 v207, v242, v237\n"
    "v_fma_f32 v233, v169, s36, v212\n"
    "v_mfma_f32_16x16x32_bf16 v[184:187], a[96:99], a[80:83], 0\n"
    "v_exp_f32 v232, v232\n"
    "v_mfma_f32_16x16x32_bf16 v[224:227], a[172:175], v[204:207], v[224:227]\n"
    "ds_read_b64_tr_b16 a[150:151], v250 offset:4608\n"
    "ds_read_b64_tr_b16 a[152:153], v247 offset:768\n"
    "ds_read_b64_tr_b16 a[154:155], v247 offset:4864\n"
    "v_mfma_f32_16x16x32_bf16 v[184:187], a[100:103], a[84:87], v[184:187]\n"
    "ds_read_b64_tr_b16 a[156:157], v250 offset:768\n"
    "ds_read_b64_tr_b16 a[158:159], v250 offset:4864\n"
    "v_mfma_f32_16x16x32_bf16 v[184:187], a[104:107], a[88:91], v[184:187]\n"
    "v_exp_f32 v233, v233\n"
    "v_mfma_f32_16x16x32_bf16 v[184:187], a[108:111], a[92:95], v[184:187]\n"
    "v_add_u32 v246, s60, v248\n"
    "v_fma_f32 v234, v170, s36, v212\n"
    "v_mfma_f32_16x16x32_bf16 v[188:191], a[112:115], a[80:83], 0\n"
    "v_fma_f32 v235, v171, s36, v212\n"
    "v_max3_f32 v241, v224, v225, v226\n"
    "v_mfma_f32_16x16x32_bf16 v[188:191], a[116:119], a[84:87], v[188:191]\n"
    "s_sub_u32 s62, s62, 1\n"
    "v_max_f32 v241, v241, v227\n"
    "v_cmp_nge_f32 vcc, s37, v241\n"
    "v_mfma_f32_16x16x32_bf16 v[188:191], a[120:123], a[88:91], v[188:191]\n"
    "s_cmp_lg_u32 s62, s14\n"
    "v_exp_f32 v234, v234\n"
    "v_mfma_f32_16x16x32_bf16 v[188:191], a[124:127], a[92:95], v[188:191]\n"
    "s_waitcnt lgkmcnt(0)\n"
    "v_exp_f32 v235, v235\n"
    "s_cbranch_vccnz L_rc_L2b\n"
    "L_rr_L2b:\n"
    "s_cbranch_scc1 L_L2\n"
    "L_T0b:\n"
    "s_mov_b32 s21, 32768\n"
    "s_mov_b32 s15, -1\n"
    "s_mov_b32 s14, -1\n"
    "s_cmp_eq_u32 s39, 7\n"
    "s_cbranch_scc1 L_mo_T0b\n"
    "L_ms_T0b:\n"
    "v_mfma_f32_16x16x32_bf16 v[32:35], a[128:131], v[196:199], v[32:35]\n"
    "v_fma_f32 v236, v172, s36, v212\n"
    "v_fma_f32 v237, v173, s36, v212\n"
    "v_mfma_f32_16x16x32_bf16 v[36:39], a[132:135], v[196:199], v[36:39]\n"
    "v_exp_f32 v236, v236\n"
    "v_mfma_f32_16x16x32_bf16 v[40:43], a[136:139], v[196:199], v[40:43]\n"
    "s_waitcnt vmcnt(16)\n"
    "v_exp_f32 v237, v237\n"
    "v_mfma_f32_16x16x32_bf16 v[44:47], a[140:143], v[196:199], v[44:47]\n"
    "s_barrier\n"
    "v_fma_f32 v238, v174, s36, v212\n"
    "v_fma_f32 v239, v175, s36, v212\n"
    "v_fma_f32 v240, v160, s36, v208\n"
    "v_mfma_f32_16x16x32_bf16 v[48:51], a[144:147], v[196:199], v[48:51]\n"
    "v_exp_f32 v238, v238\n"
    "v_mfma_f32_16x16x32_bf16 v[52:55], a[148:151], v[196:199], v[52:55]\n"
    "ds_read_b128 a[96:99], v246 offset:0\n"
    "v_exp_f32 v239, v239\n"
    "v_mfma_f32_16x16x32_bf16 v[56:59], a[152:155], v[196:199], v[56:59]\n"
    "ds_read_b128 a[100:103], v246 offset:256\n"
    "v_fma_f32 v241, v161, s36, v208\n"
    "v_fma_f32 v242, v162, s36, v208\n"
    "v_mfma_f32_16x16x32_bf16 v[60:63], a[156:159], v[196:199], v[60:63]\n"
    "v_cvt_pk_bf16_f32 v197, v234, v235\n"
    "v_cvt_pk_bf16_f32 v198, v236, v237\n"
    "v_fma_f32 v243, v163, s36, v208\n"
    "v_mfma_f32_16x16x32_bf16 v[0:3], a[128:131], v[192:195], v[0:3]\n"
    "v_exp_f32 v240, v240\n"
    "v_mfma_f32_16x16x32_bf16 v[4:7], a[132:135], v[192:195], v[4:7]\n"
    "v_cvt_pk_bf16_f32 v196, v232, v233\n"
    "v_fma_f32 v236, v164, s36, v208\n"
    "v_mfma_f32_16x16x32_bf16 v[8:11], a[136:139], v[192:195], v[8:11]\n"
    "v_cvt_pk_bf16_f32 v199, v238, v239\n"
    "v_exp_f32 v241, v241\n"
    "v_mfma_f32_16x16x32_bf16 v[12:15], a[140:143], v[192:195], v[12:15]\n"
    "v_exp_f32 v242, v242\n"
    "v_mfma_f32_16x16x32_bf16 v[228:231], a[164:167], v[196:199], v[224:227]\n"
    "v_exp_f32 v243, v243\n"
    "v_mfma_f32_16x16x32_bf16 v[16:19], a[144:147], v[192:195], v[16:19]\n"
    "s_and_b32 m0, s58, 0xffff\n"
    "v_exp_f32 v236, v236\n"
    "v_mfma_f32_16x16x32_bf16 v[20:23], a[148:151], v[192:195], v[20:23]\n"
    "buffer_load_dwordx4 v251, s[40:43], s56 offen offset:0 lds\n"
    "v_mfma_f32_16x16x32_bf16 v[24:27], a[152:155], v[192:195], v[24:27]\n"
    "ds_read_b128 a[104:107], v246 offset:512\n"
    "v_fma_f32 v237, v165, s36, v208\n"
    "v_fma_f32 v238, v166, s36, v208\n"
    "v_mfma_f32_16x16x32_bf16 v[28:31], a[156:159], v[192:195], v[28:31]\n"
    "ds_read_b128 a[108:111], v246 offset:768\n"
    "v_exp_f32 v237, v237\n"
    "v_mfma_f32_16x16x32_bf16 a[176:179], a[128:131], v[200:203], a[176:179]\n"
    "v_cvt_pk_bf16_f32 v194, v236, v237\n"
    "v_fma_f32 v239, v167, s36, v208\n"
    "v_mfma_f32_16x16x32_bf16 a[208:211], a[128:131], v[204:207], a[208:211]\n"
    "buffer_load_dwordx4 v252, s[40:43], s56 offen offset:1024 lds\n"
    "v_mfma_f32_16x16x32_bf16 a[180:183], a[132:135], v[200:203], a[180:183]\n"
    "ds_read_b128 a[112:115], v246 offset:4096\n"
    "v_exp_f32 v238, v238\n"
    "v_mfma_f32_16x16x32_bf16 a[212:215], a[132:135], v[204:207], a[212:215]\n"
    "ds_read_b128 a[116:119], v246 offset:4352\n"
    "ds_read_b128 a[120:123], v246 offset:4608\n"
    "ds_read_b64_tr_b16 a[128:129], v247 offset:16384\n"
    "v_mfma_f32_16x16x32_bf16 a[184:187], a[136:139], v[200:203], a[184:187]\n"
    "v_cvt_pk_bf16_f32 v192, v240, v241\n"
    "v_fma_f32 v236, v176, s36, v216\n"
    "v_mfma_f32_16x16x32_bf16 a[216:219], a[136:139], v[204:207], a[216:219]\n"
    "v_exp_f32 v239, v239\n"
    "v_mfma_f32_16x16x32_bf16 a[188:191], a[140:143], v[200:203], a[188:191]\n"
    "buffer_load_dwordx4 v251, s[40:43], s56 offen offset:2048 lds\n"
    "v_mfma_f32_16x16x32_bf16 a[220:223], a[140:143], v[204:207], a[220:223]\n"
    "v_cvt_pk_bf16_f32 v195, v238, v239\n"
    "v_fma_f32 v237, v177, s36, v216\n"
    "v_fma_f32 v240, v178, s36, v216\n"
    "v_mfma_f32_16x16x32_bf16 a[192:195], a[144:147], v[200:203], a[192:195]\n"
    "s_waitcnt vmcnt(19)\n"
    "ds_read_b128 a[124:127], v246 offset:4864\n"
    "ds_read_b128 a[32:35], v253 offset:0\n"
    "v_cvt_pk_bf16_f32 v193, v242, v243\n"
    "v_mfma_f32_16x16x32_bf16 a[224:227], a[144:147], v[204:207], a[224:227]\n"
    "v_exp_f32 v236, v236\n"
    "v_mfma_f32_16x16x32_bf16 v[228:231], a[160:163], v[192:195], v[228:231]\n"
    "ds_read_b128 a[36:39], v253 offset:256\n"
    "v_exp_f32 v237, v237\n"
    "v_mfma_f32_16x16x32_bf16 a[196:199], a[148:151], v[200:203], a[196:199]\n"
    "buffer_load_dwordx4 v252, s[40:43], s56 offen offset:3072 lds\n"
    "v_mfma_f32_16x16x32_bf16 a[228:231], a[148:151], v[204:207], a[228:231]\n"
    "s_add_u32 s58, s58, 16384\n"
    "v_exp_f32 v240, v240\n"
    "v_mfma_f32_16x16x32_bf16 a[200:203], a[152:155], v[200:203], a[200:203]\n"
    "s_sub_u32 s58, s58, s93\n"
    "ds_read_b64_tr_b16 a[130:131], v247 offset:20480\n"
    "ds_read_b64_tr_b16 a[132:133], v250 offset:16384\n"
    "ds_read_b64_tr_b16 a[134:135], v250 offset:20480\n"
    "v_mfma_f32_16x16x32_bf16 a[232:235], a[152:155], v[204:207], a[232:235]\n"
    "s_and_b32 m0, s59, 0xffff\n"
    "v_fma_f32 v238, v179, s36, v216\n"
    "v_fma_f32 v239, v180, s36, v216\n"
    "v_fma_f32 v241, v181, s36, v216\n"
    "v_mfma_f32_16x16x32_bf16 a[204:207], a[156:159], v[200:203], a[204:207]\n"
    "s_bitset1_b32 m0, 16\n"
    "ds_read_b128 a[40:43], v253 offset:512\n"
    "v_fma_f32 v242, v182, s36, v216\n"
    "v_mfma_f32_16x16x32_bf16 a[236:239], a[156:159], v[204:207], a[236:239]\n"
    "s_waitcnt lgkmcnt(5)\n"
    "s_cmp_lt_u32 s58, s17\n"
    "buffer_load_dwordx4 v251, s[44:47], s57 offen offset:0 lds\n"
    "v_mfma_f32_16x16x32_bf16 v[136:139], a[96:99], a[16:19], 0\n"
    "s_cselect_b32 s56, s18, 0\n"
    "v_cvt_pk_bf16_f32 v200, v236, v237\n"
    "v_exp_f32 v238, v238\n"
    "v_mfma_f32_16x16x32_bf16 v[136:139], a[100:103], a[20:23], v[136:139]\n"
    "s_cmp_lt_u32 s58, s16\n"
    "v_exp_f32 v239, v239\n"
    "v_fma_f32 v236, v183, s36, v216\n"
    "v_mfma_f32_16x16x32_bf16 v[136:139], a[104:107], a[24:27], v[136:139]\n"
    "s_cselect_b32 s56, 0x40000, s56\n"
    "v_cvt_pk_bf16_f32 v201, v240, v238\n"
    "v_exp_f32 v241, v241\n"
    "v_mfma_f32_16x16x32_bf16 v[136:139], a[108:111], a[28:31], v[136:139]\n"
    "s_add_u32 s56, s56, s58\n"
    "v_cvt_pk_bf16_f32 v202, v239, v241\n"
    "v_exp_f32 v242, v242\n"
    "v_mfma_f32_16x16x32_bf16 v[140:143], a[112:115], a[16:19], 0\n"
    "buffer_load_dwordx4 v252, s[44:47], s57 offen offset:1024 lds\n"
    "v_mfma_f32_16x16x32_bf16 v[140:143], a[116:119], a[20:23], v[140:143]\n"
    "v_exp_f32 v236, v236\n"
    "v_mfma_f32_16x16x32_bf16 v[140:143], a[120:123], a[24:27], v[140:143]\n"
    "ds_read_b128 a[44:47], v253 offset:768\n"
    "ds_read_b128 a[48:51], v253 offset:4096\n"
    "v_fma_f32 v237, v184, s36, v220\n"
    "v_mfma_f32_16x16x32_bf16 v[140:143], a[124:127], a[28:31], v[140:143]\n"
    "v_cvt_pk_bf16_f32 v203, v242, v236\n"
    "v_fma_f32 v238, v185, s36, v220\n"
    "v_fma_f32 v239, v186, s36, v220\n"
    "v_mfma_f32_16x16x32_bf16 v[128:131], a[96:99], a[0:3], 0\n"
    "ds_read_b64_tr_b16 a[136:137], v247 offset:16640\n"
    "ds_read_b64_tr_b16 a[138:139], v247 offset:20736\n"
    "v_mfma_f32_16x16x32_bf16 v[228:231], a[168:171], v[200:203], v[228:231]\n"
    "buffer_load_dwordx4 v251, s[44:47], s57 offen offset:2048 lds\n"
    "v_mfma_f32_16x16x32_bf16 v[128:131], a[100:103], a[4:7], v[128:131]\n"
    "ds_read_b128 a[52:55], v253 offset:4352\n"
    "v_exp_f32 v237, v237\n"
    "v_mfma_f32_16x16x32_bf16 v[128:131], a[104:107], a[8:11], v[128:131]\n"
    "ds_read_b64_tr_b16 a[140:141], v250 offset:16640\n"
    "v_fma_f32 v236, v187, s36, v220\n"
    "v_fma_f32 v240, v188, s36, v220\n"
    "v_mfma_f32_16x16x32_bf16 v[128:131], a[108:111], a[12:15], v[128:131]\n"
    "ds_read_b64_tr_b16 a[142:143], v250 offset:20736\n"
    "ds_read_b64_tr_b16 a[144:145], v247 offset:16896\n"
    "v_fma_f32 v241, v189, s36, v220\n"
    "v_mfma_f32_16x16x32_bf16 v[132:135], a[112:115], a[0:3], 0\n"
    "ds_read_b128 a[56:59], v253 offset:4608\n"
    "ds_read_b64_tr_b16 a[146:147], v247 offset:20992\n"
    "v_fma_f32 v242, v190, s36, v220\n"
    "v_mfma_f32_16x16x32_bf16 v[132:135], a[116:119], a[4:7], v[132:135]\n"
    "buffer_load_dwordx4 v252, s[44:47], s57 offen offset:3072 lds\n"
    "v_mfma_f32_16x16x32_bf16 v[132:135], a[120:123], a[8:11], v[132:135]\n"
    "s_add_u32 s59, s59, 16384\n"
    "ds_read_b128 a[60:63], v253 offset:4864\n"
    "v_exp_f32 v238, v238\n"
    "v_mfma_f32_16x16x32_bf16 v[132:135], a[124:127], a[12:15], v[132:135]\n"
    "s_cmp_lt_u32 s59, s17\n"
    "ds_read_b64_tr_b16 a[148:149], v250 offset:16896\n"
    "v_fma_f32 v243, v191, s36, v220\n"
    "v_mfma_f32_16x16x32_bf16 v[144:147], a[96:99], a[32:35], 0\n"
    "s_waitcnt lgkmcnt(7)\n"
    "s_cselect_b32 s57, s18, 0\n"
    "v_cvt_pk_bf16_f32 v204, v237, v238\n"
    "v_exp_f32 v239, v239\n"
    "v_mfma_f32_16x16x32_bf16 v[144:147], a[100:103], a[36:39], v[144:147]\n"
    "s_cmp_lt_u32 s59, s16\n"
    "v_exp_f32 v236, v236\n"
    "v_mfma_f32_16x16x32_bf16 v[144:147], a[104:107], a[40:43], v[144:147]\n"
    "s_cselect_b32 s57, 0x40000, s57\n"
    "v_exp_f32 v240, v240\n"
    "v_mfma_f32_16x16x32_bf16 v[144:147], a[108:111], a[44:47], v[144:147]\n"
    "s_add_u32 s57, s57, s59\n"
    "v_exp_f32 v241, v241\n"
    "v_fma_f32 v232, v136, s36, v212\n"
    "v_mfma_f32_16x16x32_bf16 v[148:151], a[112:115], a[32:35], 0\n"
    "v_cvt_pk_bf16_f32 v205, v239, v236\n"
    "v_exp_f32 v242, v242\n"
    "v_mfma_f32_16x16x32_bf16 v[148:151], a[116:119], a[36:39], v[148:151]\n"
    "v_exp_f32 v243, v243\n"
    "v_mfma_f32_16x16x32_bf16 v[148:151], a[120:123], a[40:43], v[148:151]\n"
    "ds_read_b64_tr_b16 a[150:151], v250 offset:20992\n"
    "v_cvt_pk_bf16_f32 v207, v242, v243\n"
    "v_mfma_f32_16x16x32_bf16 v[148:151], a[124:127], a[44:47], v[148:151]\n"
    "ds_read_b64_tr_b16 a[152:153], v247 offset:17152\n"
    "v_cvt_pk_bf16_f32 v206, v240, v241\n"
    "v_mfma_f32_16x16x32_bf16 v[152:155], a[96:99], a[48:51], 0\n"
    "v_exp_f32 v232, v232\n"
    "v_mfma_f32_16x16x32_bf16 v[228:231], a[172:175], v[204:207], v[228:231]\n"
    "ds_read_b64_tr_b16 a[154:155], v247 offset:21248\n"
    "ds_read_b64_tr_b16 a[156:157], v250 offset:17152\n"
    "v_mfma_f32_16x16x32_bf16 v[152:155], a[100:103], a[52:55], v[152:155]\n"
    "s_waitcnt lgkmcnt(4)\n"
    "ds_read_b64_tr_b16 a[158:159], v250 offset:21248\n"
    "v_fma_f32 v233, v137, s36, v212\n"
    "v_mfma_f32_16x16x32_bf16 v[152:155], a[104:107], a[56:59], v[152:155]\n"
    "v_exp_f32 v233, v233\n"
    "v_mfma_f32_16x16x32_bf16 v[152:155], a[108:111], a[60:63], v[152:155]\n"
    "v_fma_f32 v234, v138, s36, v212\n"
    "v_fma_f32 v235, v139, s36, v212\n"
    "v_mfma_f32_16x16x32_bf16 v[156:159], a[112:115], a[48:51], 0\n"
    "v_max3_f32 v241, v228, v229, v230\n"
    "v_max_f32 v241, v241, v231\n"
    "v_mfma_f32_16x16x32_bf16 v[156:159], a[116:119], a[52:55], v[156:159]\n"
    "v_exp_f32 v234, v234\n"
    "v_mfma_f32_16x16x32_bf16 v[156:159], a[120:123], a[56:59], v[156:159]\n"
    "v_cmp_nge_f32 vcc, s37, v241\n"
    "v_mfma_f32_16x16x32_bf16 v[156:159], a[124:127], a[60:63], v[156:159]\n"
    "s_waitcnt lgkmcnt(0)\n"
    "v_exp_f32 v235, v235\n"
    "s_cbranch_vccnz L_rc_T0b\n"
    "L_rr_T0b:\n"
    "s_cmp_eq_u32 s39, 0\n"
    "s_cbranch_scc1 L_mo_T0c\n"
    "L_ms_T0c:\n"
    "v_mfma_f32_16x16x32_bf16 v[32:35], a[128:131], v[196:199], v[32:35]\n"
    "v_accvgpr_write_b32 a242, v230\n"
    "v_accvgpr_write_b32 a243, v231\n"
    "v_fma_f32 v236, v140, s36, v212\n"
    "v_mfma_f32_16x16x32_bf16 v[36:39], a[132:135], v[196:199], v[36:39]\n"
    "v_exp_f32 v236, v236\n"
    "v_mfma_f32_16x16x32_bf16 v[40:43], a[136:139], v[196:199], v[40:43]\n"
    "s_waitcnt vmcnt(16)\n"
    "v_fma_f32 v237, v141, s36, v212\n"
    "v_fma_f32 v238, v142, s36, v212\n"
    "v_fma_f32 v239, v143, s36, v212\n"
    "v_mfma_f32_16x16x32_bf16 v[44:47], a[140:143], v[196:199], v[44:47]\n"
    "s_barrier\n"
    "ds_read_b128 a[96:99], v246 offset:8192\n"
    "v_exp_f32 v237, v237\n"
    "v_mfma_f32_16x16x32_bf16 v[48:51], a[144:147], v[196:199], v[48:51]\n"
    "s_add_u32 s61, s61, 32768\n"
    "v_max3_f32 v253, v144, v145, v146\n"
    "v_exp_f32 v238, v238\n"
    "v_fma_f32 v240, v128, s36, v208\n"
    "v_mfma_f32_16x16x32_bf16 v[52:55], a[148:151], v[196:199], v[52:55]\n"
    "s_sub_u32 s61, s61, s63\n"
    "v_mov_b32 v230, 0\n"
    "v_exp_f32 v239, v239\n"
    "v_mfma_f32_16x16x32_bf16 v[56:59], a[152:155], v[196:199], v[56:59]\n"
    "s_and_b32 s61, s61, 0xffff\n"
    "v_max3_f32 v242, v152, v153, v154\n"
    "v_max3_f32 v253, v253, v147, v148\n"
    "v_max3_f32 v242, v242, v155, v156\n"
    "v_mfma_f32_16x16x32_bf16 v[60:63], a[156:159], v[196:199], v[60:63]\n"
    "s_add_u32 s60, s60, 16384\n"
    "v_max3_f32 v253, v253, v149, v150\n"
    "v_cvt_pk_bf16_f32 v196, v232, v233\n"
    "v_cvt_pk_bf16_f32 v198, v236, v237\n"
    "v_mfma_f32_16x16x32_bf16 v[0:3], a[128:131], v[192:195], v[0:3]\n"
    "s_and_b32 s60, s60, 0xffff\n"
    "v_max3_f32 v242, v242, v157, v158\n"
    "v_cvt_pk_bf16_f32 v197, v234, v235\n"
    "v_fma_f32 v236, v129, s36, v208\n"
    "v_mfma_f32_16x16x32_bf16 v[4:7], a[132:135], v[192:195], v[4:7]\n"
    "v_max_f32 v253, v253, v151\n"
    "v_cvt_pk_bf16_f32 v199, v238, v239\n"
    "v_fma_f32 v237, v130, s36, v208\n"
    "v_mfma_f32_16x16x32_bf16 v[8:11], a[136:139], v[192:195], v[8:11]\n"
    "ds_read_b128 a[100:103], v246 offset:8448\n"
    "v_mov_b32 v231, 0\n"
    "v_max_f32 v242, v242, v159\n"
    "v_mfma_f32_16x16x32_bf16 v[12:15], a[140:143], v[192:195], v[12:15]\n"
    "v_mov_b32 v254, v253\n"
    "v_exp_f32 v240, v240\n"
    "v_mfma_f32_16x16x32_bf16 v[224:227], a[164:167], v[196:199], v[228:231]\n"
    "v_add_u32 v247, s61, v249\n"
    "v_xor_b32 v250, 32, v247\n"
    "v_fma_f32 v238, v131, s36, v208\n"
    "v_mfma_f32_16x16x32_bf16 v[16:19], a[144:147], v[192:195], v[16:19]\n"
    "s_and_b32 m0, s58, 0xffff\n"
    "v_mov_b32 v243, v242\n"
    "v_exp_f32 v236, v236\n"
    "v_mfma_f32_16x16x32_bf16 v[20:23], a[148:151], v[192:195], v[20:23]\n"
    "buffer_load_dwordx4 v251, s[40:43], s56 offen offset:0 lds\n"
    "v_mfma_f32_16x16x32_bf16 v[24:27], a[152:155], v[192:195], v[24:27]\n"
    "v_permlane16_swap_b32 v253, v254\n"
    "v_permlane16_swap_b32 v242, v243\n"
    "v_fma_f32 v239, v132, s36, v208\n"
    "v_mfma_f32_16x16x32_bf16 v[28:31], a[156:159], v[192:195], v[28:31]\n"
    "ds_read_b128 a[104:107], v246 offset:8704\n"
    "v_max_f32 v253, v253, v254\n"
    "v_cvt_pk_bf16_f32 v192, v240, v236\n"
    "v_mfma_f32_16x16x32_bf16 a[176:179], a[128:131], v[200:203], a[176:179]\n"
    "v_exp_f32 v237, v237\n"
    "v_fma_f32 v236, v133, s36, v208\n"
    "v_mfma_f32_16x16x32_bf16 a[208:211], a[128:131], v[204:207], a[208:211]\n"
    "ds_read_b128 a[108:111], v246 offset:8960\n"
    "v_exp_f32 v238, v238\n"
    "v_mfma_f32_16x16x32_bf16 a[180:183], a[132:135], v[200:203], a[180:183]\n"
    "ds_read_b128 a[112:115], v246 offset:12288\n"
    "v_exp_f32 v239, v239\n"
    "v_mfma_f32_16x16x32_bf16 a[212:215], a[132:135], v[204:207], a[212:215]\n"
    "buffer_load_dwordx4 v252, s[40:43], s56 offen offset:1024 lds\n"
    "v_mfma_f32_16x16x32_bf16 a[184:187], a[136:139], v[200:203], a[184:187]\n"
    "s_add_u32 s58, s58, 8192\n"
    "v_cvt_pk_bf16_f32 v193, v237, v238\n"
    "v_exp_f32 v236, v236\n"
    "v_mfma_f32_16x16x32_bf16 a[216:219], a[136:139], v[204:207], a[216:219]\n"
    "s_cmp_lt_u32 s58, s17\n"
    "v_max_f32 v242, v242, v243\n"
    "v_fma_f32 v237, v134, s36, v208\n"
    "v_fma_f32 v238, v135, s36, v208\n"
    "v_mfma_f32_16x16x32_bf16 a[188:191], a[140:143], v[200:203], a[188:191]\n"
    "s_cselect_b32 s56, s18, 0\n"
    "v_mov_b32 v254, v253\n"
    "v_cvt_pk_bf16_f32 v194, v239, v236\n"
    "v_exp_f32 v237, v237\n"
    "v_mfma_f32_16x16x32_bf16 a[220:223], a[140:143], v[204:207], a[220:223]\n"
    "s_add_u32 s56, s56, s58\n"
    "v_mov_b32 v243, v242\n"
    "v_exp_f32 v238, v238\n"
    "v_mfma_f32_16x16x32_bf16 a[192:195], a[144:147], v[200:203], a[192:195]\n"
    "s_and_b32 m0, s59, 0xffff\n"
    "v_permlane32_swap_b32 v253, v254\n"
    "v_permlane32_swap_b32 v242, v243\n"
    "v_cvt_pk_bf16_f32 v195, v237, v238\n"
    "v_mfma_f32_16x16x32_bf16 a[224:227], a[144:147], v[204:207], a[224:227]\n"
    "s_bitset1_b32 m0, 16\n"
    "v_max_f32 v253, v253, v254\n"
    "v_max_f32 v242, v242, v243\n"
    "v_mul_f32 v253, s36, v253\n"
    "v_mfma_f32_16x16x32_bf16 v[224:227], a[160:163], v[192:195], v[224:227]\n"
    "buffer_load_dwordx4 v251, s[44:47], s57 offen offset:0 lds\n"
    "v_mfma_f32_16x16x32_bf16 a[196:199], a[148:151], v[200:203], a[196:199]\n"
    "v_mul_f32 v242, s36, v242\n"
    "v_sub_f32 v244, 0, v253\n"
    "v_sub_f32 v245, 0, v242\n"
    "v_mfma_f32_16x16x32_bf16 a[228:231], a[148:151], v[204:207], a[228:231]\n"
    "v_mov_b32 v254, s35\n"
    "v_fma_f32 v236, v144, s36, v244\n"
    "v_fma_f32 v237, v145, s36, v244\n"
    "v_mfma_f32_16x16x32_bf16 a[200:203], a[152:155], v[200:203], a[200:203]\n"
    "v_exp_f32 v236, v236\n"
    "v_fma_f32 v238, v146, s36, v244\n"
    "v_mfma_f32_16x16x32_bf16 a[232:235], a[152:155], v[204:207], a[232:235]\n"
    "v_exp_f32 v237, v237\n"
    "v_fma_f32 v239, v147, s36, v244\n"
    "v_mfma_f32_16x16x32_bf16 a[204:207], a[156:159], v[200:203], a[204:207]\n"
    "ds_read_b128 a[116:119], v246 offset:12544\n"
    "v_fma_f32 v240, v148, s36, v244\n"
    "v_fma_f32 v241, v149, s36, v244\n"
    "v_mfma_f32_16x16x32_bf16 a[236:239], a[156:159], v[204:207], a[236:239]\n"
    "s_waitcnt lgkmcnt(1)\n"
    "ds_read_b128 a[120:123], v246 offset:12800\n"
    "ds_read_b128 a[124:127], v246 offset:13056\n"
    "v_mfma_f32_16x16x32_bf16 v[168:171], a[96:99], a[16:19], 0\n"
    "buffer_load_dwordx4 v252, s[44:47], s57 offen offset:1024 lds\n"
    "v_mfma_f32_16x16x32_bf16 v[168:171], a[100:103], a[20:23], v[168:171]\n"
    "ds_read_b64_tr_b16 a[128:129], v247 offset:0\n"
    "v_cvt_pk_bf16_f32 v200, v236, v237\n"
    "v_exp_f32 v238, v238\n"
    "v_mfma_f32_16x16x32_bf16 v[168:171], a[104:107], a[24:27], v[168:171]\n"
    "v_exp_f32 v239, v239\n"
    "v_fma_f32 v236, v150, s36, v244\n"
    "v_mfma_f32_16x16x32_bf16 v[168:171], a[108:111], a[28:31], v[168:171]\n"
    "v_exp_f32 v240, v240\n"
    "v_fma_f32 v237, v151, s36, v244\n"
    "v_mfma_f32_16x16x32_bf16 v[172:175], a[112:115], a[16:19], 0\n"
    "s_waitcnt lgkmcnt(3)\n"
    "v_cvt_pk_bf16_f32 v201, v238, v239\n"
    "v_exp_f32 v241, v241\n"
    "v_mfma_f32_16x16x32_bf16 v[172:175], a[116:119], a[20:23], v[172:175]\n"
    "s_waitcnt lgkmcnt(2)\n"
    "v_exp_f32 v236, v236\n"
    "v_fma_f32 v238, v152, s36, v245\n"
    "v_mfma_f32_16x16x32_bf16 v[172:175], a[120:123], a[24:27], v[172:175]\n"
    "s_waitcnt lgkmcnt(1)\n"
    "v_cvt_pk_bf16_f32 v202, v240, v241\n"
    "v_exp_f32 v237, v237\n"
    "v_mfma_f32_16x16x32_bf16 v[172:175], a[124:127], a[28:31], v[172:175]\n"
    "buffer_load_dwordx4 v251, s[44:47], s57 offen offset:2048 lds\n"
    "v_cvt_pk_bf16_f32 v203, v236, v237\n"
    "v_mfma_f32_16x16x32_bf16 v[160:163], a[96:99], a[0:3], 0\n"
    "v_fma_f32 v236, v153, s36, v245\n"
    "v_fma_f32 v237, v154, s36, v245\n"
    "v_fma_f32 v239, v155, s36, v245\n"
    "v_mfma_f32_16x16x32_bf16 v[224:227], a[168:171], v[200:203], v[224:227]\n"
    "v_exp_f32 v238, v238\n"
    "v_fma_f32 v240, v156, s36, v245\n"
    "v_mfma_f32_16x16x32_bf16 v[160:163], a[100:103], a[4:7], v[160:163]\n"
    "ds_read_b64_tr_b16 a[130:131], v247 offset:4096\n"
    "ds_read_b64_tr_b16 a[132:133], v250 offset:0\n"
    "ds_read_b64_tr_b16 a[134:135], v250 offset:4096\n"
    "v_mfma_f32_16x16x32_bf16 v[160:163], a[104:107], a[8:11], v[160:163]\n"
    "v_exp_f32 v236, v236\n"
    "v_mfma_f32_16x16x32_bf16 v[160:163], a[108:111], a[12:15], v[160:163]\n"
    "v_cvt_pk_bf16_f32 v204, v238, v236\n"
    "v_exp_f32 v237, v237\n"
    "v_mfma_f32_16x16x32_bf16 v[164:167], a[112:115], a[0:3], 0\n"
    "v_exp_f32 v239, v239\n"
    "v_mfma_f32_16x16x32_bf16 v[164:167], a[116:119], a[4:7], v[164:167]\n"
    "buffer_load_dwordx4 v252, s[44:47], s57 offen offset:3072 lds\n"
    "v_mfma_f32_16x16x32_bf16 v[164:167], a[120:123], a[8:11], v[164:167]\n"
    "s_add_u32 s59, s59, 16384\n"
    "v_exp_f32 v240, v240\n"
    "v_mfma_f32_16x16x32_bf16 v[164:167], a[124:127], a[12:15], v[164:167]\n"
    "s_sub_u32 s59, s59, s93\n"
    "v_fma_f32 v236, v157, s36, v245\n"
    "v_fma_f32 v238, v158, s36, v245\n"
    "v_mfma_f32_16x16x32_bf16 v[176:179], a[96:99], a[32:35], 0\n"
    "s_cmp_lt_u32 s59, s17\n"
    "ds_read_b64_tr_b16 a[136:137], v247 offset:256\n"
    "ds_read_b64_tr_b16 a[138:139], v247 offset:4352\n"
    "v_fma_f32 v241, v159, s36, v245\n"
    "v_mfma_f32_16x16x32_bf16 v[176:179], a[100:103], a[36:39], v[176:179]\n"
    "s_cselect_b32 s57, s18, 0\n"
    "v_exp_f32 v236, v236\n"
    "v_fma_f32 v232, v168, s36, v212\n"
    "v_mfma_f32_16x16x32_bf16 v[176:179], a[104:107], a[40:43], v[176:179]\n"
    "s_add_u32 s57, s57, s59\n"
    "ds_read_b64_tr_b16 a[140:141], v250 offset:256\n"
    "v_cvt_pk_bf16_f32 v206, v240, v236\n"
    "v_mfma_f32_16x16x32_bf16 v[176:179], a[108:111], a[44:47], v[176:179]\n"
    "ds_read_b64_tr_b16 a[142:143], v250 offset:4352\n"
    "ds_read_b64_tr_b16 a[144:145], v247 offset:512\n"
    "v_mfma_f32_16x16x32_bf16 v[180:183], a[112:115], a[32:35], 0\n"
    "v_exp_f32 v238, v238\n"
    "v_mfma_f32_16x16x32_bf16 v[180:183], a[116:119], a[36:39], v[180:183]\n"
    "ds_read_b64_tr_b16 a[146:147], v247 offset:4608\n"
    "v_exp_f32 v241, v241\n"
    "v_mfma_f32_16x16x32_bf16 v[180:183], a[120:123], a[40:43], v[180:183]\n"
    "v_add_u32 v246, s60, v248\n"
    "v_cvt_pk_bf16_f32 v205, v237, v239\n"
    "v_mfma_f32_16x16x32_bf16 v[180:183], a[124:127], a[44:47], v[180:183]\n"
    "ds_read_b64_tr_b16 a[148:149], v250 offset:512\n"
    "v_cvt_pk_bf16_f32 v207, v238, v241\n"
    "v_mfma_f32_16x16x32_bf16 v[184:187], a[96:99], a[48:51], 0\n"
    "ds_read_b64_tr_b16 a[150:151], v250 offset:4608\n"
    "v_fma_f32 v233, v169, s36, v212\n"
    "v_mfma_f32_16x16x32_bf16 v[224:227], a[172:175], v[204:207], v[224:227]\n"
    "ds_read_b64_tr_b16 a[152:153], v247 offset:768\n"
    "ds_read_b64_tr_b16 a[154:155], v247 offset:4864\n"
    "v_mfma_f32_16x16x32_bf16 v[184:187], a[100:103], a[52:55], v[184:187]\n"
    "ds_read_b64_tr_b16 a[156:157], v250 offset:768\n"
    "ds_read_b64_tr_b16 a[158:159], v250 offset:4864\n"
    "v_mfma_f32_16x16x32_bf16 v[184:187], a[104:107], a[56:59], v[184:187]\n"
    "v_exp_f32 v232, v232\n"
    "v_fma_f32 v234, v170, s36, v212\n"
    "v_mfma_f32_16x16x32_bf16 v[184:187], a[108:111], a[60:63], v[184:187]\n"
    "v_exp_f32 v233, v233\n"
    "v_mfma_f32_16x16x32_bf16 v[188:191], a[112:115], a[48:51], 0\n"
    "v_max3_f32 v253, v224, v225, v226\n"
    "v_max_f32 v253, v253, v227\n"
    "v_mfma_f32_16x16x32_bf16 v[188:191], a[116:119], a[52:55], v[188:191]\n"
    "s_lshl_b32 s62, s39, 1\n"
    "v_fma_f32 v235, v171, s36, v212\n"
    "v_cmp_nge_f32 vcc, s37, v253\n"
    "v_mfma_f32_16x16x32_bf16 v[188:191], a[120:123], a[56:59], v[188:191]\n"
    "s_cmp_eq_u32 s62, 0\n"
    "v_exp_f32 v234, v234\n"
    "v_mfma_f32_16x16x32_bf16 v[188:191], a[124:127], a[60:63], v[188:191]\n"
    "s_waitcnt lgkmcnt(0)\n"
    "v_exp_f32 v235, v235\n"
    "s_cbranch_vccnz L_rc_T0c\n"
    "L_rr_T0c:\n"
    "s_cbranch_scc1 L_T1b\n"
    ".p2align 6\n"
    "L_L1:\n"
    "v_mfma_f32_16x16x32_bf16 v[32:35], a[128:131], v[196:199], v[32:35]\n"
    "v_fma_f32 v236, v172, s36, v212\n"
    "v_fma_f32 v237, v173, s36, v212\n"
    "v_mfma_f32_16x16x32_bf16 v[36:39], a[132:135], v[196:199], v[36:39]\n"
    "v_fma_f32 v238, v174, s36, v212\n"
    "v_fma_f32 v239, v175, s36, v212\n"
    "v_mfma_f32_16x16x32_bf16 v[40:43], a[136:139], v[196:199], v[40:43]\n"
    "s_waitcnt vmcnt(22)\n"
    "v_exp_f32 v236, v236\n"
    "v_fma_f32 v240, v160, s36, v208\n"
    "v_mfma_f32_16x16x32_bf16 v[44:47], a[140:143], v[196:199], v[44:47]\n"
    "s_barrier\n"
    "v_exp_f32 v237, v237\n"
    "v_mfma_f32_16x16x32_bf16 v[48:51], a[144:147], v[196:199], v[48:51]\n"
    "v_exp_f32 v238, v238\n"
    "v_mfma_f32_16x16x32_bf16 v[52:55], a[148:151], v[196:199], v[52:55]\n"
    "v_exp_f32 v239, v239\n"
    "v_fma_f32 v241, v161, s36, v208\n"
    "v_mfma_f32_16x16x32_bf16 v[56:59], a[152:155], v[196:199], v[56:59]\n"
    "ds_read_b128 a[96:99], v246 offset:0\n"
    "v_exp_f32 v240, v240\n"
    "v_mfma_f32_16x16x32_bf16 v[60:63], a[156:159], v[196:199], v[60:63]\n"
    "v_cvt_pk_bf16_f32 v196, v232, v233\n"
    "v_cvt_pk_bf16_f32 v199, v238, v239\n"
    "v_mfma_f32_16x16x32_bf16 v[0:3], a[128:131], v[192:195], v[0:3]\n"
    "v_cvt_pk_bf16_f32 v198, v236, v237\n"
    "v_fma_f32 v238, v162, s36, v208\n"
    "v_mfma_f32_16x16x32_bf16 v[4:7], a[132:135], v[192:195], v[4:7]\n"
    "v_cvt_pk_bf16_f32 v197, v234, v235\n"
    "v_fma_f32 v236, v163, s36, v208\n"
    "v_fma_f32 v237, v164, s36, v208\n"
    "v_mfma_f32_16x16x32_bf16 v[8:11], a[136:139], v[192:195], v[8:11]\n"
    "ds_read_b128 a[100:103], v246 offset:256\n"
    "v_exp_f32 v241, v241\n"
    "v_mfma_f32_16x16x32_bf16 v[12:15], a[140:143], v[192:195], v[12:15]\n"
    "ds_read_b128 a[104:107], v246 offset:512\n"
    "ds_read_b128 a[108:111], v246 offset:768\n"
    "v_mfma_f32_16x16x32_bf16 v[228:231], a[164:167], v[196:199], v[224:227]\n"
    "ds_read_b128 a[112:115], v246 offset:4096\n"
    "v_exp_f32 v238, v238\n"
    "v_mfma_f32_16x16x32_bf16 v[16:19], a[144:147], v[192:195], v[16:19]\n"
    "s_and_b32 m0, s58, 0xffff\n"
    "v_exp_f32 v236, v236\n"
    "v_mfma_f32_16x16x32_bf16 v[20:23], a[148:151], v[192:195], v[20:23]\n"
    "buffer_load_dwordx4 v251, s[40:43], s56 offen offset:0 lds\n"
    "v_mfma_f32_16x16x32_bf16 v[24:27], a[152:155], v[192:195], v[24:27]\n"
    "v_fma_f32 v239, v165, s36, v208\n"
    "v_fma_f32 v242, v166, s36, v208\n"
    "v_mfma_f32_16x16x32_bf16 v[28:31], a[156:159], v[192:195], v[28:31]\n"
    "ds_read_b128 a[116:119], v246 offset:4352\n"
    "v_cvt_pk_bf16_f32 v192, v240, v241\n"
    "v_mfma_f32_16x16x32_bf16 v[64:67], a[128:131], v[200:203], v[64:67]\n"
    "v_exp_f32 v237, v237\n"
    "v_mfma_f32_16x16x32_bf16 v[96:99], a[128:131], v[204:207], v[96:99]\n"
    "v_exp_f32 v239, v239\n"
    "v_mfma_f32_16x16x32_bf16 v[68:71], a[132:135], v[200:203], v[68:71]\n"
    "buffer_load_dwordx4 v252, s[40:43], s56 offen offset:1024 lds\n"
    "v_mfma_f32_16x16x32_bf16 v[100:103], a[132:135], v[204:207], v[100:103]\n"
    "s_add_u32 s58, s58, 8192\n"
    "v_cvt_pk_bf16_f32 v193, v238, v236\n"
    "v_fma_f32 v240, v167, s36, v208\n"
    "v_mfma_f32_16x16x32_bf16 v[72:75], a[136:139], v[200:203], v[72:75]\n"
    "v_exp_f32 v242, v242\n"
    "v_fma_f32 v236, v176, s36, v244\n"
    "v_mfma_f32_16x16x32_bf16 v[104:107], a[136:139], v[204:207], v[104:107]\n"
    "s_and_b32 m0, s59, 0xffff\n"
    "ds_read_b128 a[120:123], v246 offset:4608\n"
    "v_fma_f32 v238, v177, s36, v244\n"
    "v_mfma_f32_16x16x32_bf16 v[76:79], a[140:143], v[200:203], v[76:79]\n"
    "s_bitset1_b32 m0, 16\n"
    "v_cvt_pk_bf16_f32 v194, v237, v239\n"
    "v_exp_f32 v240, v240\n"
    "v_mfma_f32_16x16x32_bf16 v[108:111], a[140:143], v[204:207], v[108:111]\n"
    "s_cmp_lt_u32 s58, s17\n"
    "buffer_load_dwordx4 v251, s[44:47], s57 offen offset:0 lds\n"
    "v_mfma_f32_16x16x32_bf16 v[80:83], a[144:147], v[200:203], v[80:83]\n"
    "s_cselect_b32 s56, s18, 0\n"
    "v_cvt_pk_bf16_f32 v195, v242, v240\n"
    "v_fma_f32 v237, v178, s36, v244\n"
    "v_fma_f32 v239, v179, s36, v244\n"
    "v_mfma_f32_16x16x32_bf16 v[112:115], a[144:147], v[204:207], v[112:115]\n"
    "s_add_u32 s56, s56, s58\n"
    "v_exp_f32 v236, v236\n"
    "v_mfma_f32_16x16x32_bf16 v[228:231], a[160:163], v[192:195], v[228:231]\n"
    "v_exp_f32 v238, v238\n"
    "v_fma_f32 v240, v180, s36, v244\n"
    "v_mfma_f32_16x16x32_bf16 v[84:87], a[148:151], v[200:203], v[84:87]\n"
    "v_exp_f32 v237, v237\n"
    "v_mfma_f32_16x16x32_bf16 v[116:119], a[148:151], v[204:207], v[116:119]\n"
    "v_exp_f32 v239, v239\n"
    "v_fma_f32 v241, v181, s36, v244\n"
    "v_mfma_f32_16x16x32_bf16 v[88:91], a[152:155], v[200:203], v[88:91]\n"
    "buffer_load_dwordx4 v252, s[44:47], s57 offen offset:1024 lds\n"
    "v_mfma_f32_16x16x32_bf16 v[120:123], a[152:155], v[204:207], v[120:123]\n"
    "s_add_u32 s59, s59, 8192\n"
    "v_exp_f32 v240, v240\n"
    "v_mfma_f32_16x16x32_bf16 v[92:95], a[156:159], v[200:203], v[92:95]\n"
    "s_cmp_lt_u32 s59, s17\n"
    "ds_read_b64_tr_b16 a[128:129], v247 offset:8192\n"
    "v_fma_f32 v242, v182, s36, v244\n"
    "v_mfma_f32_16x16x32_bf16 v[124:127], a[156:159], v[204:207], v[124:127]\n"
    "s_waitcnt lgkmcnt(1)\n"
    "s_cselect_b32 s57, s18, 0\n"
    "v_cvt_pk_bf16_f32 v200, v236, v238\n"
    "v_fma_f32 v243, v183, s36, v244\n"
    "v_mfma_f32_16x16x32_bf16 v[136:139], a[96:99], a[16:19], 0\n"
    "s_add_u32 s57, s57, s59\n"
    "ds_read_b128 a[124:127], v246 offset:4864\n"
    "v_exp_f32 v241, v241\n"
    "v_mfma_f32_16x16x32_bf16 v[136:139], a[100:103], a[20:23], v[136:139]\n"
    "v_exp_f32 v242, v242\n"
    "v_mfma_f32_16x16x32_bf16 v[136:139], a[104:107], a[24:27], v[136:139]\n"
    "v_cvt_pk_bf16_f32 v202, v240, v241\n"
    "v_fma_f32 v236, v184, s36, v245\n"
    "v_mfma_f32_16x16x32_bf16 v[136:139], a[108:111], a[28:31], v[136:139]\n"
    "v_exp_f32 v243, v243\n"
    "v_mfma_f32_16x16x32_bf16 v[140:143], a[112:115], a[16:19], 0\n"
    "v_cvt_pk_bf16_f32 v201, v237, v239\n"
    "v_fma_f32 v238, v185, s36, v245\n"
    "v_mfma_f32_16x16x32_bf16 v[140:143], a[116:119], a[20:23], v[140:143]\n"
    "ds_read_b64_tr_b16 a[130:131], v247 offset:12288\n"
    "v_cvt_pk_bf16_f32 v203, v242, v243\n"
    "v_mfma_f32_16x16x32_bf16 v[140:143], a[120:123], a[24:27], v[140:143]\n"
    "s_waitcnt lgkmcnt(1)\n"
    "v_exp_f32 v236, v236\n"
    "v_mfma_f32_16x16x32_bf16 v[140:143], a[124:127], a[28:31], v[140:143]\n"
    "v_exp_f32 v238, v238\n"
    "v_mfma_f32_16x16x32_bf16 v[128:131], a[96:99], a[0:3], 0\n"
    "v_fma_f32 v237, v186, s36, v245\n"
    "v_fma_f32 v239, v187, s36, v245\n"
    "v_mfma_f32_16x16x32_bf16 v[228:231], a[168:171], v[200:203], v[228:231]\n"
    "v_exp_f32 v237, v237\n"
    "v_mfma_f32_16x16x32_bf16 v[128:131], a[100:103], a[4:7], v[128:131]\n"
    "v_exp_f32 v239, v239\n"
    "v_mfma_f32_16x16x32_bf16 v[128:131], a[104:107], a[8:11], v[128:131]\n"
    "ds_read_b64_tr_b16 a[132:133], v250 offset:8192\n"
    "ds_read_b64_tr_b16 a[134:135], v250 offset:12288\n"
    "v_mfma_f32_16x16x32_bf16 v[128:131], a[108:111], a[12:15], v[128:131]\n"
    "v_cvt_pk_bf16_f32 v205, v237, v239\n"
    "v_fma_f32 v240, v188, s36, v245\n"
    "v_mfma_f32_16x16x32_bf16 v[132:135], a[112:115], a[0:3], 0\n"
    "v_exp_f32 v240, v240\n"
    "v_mfma_f32_16x16x32_bf16 v[132:135], a[116:119], a[4:7], v[132:135]\n"
    "ds_read_b64_tr_b16 a[136:137], v247 offset:8448\n"
    "ds_read_b64_tr_b16 a[138:139], v247 offset:12544\n"
    "v_mfma_f32_16x16x32_bf16 v[132:135], a[120:123], a[8:11], v[132:135]\n"
    "ds_read_b64_tr_b16 a[140:141], v250 offset:8448\n"
    "ds_read_b64_tr_b16 a[142:143], v250 offset:12544\n"
    "v_mfma_f32_16x16x32_bf16 v[132:135], a[124:127], a[12:15], v[132:135]\n"
    "v_fma_f32 v237, v189, s36, v245\n"
    "v_fma_f32 v239, v190, s36, v245\n"
    "v_mfma_f32_16x16x32_bf16 v[144:147], a[96:99], a[32:35], 0\n"
    "v_fma_f32 v241, v191, s36, v245\n"
    "v_fma_f32 v232, v136, s36, v212\n"
    "v_mfma_f32_16x16x32_bf16 v[144:147], a[100:103], a[36:39], v[144:147]\n"
    "v_cvt_pk_bf16_f32 v204, v236, v238\n"
    "v_fma_f32 v233, v137, s36, v212\n"
    "v_mfma_f32_16x16x32_bf16 v[144:147], a[104:107], a[40:43], v[144:147]\n"
    "ds_read_b64_tr_b16 a[144:145], v247 offset:8704\n"
    "ds_read_b64_tr_b16 a[146:147], v247 offset:12800\n"
    "v_mfma_f32_16x16x32_bf16 v[144:147], a[108:111], a[44:47], v[144:147]\n"
    "ds_read_b64_tr_b16 a[148:149], v250 offset:8704\n"
    "ds_read_b64_tr_b16 a[150:151], v250 offset:12800\n"
    "v_mfma_f32_16x16x32_bf16 v[148:151], a[112:115], a[32:35], 0\n"
    "v_exp_f32 v237, v237\n"
    "v_mfma_f32_16x16x32_bf16 v[148:151], a[116:119], a[36:39], v[148:151]\n"
    "v_exp_f32 v239, v239\n"
    "v_mfma_f32_16x16x32_bf16 v[148:151], a[120:123], a[40:43], v[148:151]\n"
    "v_exp_f32 v241, v241\n"
    "v_mfma_f32_16x16x32_bf16 v[148:151], a[124:127], a[44:47], v[148:151]\n"
    "v_cvt_pk_bf16_f32 v206, v240, v237\n"
    "v_cvt_pk_bf16_f32 v207, v239, v241\n"
    "v_mfma_f32_16x16x32_bf16 v[152:155], a[96:99], a[48:51], 0\n"
    "ds_read_b64_tr_b16 a[152:153], v247 offset:8960\n"
    "ds_read_b64_tr_b16 a[154:155], v247 offset:13056\n"
    "v_mfma_f32_16x16x32_bf16 v[228:231], a[172:175], v[204:207], v[228:231]\n"
    "ds_read_b64_tr_b16 a[156:157], v250 offset:8960\n"
    "v_fma_f32 v234, v138, s36, v212\n"
    "v_mfma_f32_16x16x32_bf16 v[152:155], a[100:103], a[52:55], v[152:155]\n"
    "ds_read_b64_tr_b16 a[158:159], v250 offset:13056\n"
    "v_fma_f32 v235, v139, s36, v212\n"
    "v_mfma_f32_16x16x32_bf16 v[152:155], a[104:107], a[56:59], v[152:155]\n"
    "v_exp_f32 v232, v232\n"
    "v_mfma_f32_16x16x32_bf16 v[152:155], a[108:111], a[60:63], v[152:155]\n"
    "v_exp_f32 v233, v233\n"
    "v_mfma_f32_16x16x32_bf16 v[156:159], a[112:115], a[48:51], 0\n"
    "v_exp_f32 v234, v234\n"
    "v_mfma_f32_16x16x32_bf16 v[156:159], a[116:119], a[52:55], v[156:159]\n"
    "v_max3_f32 v241, v228, v229, v230\n"
    "v_max_f32 v241, v241, v231\n"
    "v_mfma_f32_16x16x32_bf16 v[156:159], a[120:123], a[56:59], v[156:159]\n"
    "v_cmp_nge_f32 vcc, s37, v241\n"
    "v_mfma_f32_16x16x32_bf16 v[156:159], a[124:127], a[60:63], v[156:159]\n"
    "s_waitcnt lgkmcnt(0)\n"
    "v_exp_f32 v235, v235\n"
    "s_cbranch_vccnz L_rc_L1b\n"
    "L_rr_L1b:\n"
    "v_mfma_f32_16x16x32_bf16 v[32:35], a[128:131], v[196:199], v[32:35]\n"
    "s_cmp_eq_u32 s62, 1\n"
    "v_fma_f32 v236, v140, s36, v212\n"
    "v_fma_f32 v237, v141, s36, v212\n"
    "v_mfma_f32_16x16x32_bf16 v[36:39], a[132:135], v[196:199], v[36:39]\n"
    "s_cbranch_scc1 L_mo_L1a\n"
    "L_ms_L1a:\n"
    "v_fma_f32 v238, v142, s36, v212\n"
    "v_fma_f32 v239, v143, s36, v212\n"
    "v_mfma_f32_16x16x32_bf16 v[40:43], a[136:139], v[196:199], v[40:43]\n"
    "s_waitcnt vmcnt(18)\n"
    "v_fma_f32 v240, v128, s36, v208\n"
    "v_fma_f32 v241, v129, s36, v208\n"
    "v_mfma_f32_16x16x32_bf16 v[44:47], a[140:143], v[196:199], v[44:47]\n"
    "s_barrier\n"
    "v_exp_f32 v236, v236\n"
    "v_mfma_f32_16x16x32_bf16 v[48:51], a[144:147], v[196:199], v[48:51]\n"
    "s_add_u32 s61, s61, 16384\n"
    "ds_read_b128 a[96:99], v246 offset:8192\n"
    "v_exp_f32 v237, v237\n"
    "v_mfma_f32_16x16x32_bf16 v[52:55], a[148:151], v[196:199], v[52:55]\n"
    "s_and_b32 s61, s61, 0xffff\n"
    "v_exp_f32 v238, v238\n"
    "v_mfma_f32_16x16x32_bf16 v[56:59], a[152:155], v[196:199], v[56:59]\n"
    "s_add_u32 s60, s60, 16384\n"
    "v_exp_f32 v239, v239\n"
    "v_mfma_f32_16x16x32_bf16 v[60:63], a[156:159], v[196:199], v[60:63]\n"
    "s_and_b32 s60, s60, 0xffff\n"
    "v_cvt_pk_bf16_f32 v196, v232, v233\n"
    "v_cvt_pk_bf16_f32 v199, v238, v239\n"
    "v_mfma_f32_16x16x32_bf16 v[0:3], a[128:131], v[192:195], v[0:3]\n"
    "v_cvt_pk_bf16_f32 v197, v234, v235\n"
    "v_cvt_pk_bf16_f32 v198, v236, v237\n"
    "v_mfma_f32_16x16x32_bf16 v[4:7], a[132:135], v[192:195], v[4:7]\n"
    "ds_read_b128 a[100:103], v246 offset:8448\n"
    "ds_read_b128 a[104:107], v246 offset:8704\n"
    "v_mfma_f32_16x16x32_bf16 v[8:11], a[136:139], v[192:195], v[8:11]\n"
    "v_fma_f32 v236, v130, s36, v208\n"
    "v_fma_f32 v237, v131, s36, v208\n"
    "v_mfma_f32_16x16x32_bf16 v[12:15], a[140:143], v[192:195], v[12:15]\n"
    "v_exp_f32 v240, v240\n"
    "v_mfma_f32_16x16x32_bf16 v[224:227], a[164:167], v[196:199], v[228:231]\n"
    "v_exp_f32 v241, v241\n"
    "v_fma_f32 v238, v132, s36, v208\n"
    "v_mfma_f32_16x16x32_bf16 v[16:19], a[144:147], v[192:195], v[16:19]\n"
    "s_and_b32 m0, s58, 0xffff\n"
    "ds_read_b128 a[108:111], v246 offset:8960\n"
    "v_fma_f32 v239, v133, s36, v208\n"
    "v_mfma_f32_16x16x32_bf16 v[20:23], a[148:151], v[192:195], v[20:23]\n"
    "buffer_load_dwordx4 v251, s[40:43], s56 offen offset:0 lds\n"
    "v_mfma_f32_16x16x32_bf16 v[24:27], a[152:155], v[192:195], v[24:27]\n"
    "v_exp_f32 v236, v236\n"
    "v_fma_f32 v242, v134, s36, v208\n"
    "v_mfma_f32_16x16x32_bf16 v[28:31], a[156:159], v[192:195], v[28:31]\n"
    "v_cvt_pk_bf16_f32 v192, v240, v241\n"
    "v_fma_f32 v243, v135, s36, v208\n"
    "v_mfma_f32_16x16x32_bf16 v[64:67], a[128:131], v[200:203], v[64:67]\n"
    "ds_read_b128 a[112:115], v246 offset:12288\n"
    "v_exp_f32 v237, v237\n"
    "v_mfma_f32_16x16x32_bf16 v[96:99], a[128:131], v[204:207], v[96:99]\n"
    "ds_read_b128 a[116:119], v246 offset:12544\n"
    "v_exp_f32 v238, v238\n"
    "v_mfma_f32_16x16x32_bf16 v[68:71], a[132:135], v[200:203], v[68:71]\n"
    "buffer_load_dwordx4 v252, s[40:43], s56 offen offset:1024 lds\n"
    "v_mfma_f32_16x16x32_bf16 v[100:103], a[132:135], v[204:207], v[100:103]\n"
    "s_add_u32 s58, s58, 8192\n"
    "v_exp_f32 v239, v239\n"
    "v_mfma_f32_16x16x32_bf16 v[72:75], a[136:139], v[200:203], v[72:75]\n"
    "v_cvt_pk_bf16_f32 v194, v238, v239\n"
    "v_exp_f32 v242, v242\n"
    "v_mfma_f32_16x16x32_bf16 v[104:107], a[136:139], v[204:207], v[104:107]\n"
    "s_and_b32 m0, s59, 0xffff\n"
    "v_exp_f32 v243, v243\n"
    "v_fma_f32 v238, v144, s36, v244\n"
    "v_mfma_f32_16x16x32_bf16 v[76:79], a[140:143], v[200:203], v[76:79]\n"
    "s_bitset1_b32 m0, 16\n"
    "v_exp_f32 v238, v238\n"
    "v_fma_f32 v239, v145, s36, v244\n"
    "v_mfma_f32_16x16x32_bf16 v[108:111], a[140:143], v[204:207], v[108:111]\n"
    "s_cmp_lt_u32 s58, s17\n"
    "buffer_load_dwordx4 v251, s[44:47], s57 offen offset:0 lds\n"
    "v_mfma_f32_16x16x32_bf16 v[80:83], a[144:147], v[200:203], v[80:83]\n"
    "s_cselect_b32 s56, s18, 0\n"
    "v_cvt_pk_bf16_f32 v193, v236, v237\n"
    "v_cvt_pk_bf16_f32 v195, v242, v243\n"
    "v_fma_f32 v240, v146, s36, v244\n"
    "v_mfma_f32_16x16x32_bf16 v[112:115], a[144:147], v[204:207], v[112:115]\n"
    "s_add_u32 s56, s56, s58\n"
    "v_add_u32 v247, s61, v249\n"
    "v_xor_b32 v250, 32, v247\n"
    "v_mfma_f32_16x16x32_bf16 v[224:227], a[160:163], v[192:195], v[224:227]\n"
    "v_exp_f32 v239, v239\n"
    "v_mfma_f32_16x16x32_bf16 v[84:87], a[148:151], v[200:203], v[84:87]\n"
    "ds_read_b128 a[120:123], v246 offset:12800\n"
    "v_fma_f32 v236, v147, s36, v244\n"
    "v_mfma_f32_16x16x32_bf16 v[116:119], a[148:151], v[204:207], v[116:119]\n"
    "v_exp_f32 v240, v240\n"
    "v_mfma_f32_16x16x32_bf16 v[88:91], a[152:155], v[200:203], v[88:91]\n"
    "buffer_load_dwordx4 v252, s[44:47], s57 offen offset:1024 lds\n"
    "v_mfma_f32_16x16x32_bf16 v[120:123], a[152:155], v[204:207], v[120:123]\n"
    "s_add_u32 s59, s59, 8192\n"
    "ds_read_b128 a[124:127], v246 offset:13056\n"
    "ds_read_b64_tr_b16 a[128:129], v247 offset:0\n"
    "v_fma_f32 v237, v148, s36, v244\n"
    "v_mfma_f32_16x16x32_bf16 v[92:95], a[156:159], v[200:203], v[92:95]\n"
    "s_cmp_lt_u32 s59, s17\n"
    "v_exp_f32 v236, v236\n"
    "v_mfma_f32_16x16x32_bf16 v[124:127], a[156:159], v[204:207], v[124:127]\n"
    "s_waitcnt lgkmcnt(3)\n"
    "s_cselect_b32 s57, s18, 0\n"
    "v_cvt_pk_bf16_f32 v200, v238, v239\n"
    "v_exp_f32 v237, v237\n"
    "v_mfma_f32_16x16x32_bf16 v[168:171], a[96:99], a[16:19], 0\n"
    "s_add_u32 s57, s57, s59\n"
    "v_fma_f32 v238, v149, s36, v244\n"
    "v_fma_f32 v239, v150, s36, v244\n"
    "v_mfma_f32_16x16x32_bf16 v[168:171], a[100:103], a[20:23], v[168:171]\n"
    "ds_read_b64_tr_b16 a[130:131], v247 offset:4096\n"
    "ds_read_b64_tr_b16 a[132:133], v250 offset:0\n"
    "v_fma_f32 v241, v151, s36, v244\n"
    "v_mfma_f32_16x16x32_bf16 v[168:171], a[104:107], a[24:27], v[168:171]\n"
    "v_cvt_pk_bf16_f32 v201, v240, v236\n"
    "v_fma_f32 v242, v152, s36, v245\n"
    "v_mfma_f32_16x16x32_bf16 v[168:171], a[108:111], a[28:31], v[168:171]\n"
    "v_exp_f32 v238, v238\n"
    "v_mfma_f32_16x16x32_bf16 v[172:175], a[112:115], a[16:19], 0\n"
    "v_exp_f32 v239, v239\n"
    "v_mfma_f32_16x16x32_bf16 v[172:175], a[116:119], a[20:23], v[172:175]\n"
    "s_waitcnt lgkmcnt(2)\n"
    "ds_read_b64_tr_b16 a[134:135], v250 offset:4096\n"
    "ds_read_b64_tr_b16 a[136:137], v247 offset:256\n"
    "v_mfma_f32_16x16x32_bf16 v[172:175], a[120:123], a[24:27], v[172:175]\n"
    "v_exp_f32 v241, v241\n"
    "v_mfma_f32_16x16x32_bf16 v[172:175], a[124:127], a[28:31], v[172:175]\n"
    "v_cvt_pk_bf16_f32 v202, v237, v238\n"
    "v_cvt_pk_bf16_f32 v203, v239, v241\n"
    "v_mfma_f32_16x16x32_bf16 v[160:163], a[96:99], a[0:3], 0\n"
    "v_exp_f32 v242, v242\n"
    "v_mfma_f32_16x16x32_bf16 v[224:227], a[168:171], v[200:203], v[224:227]\n"
    "ds_read_b64_tr_b16 a[138:139], v247 offset:4352\n"
    "v_fma_f32 v236, v153, s36, v245\n"
    "v_mfma_f32_16x16x32_bf16 v[160:163], a[100:103], a[4:7], v[160:163]\n"
    "v_exp_f32 v236, v236\n"
    "v_mfma_f32_16x16x32_bf16 v[160:163], a[104:107], a[8:11], v[160:163]\n"
    "v_fma_f32 v237, v154, s36, v245\n"
    "v_fma_f32 v238, v155, s36, v245\n"
    "v_mfma_f32_16x16x32_bf16 v[160:163], a[108:111], a[12:15], v[160:163]\n"
    "ds_read_b64_tr_b16 a[140:141], v250 offset:256\n"
    "v_exp_f32 v237, v237\n"
    "v_mfma_f32_16x16x32_bf16 v[164:167], a[112:115], a[0:3], 0\n"
    "ds_read_b64_tr_b16 a[142:143], v250 offset:4352\n"
    "ds_read_b64_tr_b16 a[144:145], v247 offset:512\n"
    "v_fma_f32 v239, v156, s36, v245\n"
    "v_mfma_f32_16x16x32_bf16 v[164:167], a[116:119], a[4:7], v[164:167]\n"
    "v_exp_f32 v238, v238\n"
    "v_mfma_f32_16x16x32_bf16 v[164:167], a[120:123], a[8:11], v[164:167]\n"
    "ds_read_b64_tr_b16 a[146:147], v247 offset:4608\n"
    "ds_read_b64_tr_b16 a[148:149], v250 offset:512\n"
    "v_mfma_f32_16x16x32_bf16 v[164:167], a[124:127], a[12:15], v[164:167]\n"
    "ds_read_b64_tr_b16 a[150:151], v250 offset:4608\n"
    "v_fma_f32 v240, v157, s36, v245\n"
    "v_mfma_f32_16x16x32_bf16 v[176:179], a[96:99], a[32:35], 0\n"
    "v_exp_f32 v239, v239\n"
    "v_mfma_f32_16x16x32_bf16 v[176:179], a[100:103], a[36:39], v[176:179]\n"
    "ds_read_b64_tr_b16 a[152:153], v247 offset:768\n"
    "v_fma_f32 v241, v158, s36, v245\n"
    "v_mfma_f32_16x16x32_bf16 v[176:179], a[104:107], a[40:43], v[176:179]\n"
    "v_exp_f32 v240, v240\n"
    "v_mfma_f32_16x16x32_bf16 v[176:179], a[108:111], a[44:47], v[176:179]\n"
    "v_cvt_pk_bf16_f32 v205, v237, v238\n"
    "v_fma_f32 v243, v159, s36, v245\n"
    "v_mfma_f32_16x16x32_bf16 v[180:183], a[112:115], a[32:35], 0\n"
    "v_exp_f32 v241, v241\n"
    "v_mfma_f32_16x16x32_bf16 v[180:183], a[116:119], a[36:39], v[180:183]\n"
    "ds_read_b64_tr_b16 a[154:155], v247 offset:4864\n"
    "v_cvt_pk_bf16_f32 v206, v239, v240\n"
    "v_mfma_f32_16x16x32_bf16 v[180:183], a[120:123], a[40:43], v[180:183]\n"
    "v_exp_f32 v243, v243\n"
    "v_mfma_f32_16x16x32_bf16 v[180:183], a[124:127], a[44:47], v[180:183]\n"
    "v_cvt_pk_bf16_f32 v204, v242, v236\n"
    "v_cvt_pk_bf16_f32 v207, v241, v243\n"
    "v_mfma_f32_16x16x32_bf16 v[184:187], a[96:99], a[48:51], 0\n"
    "ds_read_b64_tr_b16 a[156:157], v250 offset:768\n"
    "ds_read_b64_tr_b16 a[158:159], v250 offset:4864\n"
    "v_fma_f32 v232, v168, s36, v212\n"
    "v_mfma_f32_16x16x32_bf16 v[224:227], a[172:175], v[204:207], v[224:227]\n"
    "v_exp_f32 v232, v232\n"
    "v_mfma_f32_16x16x32_bf16 v[184:187], a[100:103], a[52:55], v[184:187]\n"
    "v_fma_f32 v233, v169, s36, v212\n"
    "v_fma_f32 v234, v170, s36, v212\n"
    "v_mfma_f32_16x16x32_bf16 v[184:187], a[104:107], a[56:59], v[184:187]\n"
    "v_exp_f32 v233, v233\n"
    "v_mfma_f32_16x16x32_bf16 v[184:187], a[108:111], a[60:63], v[184:187]\n"
    "v_exp_f32 v234, v234\n"
    "v_mfma_f32_16x16x32_bf16 v[188:191], a[112:115], a[48:51], 0\n"
    "v_fma_f32 v235, v171, s36, v212\n"
    "v_max3_f32 v241, v224, v225, v226\n"
    "v_mfma_f32_16x16x32_bf16 v[188:191], a[116:119], a[52:55], v[188:191]\n"
    "s_sub_u32 s62, s62, 1\n"
    "v_add_u32 v246, s60, v248\n"
    "v_mfma_f32_16x16x32_bf16 v[188:191], a[120:123], a[56:59], v[188:191]\n"
    "s_cmp_lg_u32 s62, 0\n"
    "v_max_f32 v241, v241, v227\n"
    "v_cmp_nge_f32 vcc, s37, v241\n"
    "v_mfma_f32_16x16x32_bf16 v[188:191], a[124:127], a[60:63], v[188:191]\n"
    "s_waitcnt lgkmcnt(0)\n"
    "v_exp_f32 v235, v235\n"
    "s_cbranch_vccnz L_rc_L1a\n"
    "L_rr_L1a:\n"
    "s_cbranch_scc1 L_L1\n"
    "L_T1b:\n"
    "s_sub_i32 s92, s38, 2\n"
    "s_cmp_lt_i32 s92, 0\n"
    "s_cselect_b64 s[84:85], -1, 0\n"
    "s_cmp_eq_i32 s92, 0\n"
    "s_cselect_b64 s[72:73], s[64:65], s[84:85]\n"
    "s_cselect_b64 s[74:75], s[66:67], s[84:85]\n"
    "s_cselect_b64 s[76:77], s[68:69], s[84:85]\n"
    "s_cselect_b64 s[78:79], s[70:71], s[84:85]\n"
    "s_cmp_lt_i32 s92, 1\n"
    "s_cselect_b64 s[84:85], -1, 0\n"
    "s_cmp_eq_i32 s92, 1\n"
    "s_cselect_b64 s[80:81], s[64:65], s[84:85]\n"
    "s_cselect_b64 s[82:83], s[66:67], s[84:85]\n"
    "s_cselect_b64 s[88:89], s[68:69], s[84:85]\n"
    "s_cselect_b64 s[90:91], s[70:71], s[84:85]\n"
    "v_mfma_f32_16x16x32_bf16 v[32:35], a[128:131], v[196:199], v[32:35]\n"
    "v_fma_f32 v236, v172, s36, v212\n"
    "v_fma_f32 v237, v173, s36, v212\n"
    "v_fma_f32 v238, v174, s36, v212\n"
    "v_mfma_f32_16x16x32_bf16 v[36:39], a[132:135], v[196:199], v[36:39]\n"
    "v_exp_f32 v236, v236\n"
    "v_mfma_f32_16x16x32_bf16 v[40:43], a[136:139], v[196:199], v[40:43]\n"
    "s_waitcnt vmcnt(22)\n"
    "v_exp_f32 v237, v237\n"
    "v_mfma_f32_16x16x32_bf16 v[44:47], a[140:143], v[196:199], v[44:47]\n"
    "s_barrier\n"
    "v_cndmask_b32 v176, v176, v254, s[72:73]\n"
    "v_exp_f32 v238, v238\n"
    "v_fma_f32 v239, v175, s36, v212\n"
    "v_mfma_f32_16x16x32_bf16 v[48:51], a[144:147], v[196:199], v[48:51]\n"
    "v_cndmask_b32 v177, v177, v254, s[74:75]\n"
    "v_exp_f32 v239, v239\n"
    "v_fma_f32 v240, v160, s36, v208\n"
    "v_mfma_f32_16x16x32_bf16 v[52:55], a[148:151], v[196:199], v[52:55]\n"
    "ds_read_b128 a[96:99], v246 offset:0\n"
    "ds_read_b128 a[100:103], v246 offset:256\n"
    "v_mfma_f32_16x16x32_bf16 v[56:59], a[152:155], v[196:199], v[56:59]\n"
    "v_exp_f32 v240, v240\n"
    "v_fma_f32 v241, v161, s36, v208\n"
    "v_mfma_f32_16x16x32_bf16 v[60:63], a[156:159], v[196:199], v[60:63]\n"
    "v_cndmask_b32 v180, v180, v254, s[80:81]\n"
    "v_cvt_pk_bf16_f32 v196, v232, v233\n"
    "v_fma_f32 v242, v162, s36, v208\n"
    "v_mfma_f32_16x16x32_bf16 v[0:3], a[128:131], v[192:195], v[0:3]\n"
    "v_cndmask_b32 v181, v181, v254, s[82:83]\n"
    "v_cndmask_b32 v182, v182, v254, s[88:89]\n"
    "v_fma_f32 v243, v163, s36, v208\n"
    "v_mfma_f32_16x16x32_bf16 v[4:7], a[132:135], v[192:195], v[4:7]\n"
    "v_cvt_pk_bf16_f32 v197, v234, v235\n"
    "v_cvt_pk_bf16_f32 v198, v236, v237\n"
    "v_mfma_f32_16x16x32_bf16 v[8:11], a[136:139], v[192:195], v[8:11]\n"
    "v_cvt_pk_bf16_f32 v199, v238, v239\n"
    "v_exp_f32 v241, v241\n"
    "v_mfma_f32_16x16x32_bf16 v[12:15], a[140:143], v[192:195], v[12:15]\n"
    "ds_read_b128 a[104:107], v246 offset:512\n"
    "v_cndmask_b32 v183, v183, v254, s[90:91]\n"
    "v_fma_f32 v236, v164, s36, v208\n"
    "v_mfma_f32_16x16x32_bf16 v[228:231], a[164:167], v[196:199], v[224:227]\n"
    "v_cndmask_b32 v178, v178, v254, s[76:77]\n"
    "v_exp_f32 v242, v242\n"
    "v_mfma_f32_16x16x32_bf16 v[16:19], a[144:147], v[192:195], v[16:19]\n"
    "s_and_b32 m0, s58, 0xffff\n"
    "v_exp_f32 v243, v243\n"
    "v_fma_f32 v237, v165, s36, v208\n"
    "v_mfma_f32_16x16x32_bf16 v[20:23], a[148:151], v[192:195], v[20:23]\n"
    "buffer_load_dwordx4 v251, s[40:43], s56 offen offset:0 lds\n"
    "v_mfma_f32_16x16x32_bf16 v[24:27], a[152:155], v[192:195], v[24:27]\n"
    "v_exp_f32 v236, v236\n"
    "v_fma_f32 v238, v166, s36, v208\n"
    "v_mfma_f32_16x16x32_bf16 v[28:31], a[156:159], v[192:195], v[28:31]\n"
    "v_exp_f32 v237, v237\n"
    "v_fma_f32 v239, v167, s36, v208\n"
    "v_mfma_f32_16x16x32_bf16 v[64:67], a[128:131], v[200:203], v[64:67]\n"
    "v_cvt_pk_bf16_f32 v193, v242, v243\n"
    "v_exp_f32 v238, v238\n"
    "v_mfma_f32_16x16x32_bf16 v[96:99], a[128:131], v[204:207], v[96:99]\n"
    "v_cndmask_b32 v179, v179, v254, s[78:79]\n"
    "v_cvt_pk_bf16_f32 v194, v236, v237\n"
    "v_fma_f32 v242, v176, s36, v244\n"
    "v_mfma_f32_16x16x32_bf16 v[68:71], a[132:135], v[200:203], v[68:71]\n"
    "buffer_load_dwordx4 v252, s[40:43], s56 offen offset:1024 lds\n"
    "v_mfma_f32_16x16x32_bf16 v[100:103], a[132:135], v[204:207], v[100:103]\n"
    "s_add_u32 s58, s58, 8192\n"
    "ds_read_b128 a[108:111], v246 offset:768\n"
    "v_exp_f32 v239, v239\n"
    "v_mfma_f32_16x16x32_bf16 v[72:75], a[136:139], v[200:203], v[72:75]\n"
    "ds_read_b64_tr_b16 a[128:129], v247 offset:8192\n"
    "v_cvt_pk_bf16_f32 v195, v238, v239\n"
    "v_mfma_f32_16x16x32_bf16 v[104:107], a[136:139], v[204:207], v[104:107]\n"
    "s_and_b32 m0, s59, 0xffff\n"
    "v_cvt_pk_bf16_f32 v192, v240, v241\n"
    "v_fma_f32 v236, v177, s36, v244\n"
    "v_fma_f32 v237, v178, s36, v244\n"
    "v_mfma_f32_16x16x32_bf16 v[76:79], a[140:143], v[200:203], v[76:79]\n"
    "s_bitset1_b32 m0, 16\n"
    "ds_read_b64_tr_b16 a[130:131], v247 offset:12288\n"
    "v_exp_f32 v242, v242\n"
    "v_mfma_f32_16x16x32_bf16 v[228:231], a[160:163], v[192:195], v[228:231]\n"
    "s_cmp_lt_u32 s58, s17\n"
    "buffer_load_dwordx4 v251, s[44:47], s57 offen offset:0 lds\n"
    "v_mfma_f32_16x16x32_bf16 v[108:111], a[140:143], v[204:207], v[108:111]\n"
    "s_cselect_b32 s56, s18, 0\n"
    "ds_read_b64_tr_b16 a[132:133], v250 offset:8192\n"
    "v_exp_f32 v236, v236\n"
    "v_mfma_f32_16x16x32_bf16 v[80:83], a[144:147], v[200:203], v[80:83]\n"
    "s_add_u32 s56, s56, s58\n"
    "ds_read_b128 a[112:115], v246 offset:4096\n"
    "ds_read_b64_tr_b16 a[134:135], v250 offset:12288\n"
    "v_fma_f32 v238, v179, s36, v244\n"
    "v_mfma_f32_16x16x32_bf16 v[112:115], a[144:147], v[204:207], v[112:115]\n"
    "ds_read_b128 a[116:119], v246 offset:4352\n"
    "ds_read_b128 a[120:123], v246 offset:4608\n"
    "v_fma_f32 v239, v180, s36, v244\n"
    "v_mfma_f32_16x16x32_bf16 v[84:87], a[148:151], v[200:203], v[84:87]\n"
    "v_exp_f32 v237, v237\n"
    "v_mfma_f32_16x16x32_bf16 v[116:119], a[148:151], v[204:207], v[116:119]\n"
    "v_exp_f32 v238, v238\n"
    "v_fma_f32 v240, v181, s36, v244\n"
    "v_mfma_f32_16x16x32_bf16 v[88:91], a[152:155], v[200:203], v[88:91]\n"
    "buffer_load_dwordx4 v252, s[44:47], s57 offen offset:1024 lds\n"
    "v_mfma_f32_16x16x32_bf16 v[120:123], a[152:155], v[204:207], v[120:123]\n"
    "s_add_u32 s59, s59, 8192\n"
    "v_exp_f32 v239, v239\n"
    "v_fma_f32 v241, v182, s36, v244\n"
    "v_mfma_f32_16x16x32_bf16 v[92:95], a[156:159], v[200:203], v[92:95]\n"
    "s_cmp_lt_u32 s59, s17\n"
    "v_exp_f32 v240, v240\n"
    "v_mfma_f32_16x16x32_bf16 v[124:127], a[156:159], v[204:207], v[124:127]\n"
    "s_waitcnt lgkmcnt(4)\n"
    "s_cselect_b32 s57, s18, 0\n"
    "v_cvt_pk_bf16_f32 v201, v237, v238\n"
    "v_exp_f32 v241, v241\n"
    "v_mfma_f32_16x16x32_bf16 v[136:139], a[96:99], a[16:19], 0\n"
    "s_add_u32 s57, s57, s59\n"
    "ds_read_b128 a[124:127], v246 offset:4864\n"
    "ds_read_b64_tr_b16 a[136:137], v247 offset:8448\n"
    "v_fma_f32 v237, v183, s36, v244\n"
    "v_mfma_f32_16x16x32_bf16 v[136:139], a[100:103], a[20:23], v[136:139]\n"
    "v_cvt_pk_bf16_f32 v200, v242, v236\n"
    "v_exp_f32 v237, v237\n"
    "v_mfma_f32_16x16x32_bf16 v[136:139], a[104:107], a[24:27], v[136:139]\n"
    "v_cvt_pk_bf16_f32 v202, v239, v240\n"
    "v_cvt_pk_bf16_f32 v203, v241, v237\n"
    "v_mfma_f32_16x16x32_bf16 v[136:139], a[108:111], a[28:31], v[136:139]\n"
    "ds_read_b64_tr_b16 a[138:139], v247 offset:12544\n"
    "v_fma_f32 v236, v184, s36, v245\n"
    "v_mfma_f32_16x16x32_bf16 v[228:231], a[168:171], v[200:203], v[228:231]\n"
    "s_waitcnt lgkmcnt(3)\n"
    "ds_read_b64_tr_b16 a[140:141], v250 offset:8448\n"
    "v_exp_f32 v236, v236\n"
    "v_mfma_f32_16x16x32_bf16 v[140:143], a[112:115], a[16:19], 0\n"
    "ds_read_b64_tr_b16 a[142:143], v250 offset:12544\n"
    "v_fma_f32 v237, v185, s36, v245\n"
    "v_mfma_f32_16x16x32_bf16 v[140:143], a[116:119], a[20:23], v[140:143]\n"
    "v_fma_f32 v238, v186, s36, v245\n"
    "v_fma_f32 v239, v187, s36, v245\n"
    "v_fma_f32 v240, v188, s36, v245\n"
    "v_mfma_f32_16x16x32_bf16 v[140:143], a[120:123], a[24:27], v[140:143]\n"
    "s_waitcnt lgkmcnt(4)\n"
    "v_exp_f32 v237, v237\n"
    "v_mfma_f32_16x16x32_bf16 v[140:143], a[124:127], a[28:31], v[140:143]\n"
    "v_exp_f32 v238, v238\n"
    "v_mfma_f32_16x16x32_bf16 v[128:131], a[96:99], a[0:3], 0\n"
    "v_cvt_pk_bf16_f32 v204, v236, v237\n"
    "v_exp_f32 v239, v239\n"
    "v_mfma_f32_16x16x32_bf16 v[128:131], a[100:103], a[4:7], v[128:131]\n"
    "v_fma_f32 v236, v189, s36, v245\n"
    "v_fma_f32 v237, v190, s36, v245\n"
    "v_mfma_f32_16x16x32_bf16 v[128:131], a[104:107], a[8:11], v[128:131]\n"
    "v_exp_f32 v240, v240\n"
    "v_mfma_f32_16x16x32_bf16 v[128:131], a[108:111], a[12:15], v[128:131]\n"
    "ds_read_b64_tr_b16 a[144:145], v247 offset:8704\n"
    "v_exp_f32 v236, v236\n"
    "v_mfma_f32_16x16x32_bf16 v[132:135], a[112:115], a[0:3], 0\n"
    "v_cvt_pk_bf16_f32 v206, v240, v236\n"
    "v_fma_f32 v241, v191, s36, v245\n"
    "v_mfma_f32_16x16x32_bf16 v[132:135], a[116:119], a[4:7], v[132:135]\n"
    "ds_read_b64_tr_b16 a[146:147], v247 offset:12800\n"
    "v_exp_f32 v237, v237\n"
    "v_mfma_f32_16x16x32_bf16 v[132:135], a[120:123], a[8:11], v[132:135]\n"
    "v_exp_f32 v241, v241\n"
    "v_fma_f32 v232, v136, s36, v212\n"
    "v_mfma_f32_16x16x32_bf16 v[132:135], a[124:127], a[12:15], v[132:135]\n"
    "ds_read_b64_tr_b16 a[148:149], v250 offset:8704\n"
    "ds_read_b64_tr_b16 a[150:151], v250 offset:12800\n"
    "v_mfma_f32_16x16x32_bf16 v[152:155], a[96:99], a[48:51], 0\n"
    "ds_read_b64_tr_b16 a[152:153], v247 offset:8960\n"
    "v_cvt_pk_bf16_f32 v205, v238, v239\n"
    "v_cvt_pk_bf16_f32 v207, v237, v241\n"
    "v_mfma_f32_16x16x32_bf16 v[152:155], a[100:103], a[52:55], v[152:155]\n"
    "ds_read_b64_tr_b16 a[154:155], v247 offset:13056\n"
    "ds_read_b64_tr_b16 a[156:157], v250 offset:8960\n"
    "v_mfma_f32_16x16x32_bf16 v[228:231], a[172:175], v[204:207], v[228:231]\n"
    "ds_read_b64_tr_b16 a[158:159], v250 offset:13056\n"
    "v_fma_f32 v233, v137, s36, v212\n"
    "v_fma_f32 v234, v138, s36, v212\n"
    "v_mfma_f32_16x16x32_bf16 v[152:155], a[104:107], a[56:59], v[152:155]\n"
    "v_exp_f32 v232, v232\n"
    "v_mfma_f32_16x16x32_bf16 v[152:155], a[108:111], a[60:63], v[152:155]\n"
    "v_exp_f32 v233, v233\n"
    "v_mfma_f32_16x16x32_bf16 v[156:159], a[112:115], a[48:51], 0\n"
    "v_exp_f32 v234, v234\n"
    "v_mfma_f32_16x16x32_bf16 v[156:159], a[116:119], a[52:55], v[156:159]\n"
    "v_fma_f32 v235, v139, s36, v212\n"
    "v_max3_f32 v241, v228, v229, v230\n"
    "v_mfma_f32_16x16x32_bf16 v[156:159], a[120:123], a[56:59], v[156:159]\n"
    "v_max_f32 v241, v241, v231\n"
    "v_cmp_nge_f32 vcc, s37, v241\n"
    "v_mfma_f32_16x16x32_bf16 v[156:159], a[124:127], a[60:63], v[156:159]\n"
    "s_waitcnt lgkmcnt(0)\n"
    "v_exp_f32 v235, v235\n"
    "s_cbranch_vccnz L_rc_T1b\n"
    "L_rr_T1b:\n"
    "v_mfma_f32_16x16x32_bf16 v[32:35], a[128:131], v[196:199], v[32:35]\n"
    "v_fma_f32 v236, v140, s36, v212\n"
    "v_fma_f32 v237, v141, s36, v212\n"
    "v_mfma_f32_16x16x32_bf16 v[36:39], a[132:135], v[196:199], v[36:39]\n"
    "v_exp_f32 v236, v236\n"
    "v_fma_f32 v238, v142, s36, v212\n"
    "v_mfma_f32_16x16x32_bf16 v[40:43], a[136:139], v[196:199], v[40:43]\n"
    "s_waitcnt vmcnt(18)\n"
    "v_fma_f32 v239, v143, s36, v212\n"
    "v_fma_f32 v240, v128, s36, v208\n"
    "v_mfma_f32_16x16x32_bf16 v[44:47], a[140:143], v[196:199], v[44:47]\n"
    "s_barrier\n"
    "v_exp_f32 v237, v237\n"
    "v_fma_f32 v241, v129, s36, v208\n"
    "v_mfma_f32_16x16x32_bf16 v[48:51], a[144:147], v[196:199], v[48:51]\n"
    "s_add_u32 s61, s61, 16384\n"
    "ds_read_b128 a[96:99], v246 offset:8192\n"
    "v_exp_f32 v238, v238\n"
    "v_mfma_f32_16x16x32_bf16 v[52:55], a[148:151], v[196:199], v[52:55]\n"
    "s_and_b32 s61, s61, 0xffff\n"
    "ds_read_b128 a[100:103], v246 offset:8448\n"
    "ds_read_b128 a[104:107], v246 offset:8704\n"
    "v_mfma_f32_16x16x32_bf16 v[56:59], a[152:155], v[196:199], v[56:59]\n"
    "s_sub_i32 s92, 3, s38\n"
    "ds_read_b128 a[108:111], v246 offset:8960\n"
    "ds_read_b128 a[112:115], v246 offset:12288\n"
    "v_mfma_f32_16x16x32_bf16 v[60:63], a[156:159], v[196:199], v[60:63]\n"
    "s_cmp_lt_i32 s92, 0\n"
    "v_add_u32 v247, s61, v249\n"
    "v_exp_f32 v239, v239\n"
    "v_mfma_f32_16x16x32_bf16 v[0:3], a[128:131], v[192:195], v[0:3]\n"
    "s_cselect_b64 s[84:85], -1, 0\n"
    "v_cvt_pk_bf16_f32 v197, v234, v235\n"
    "v_fma_f32 v242, v130, s36, v208\n"
    "v_mfma_f32_16x16x32_bf16 v[4:7], a[132:135], v[192:195], v[4:7]\n"
    "v_cvt_pk_bf16_f32 v196, v232, v233\n"
    "v_cvt_pk_bf16_f32 v199, v238, v239\n"
    "v_mfma_f32_16x16x32_bf16 v[8:11], a[136:139], v[192:195], v[8:11]\n"
    "v_cvt_pk_bf16_f32 v198, v236, v237\n"
    "v_fma_f32 v238, v131, s36, v208\n"
    "v_mfma_f32_16x16x32_bf16 v[12:15], a[140:143], v[192:195], v[12:15]\n"
    "v_fma_f32 v236, v132, s36, v208\n"
    "v_fma_f32 v237, v133, s36, v208\n"
    "v_mfma_f32_16x16x32_bf16 v[224:227], a[164:167], v[196:199], v[228:231]\n"
    "v_exp_f32 v240, v240\n"
    "v_mfma_f32_16x16x32_bf16 v[16:19], a[144:147], v[192:195], v[16:19]\n"
    "s_and_b32 m0, s58, 0xffff\n"
    "v_exp_f32 v241, v241\n"
    "v_mfma_f32_16x16x32_bf16 v[20:23], a[148:151], v[192:195], v[20:23]\n"
    "s_cmp_eq_i32 s92, 0\n"
    "buffer_load_dwordx4 v251, s[40:43], s56 offen offset:0 lds\n"
    "v_mfma_f32_16x16x32_bf16 v[24:27], a[152:155], v[192:195], v[24:27]\n"
    "s_cselect_b64 s[72:73], s[64:65], s[84:85]\n"
    "v_exp_f32 v242, v242\n"
    "v_mfma_f32_16x16x32_bf16 v[28:31], a[156:159], v[192:195], v[28:31]\n"
    "s_cselect_b64 s[74:75], s[66:67], s[84:85]\n"
    "v_exp_f32 v238, v238\n"
    "v_mfma_f32_16x16x32_bf16 v[64:67], a[128:131], v[200:203], v[64:67]\n"
    "s_cselect_b64 s[76:77], s[68:69], s[84:85]\n"
    "v_xor_b32 v250, 32, v247\n"
    "v_cvt_pk_bf16_f32 v193, v242, v238\n"
    "v_mfma_f32_16x16x32_bf16 v[96:99], a[128:131], v[204:207], v[96:99]\n"
    "s_cselect_b64 s[78:79], s[70:71], s[84:85]\n"
    "v_exp_f32 v236, v236\n"
    "v_mfma_f32_16x16x32_bf16 v[68:71], a[132:135], v[200:203], v[68:71]\n"
    "s_cmp_lt_i32 s92, 1\n"
    "buffer_load_dwordx4 v252, s[40:43], s56 offen offset:1024 lds\n"
    "v_mfma_f32_16x16x32_bf16 v[100:103], a[132:135], v[204:207], v[100:103]\n"
    "s_cselect_b64 s[84:85], -1, 0\n"
    "v_fma_f32 v238, v134, s36, v208\n"
    "v_fma_f32 v239, v135, s36, v208\n"
    "v_mfma_f32_16x16x32_bf16 v[72:75], a[136:139], v[200:203], v[72:75]\n"
    "s_add_u32 s58, s58, 8192\n"
    "ds_read_b64_tr_b16 a[128:129], v247 offset:0\n"
    "v_exp_f32 v237, v237\n"
    "v_mfma_f32_16x16x32_bf16 v[104:107], a[136:139], v[204:207], v[104:107]\n"
    "s_and_b32 m0, s59, 0xffff\n"
    "v_exp_f32 v238, v238\n"
    "v_mfma_f32_16x16x32_bf16 v[76:79], a[140:143], v[200:203], v[76:79]\n"
    "s_bitset1_b32 m0, 16\n"
    "v_exp_f32 v239, v239\n"
    "v_mfma_f32_16x16x32_bf16 v[108:111], a[140:143], v[204:207], v[108:111]\n"
    "s_cmp_eq_i32 s92, 1\n"
    "s_cselect_b64 s[80:81], s[64:65], s[84:85]\n"
    "s_cselect_b64 s[82:83], s[66:67], s[84:85]\n"
    "s_cselect_b64 s[88:89], s[68:69], s[84:85]\n"
    "s_cselect_b64 s[90:91], s[70:71], s[84:85]\n"
    "buffer_load_dwordx4 v251, s[44:47], s57 offen offset:0 lds\n"
    "v_mfma_f32_16x16x32_bf16 v[80:83], a[144:147], v[200:203], v[80:83]\n"
    "s_add_u32 s60, s60, 16384\n"
    "v_cndmask_b32 v152, v152, v254, s[72:73]\n"
    "v_cvt_pk_bf16_f32 v192, v240, v241\n"
    "v_mfma_f32_16x16x32_bf16 v[112:115], a[144:147], v[204:207], v[112:115]\n"
    "s_add_u32 s60, s60, s63\n"
    "v_cndmask_b32 v156, v156, v254, s[80:81]\n"
    "v_cvt_pk_bf16_f32 v195, v238, v239\n"
    "v_mfma_f32_16x16x32_bf16 v[84:87], a[148:151], v[200:203], v[84:87]\n"
    "s_and_b32 s60, s60, 0xffff\n"
    "ds_read_b128 a[116:119], v246 offset:12544\n"
    "v_cndmask_b32 v153, v153, v254, s[74:75]\n"
    "v_mfma_f32_16x16x32_bf16 v[116:119], a[148:151], v[204:207], v[116:119]\n"
    "s_cmp_lt_u32 s58, s17\n"
    "v_cndmask_b32 v154, v154, v254, s[76:77]\n"
    "v_cvt_pk_bf16_f32 v194, v236, v237\n"
    "v_mfma_f32_16x16x32_bf16 v[88:91], a[152:155], v[200:203], v[88:91]\n"
    "s_cselect_b32 s56, s18, 0\n"
    "ds_read_b128 a[120:123], v246 offset:12800\n"
    "ds_read_b128 a[124:127], v246 offset:13056\n"
    "v_cndmask_b32 v157, v157, v254, s[82:83]\n"
    "v_mfma_f32_16x16x32_bf16 v[224:227], a[160:163], v[192:195], v[224:227]\n"
    "s_add_u32 s56, s56, s58\n"
    "buffer_load_dwordx4 v252, s[44:47], s57 offen offset:1024 lds\n"
    "v_mfma_f32_16x16x32_bf16 v[120:123], a[152:155], v[204:207], v[120:123]\n"
    "s_add_u32 s59, s59, 8192\n"
    "v_cndmask_b32 v155, v155, v254, s[78:79]\n"
    "v_fma_f32 v236, v152, s36, v245\n"
    "v_mfma_f32_16x16x32_bf16 v[92:95], a[156:159], v[200:203], v[92:95]\n"
    "s_cmp_lt_u32 s59, s17\n"
    "ds_read_b64_tr_b16 a[130:131], v247 offset:4096\n"
    "ds_read_b64_tr_b16 a[132:133], v250 offset:0\n"
    "ds_read_b64_tr_b16 a[134:135], v250 offset:4096\n"
    "v_mfma_f32_16x16x32_bf16 v[124:127], a[156:159], v[204:207], v[124:127]\n"
    "s_waitcnt lgkmcnt(6)\n"
    "s_cselect_b32 s57, s18, 0\n"
    "ds_read_b64_tr_b16 a[136:137], v247 offset:256\n"
    "v_fma_f32 v237, v153, s36, v245\n"
    "v_mfma_f32_16x16x32_bf16 v[168:171], a[96:99], a[16:19], 0\n"
    "s_add_u32 s57, s57, s59\n"
    "v_cndmask_b32 v158, v158, v254, s[88:89]\n"
    "v_fma_f32 v238, v154, s36, v245\n"
    "v_mfma_f32_16x16x32_bf16 v[168:171], a[100:103], a[20:23], v[168:171]\n"
    "ds_read_b64_tr_b16 a[138:139], v247 offset:4352\n"
    "ds_read_b64_tr_b16 a[140:141], v250 offset:256\n"
    "v_mfma_f32_16x16x32_bf16 v[168:171], a[104:107], a[24:27], v[168:171]\n"
    "v_exp_f32 v236, v236\n"
    "v_mfma_f32_16x16x32_bf16 v[168:171], a[108:111], a[28:31], v[168:171]\n"
    "v_cndmask_b32 v159, v159, v254, s[90:91]\n"
    "v_fma_f32 v239, v155, s36, v245\n"
    "v_mfma_f32_16x16x32_bf16 v[172:175], a[112:115], a[16:19], 0\n"
    "s_waitcnt lgkmcnt(6)\n"
    "v_exp_f32 v237, v237\n"
    "v_mfma_f32_16x16x32_bf16 v[172:175], a[116:119], a[20:23], v[172:175]\n"
    "v_exp_f32 v238, v238\n"
    "v_fma_f32 v240, v156, s36, v245\n"
    "v_mfma_f32_16x16x32_bf16 v[172:175], a[120:123], a[24:27], v[172:175]\n"
    "v_exp_f32 v239, v239\n"
    "v_fma_f32 v241, v157, s36, v245\n"
    "v_mfma_f32_16x16x32_bf16 v[172:175], a[124:127], a[28:31], v[172:175]\n"
    "ds_read_b64_tr_b16 a[142:143], v250 offset:4352\n"
    "v_exp_f32 v240, v240\n"
    "v_mfma_f32_16x16x32_bf16 v[160:163], a[96:99], a[0:3], 0\n"
    "v_exp_f32 v241, v241\n"
    "v_fma_f32 v242, v158, s36, v245\n"
    "v_mfma_f32_16x16x32_bf16 v[160:163], a[100:103], a[4:7], v[160:163]\n"
    "v_exp_f32 v242, v242\n"
    "v_mfma_f32_16x16x32_bf16 v[160:163], a[104:107], a[8:11], v[160:163]\n"
    "v_cvt_pk_bf16_f32 v204, v236, v237\n"
    "v_cvt_pk_bf16_f32 v206, v240, v241\n"
    "v_fma_f32 v243, v159, s36, v245\n"
    "v_mfma_f32_16x16x32_bf16 v[160:163], a[108:111], a[12:15], v[160:163]\n"
    "v_exp_f32 v243, v243\n"
    "v_mfma_f32_16x16x32_bf16 v[164:167], a[112:115], a[0:3], 0\n"
    "v_cvt_pk_bf16_f32 v205, v238, v239\n"
    "v_cvt_pk_bf16_f32 v207, v242, v243\n"
    "v_mfma_f32_16x16x32_bf16 v[164:167], a[116:119], a[4:7], v[164:167]\n"
    "ds_read_b64_tr_b16 a[144:145], v247 offset:512\n"
    "ds_read_b64_tr_b16 a[146:147], v247 offset:4608\n"
    "v_mfma_f32_16x16x32_bf16 v[164:167], a[120:123], a[8:11], v[164:167]\n"
    "ds_read_b64_tr_b16 a[148:149], v250 offset:512\n"
    "ds_read_b64_tr_b16 a[150:151], v250 offset:4608\n"
    "v_mfma_f32_16x16x32_bf16 v[164:167], a[124:127], a[12:15], v[164:167]\n"
    "ds_read_b64_tr_b16 a[152:153], v247 offset:768\n"
    "ds_read_b64_tr_b16 a[154:155], v247 offset:4864\n"
    "v_mfma_f32_16x16x32_bf16 v[224:227], a[172:175], v[204:207], v[224:227]\n"
    "ds_read_b64_tr_b16 a[156:157], v250 offset:768\n"
    "v_fma_f32 v232, v168, s36, v212\n"
    "v_mfma_f32_16x16x32_bf16 v[184:187], a[96:99], a[48:51], 0\n"
    "v_exp_f32 v232, v232\n"
    "v_mfma_f32_16x16x32_bf16 v[184:187], a[100:103], a[52:55], v[184:187]\n"
    "ds_read_b64_tr_b16 a[158:159], v250 offset:4864\n"
    "v_fma_f32 v233, v169, s36, v212\n"
    "v_mfma_f32_16x16x32_bf16 v[184:187], a[104:107], a[56:59], v[184:187]\n"
    "v_exp_f32 v233, v233\n"
    "v_mfma_f32_16x16x32_bf16 v[184:187], a[108:111], a[60:63], v[184:187]\n"
    "v_fma_f32 v234, v170, s36, v212\n"
    "v_fma_f32 v235, v171, s36, v212\n"
    "v_mfma_f32_16x16x32_bf16 v[188:191], a[112:115], a[48:51], 0\n"
    "v_exp_f32 v234, v234\n"
    "v_mfma_f32_16x16x32_bf16 v[188:191], a[116:119], a[52:55], v[188:191]\n"
    "s_nop 0\n"
    "v_max3_f32 v241, v224, v225, v226\n"
    "v_max_f32 v241, v241, v227\n"
    "v_mfma_f32_16x16x32_bf16 v[188:191], a[120:123], a[56:59], v[188:191]\n"
    "s_cmp_eq_u32 s39, 7\n"
    "v_add_u32 v246, s60, v248\n"
    "v_cmp_nge_f32 vcc, s37, v241\n"
    "v_mfma_f32_16x16x32_bf16 v[188:191], a[124:127], a[60:63], v[188:191]\n"
    "s_waitcnt lgkmcnt(0)\n"
    "v_exp_f32 v235, v235\n"
    "s_cbranch_vccnz L_rc_T1c\n"
    "L_rr_T1c:\n"
    "s_cbranch_scc1 L_X7\n"
    "v_mfma_f32_16x16x32_bf16 v[32:35], a[128:131], v[196:199], v[32:35]\n"
    "v_fma_f32 v236, v172, s36, v212\n"
    "v_fma_f32 v237, v173, s36, v212\n"
    "v_rcp_f32 v255, v226\n"
    "v_mfma_f32_16x16x32_bf16 v[36:39], a[132:135], v[196:199], v[36:39]\n"
    "v_fma_f32 v238, v174, s36, v212\n"
    "v_mul_f32 v66, v255, v66\n"
    "v_mul_f32 v67, v255, v67\n"
    "v_mul_f32 v76, v255, v76\n"
    "v_mfma_f32_16x16x32_bf16 v[40:43], a[136:139], v[196:199], v[40:43]\n"
    "s_waitcnt vmcnt(22)\n"
    "v_mul_f32 v65, v255, v65\n"
    "v_mul_f32 v72, v255, v72\n"
    "v_mul_f32 v73, v255, v73\n"
    "v_mfma_f32_16x16x32_bf16 v[44:47], a[140:143], v[196:199], v[44:47]\n"
    "s_barrier\n"
    "ds_read_b128 a[96:99], v246 offset:0\n"
    "v_exp_f32 v236, v236\n"
    "v_fma_f32 v239, v175, s36, v212\n"
    "v_mfma_f32_16x16x32_bf16 v[48:51], a[144:147], v[196:199], v[48:51]\n"
    "v_fma_f32 v240, v160, s36, v208\n"
    "v_fma_f32 v241, v161, s36, v208\n"
    "v_mul_f32 v64, v255, v64\n"
    "v_mul_f32 v74, v255, v74\n"
    "v_mfma_f32_16x16x32_bf16 v[52:55], a[148:151], v[196:199], v[52:55]\n"
    "s_sub_i32 s92, 1, s38\n"
    "ds_read_b128 a[100:103], v246 offset:256\n"
    "ds_read_b128 a[104:107], v246 offset:512\n"
    "v_exp_f32 v237, v237\n"
    "v_mfma_f32_16x16x32_bf16 v[56:59], a[152:155], v[196:199], v[56:59]\n"
    "s_cmp_lt_i32 s92, 0\n"
    "v_fma_f32 v242, v162, s36, v208\n"
    "v_mul_f32 v75, v255, v75\n"
    "v_mul_f32 v82, v255, v82\n"
    "v_mul_f32 v86, v255, v86\n"
    "v_mfma_f32_16x16x32_bf16 v[60:63], a[156:159], v[196:199], v[60:63]\n"
    "s_cselect_b64 s[84:85], -1, 0\n"
    "v_exp_f32 v238, v238\n"
    "v_mul_f32 v70, v255, v70\n"
    "v_mfma_f32_16x16x32_bf16 v[0:3], a[128:131], v[192:195], v[0:3]\n"
    "s_cmp_eq_i32 s92, 0\n"
    "ds_read_b128 a[108:111], v246 offset:768\n"
    "v_exp_f32 v239, v239\n"
    "v_mfma_f32_16x16x32_bf16 v[96:99], a[128:131], v[204:207], v[96:99]\n"
    "s_cselect_b64 s[72:73], s[64:65], s[84:85]\n"
    "v_cvt_pk_bf16_f32 v197, v234, v235\n"
    "v_cvt_pk_bf16_f32 v198, v236, v237\n"
    "v_cvt_pk_bf16_f32 v199, v238, v239\n"
    "v_fma_f32 v243, v163, s36, v208\n"
    "v_mfma_f32_16x16x32_bf16 v[4:7], a[132:135], v[192:195], v[4:7]\n"
    "s_cselect_b64 s[74:75], s[66:67], s[84:85]\n"
    "ds_read_b64_tr_b16 a[128:129], v247 offset:8192\n"
    "ds_read_b64_tr_b16 a[130:131], v247 offset:12288\n"
    "v_cvt_pk_bf16_f32 v196, v232, v233\n"
    "v_mfma_f32_16x16x32_bf16 v[100:103], a[132:135], v[204:207], v[100:103]\n"
    "s_cselect_b64 s[76:77], s[68:69], s[84:85]\n"
    "v_exp_f32 v240, v240\n"
    "v_fma_f32 v236, v164, s36, v208\n"
    "v_mfma_f32_16x16x32_bf16 v[228:231], a[164:167], v[196:199], v[224:227]\n"
    "s_cselect_b64 s[78:79], s[70:71], s[84:85]\n"
    "ds_read_b64_tr_b16 a[132:133], v250 offset:8192\n"
    "v_accvgpr_read_b32 v253, a245\n"
    "v_mul_f32 v89, v255, v89\n"
    "v_mfma_f32_16x16x32_bf16 v[8:11], a[136:139], v[192:195], v[8:11]\n"
    "s_and_b32 m0, s58, 0xffff\n"
    "ds_read_b128 a[112:115], v246 offset:4096\n"
    "v_exp_f32 v241, v241\n"
    "v_mul_f32 v88, v255, v88\n"
    "v_mfma_f32_16x16x32_bf16 v[104:107], a[136:139], v[204:207], v[104:107]\n"
    "s_cmp_lt_i32 s92, 1\n"
    "buffer_load_dwordx4 v251, s[40:43], s56 offen offset:0 lds\n"
    "v_mfma_f32_16x16x32_bf16 v[12:15], a[140:143], v[192:195], v[12:15]\n"
    "s_cselect_b64 s[84:85], -1, 0\n"
    "ds_read_b128 a[116:119], v246 offset:4352\n"
    "v_exp_f32 v242, v242\n"
    "v_cvt_pk_bf16_f32 v72, v72, v73\n"
    "v_mfma_f32_16x16x32_bf16 v[108:111], a[140:143], v[204:207], v[108:111]\n"
    "s_cmp_eq_i32 s92, 1\n"
    "v_exp_f32 v243, v243\n"
    "v_cvt_pk_bf16_f32 v64, v64, v65\n"
    "v_mfma_f32_16x16x32_bf16 v[16:19], a[144:147], v[192:195], v[16:19]\n"
    "s_cselect_b64 s[80:81], s[64:65], s[84:85]\n"
    "ds_read_b64_tr_b16 a[134:135], v250 offset:12288\n"
    "v_exp_f32 v236, v236\n"
    "v_fma_f32 v237, v165, s36, v208\n"
    "v_mfma_f32_16x16x32_bf16 v[112:115], a[144:147], v[204:207], v[112:115]\n"
    "s_cselect_b64 s[82:83], s[66:67], s[84:85]\n"
    "ds_read_b128 a[120:123], v246 offset:4608\n"
    "ds_read_b64_tr_b16 a[136:137], v247 offset:8448\n"
    "v_mul_f32 v71, v255, v71\n"
    "v_mul_f32 v77, v255, v77\n"
    "v_mfma_f32_16x16x32_bf16 v[20:23], a[148:151], v[192:195], v[20:23]\n"
    "s_cselect_b64 s[88:89], s[68:69], s[84:85]\n"
    "buffer_load_dwordx4 v252, s[40:43], s56 offen offset:1024 lds\n"
    "v_mfma_f32_16x16x32_bf16 v[116:119], a[148:151], v[204:207], v[116:119]\n"
    "s_cselect_b64 s[90:91], s[70:71], s[84:85]\n"
    "v_exp_f32 v237, v237\n"
    "v_fma_f32 v238, v166, s36, v208\n"
    "v_fma_f32 v239, v167, s36, v208\n"
    "v_mfma_f32_16x16x32_bf16 v[24:27], a[152:155], v[192:195], v[24:27]\n"
    "s_add_u32 s58, s58, 8192\n"
    "v_cndmask_b32 v184, v184, v254, s[72:73]\n"
    "v_exp_f32 v238, v238\n"
    "v_mfma_f32_16x16x32_bf16 v[120:123], a[152:155], v[204:207], v[120:123]\n"
    "s_and_b32 m0, s59, 0xffff\n"
    "ds_read_b128 a[124:127], v246 offset:4864\n"
    "v_exp_f32 v239, v239\n"
    "v_mfma_f32_16x16x32_bf16 v[28:31], a[156:159], v[192:195], v[28:31]\n"
    "s_bitset1_b32 m0, 16\n"
    "v_cvt_pk_bf16_f32 v192, v240, v241\n"
    "v_mul_f32 v69, v255, v69\n"
    "v_cvt_pk_bf16_f32 v73, v74, v75\n"
    "v_mfma_f32_16x16x32_bf16 v[124:127], a[156:159], v[204:207], v[124:127]\n"
    "s_waitcnt lgkmcnt(1)\n"
    "s_add_u32 s58, s58, s93\n"
    "buffer_load_dwordx4 v251, s[44:47], s57 offen offset:0 lds\n"
    "v_mfma_f32_16x16x32_bf16 v[136:139], a[96:99], a[16:19], 0\n"
    "s_cmp_lt_u32 s58, s17\n"
    "ds_read_b64_tr_b16 a[138:139], v247 offset:12544\n"
    "v_cvt_pk_bf16_f32 v194, v236, v237\n"
    "v_mul_f32 v87, v255, v87\n"
    "v_mfma_f32_16x16x32_bf16 v[136:139], a[100:103], a[20:23], v[136:139]\n"
    "s_cselect_b32 s56, s18, 0\n"
    "v_cndmask_b32 v185, v185, v254, s[74:75]\n"
    "v_cvt_pk_bf16_f32 v193, v242, v243\n"
    "v_mul_f32 v78, v255, v78\n"
    "v_mfma_f32_16x16x32_bf16 v[136:139], a[104:107], a[24:27], v[136:139]\n"
    "s_add_u32 s56, s56, s58\n"
    "ds_read_b64_tr_b16 a[140:141], v250 offset:8448\n"
    "ds_read_b64_tr_b16 a[142:143], v250 offset:12544\n"
    "v_cndmask_b32 v188, v188, v254, s[80:81]\n"
    "v_mfma_f32_16x16x32_bf16 v[136:139], a[108:111], a[28:31], v[136:139]\n"
    "v_cndmask_b32 v186, v186, v254, s[76:77]\n"
    "v_cvt_pk_bf16_f32 v195, v238, v239\n"
    "v_mul_f32 v90, v255, v90\n"
    "v_mfma_f32_16x16x32_bf16 v[140:143], a[112:115], a[16:19], 0\n"
    "v_cndmask_b32 v187, v187, v254, s[78:79]\n"
    "v_mul_f32 v68, v255, v68\n"
    "v_cvt_pk_bf16_f32 v65, v66, v67\n"
    "v_mfma_f32_16x16x32_bf16 v[228:231], a[160:163], v[192:195], v[228:231]\n"
    "buffer_load_dwordx4 v252, s[44:47], s57 offen offset:1024 lds\n"
    "v_mfma_f32_16x16x32_bf16 v[140:143], a[116:119], a[20:23], v[140:143]\n"
    "s_add_u32 s59, s59, 8192\n"
    "ds_read_b64_tr_b16 a[144:145], v247 offset:8704\n"
    "v_fma_f32 v236, v184, s36, v245\n"
    "v_fma_f32 v237, v185, s36, v245\n"
    "v_mfma_f32_16x16x32_bf16 v[140:143], a[120:123], a[24:27], v[140:143]\n"
    "s_waitcnt lgkmcnt(3)\n"
    "s_cmp_lt_u32 s59, s17\n"
    "v_exp_f32 v236, v236\n"
    "v_fma_f32 v238, v186, s36, v245\n"
    "v_mul_f32 v91, v255, v91\n"
    "v_mfma_f32_16x16x32_bf16 v[140:143], a[124:127], a[28:31], v[140:143]\n"
    "s_cselect_b32 s57, s18, 0\n"
    "v_exp_f32 v237, v237\n"
    "v_cvt_pk_bf16_f32 v66, v68, v69\n"
    "v_mul_f32 v84, v255, v84\n"
    "v_mfma_f32_16x16x32_bf16 v[128:131], a[96:99], a[0:3], 0\n"
    "s_add_u32 s57, s57, s59\n"
    "ds_read_b64_tr_b16 a[146:147], v247 offset:12800\n"
    "v_exp_f32 v238, v238\n"
    "v_mul_f32 v94, v255, v94\n"
    "v_mfma_f32_16x16x32_bf16 v[128:131], a[100:103], a[4:7], v[128:131]\n"
    "v_cndmask_b32 v189, v189, v254, s[82:83]\n"
    "v_fma_f32 v239, v187, s36, v245\n"
    "v_mul_f32 v81, v255, v81\n"
    "v_mul_f32 v95, v255, v95\n"
    "v_mfma_f32_16x16x32_bf16 v[128:131], a[104:107], a[8:11], v[128:131]\n"
    "ds_read_b64_tr_b16 a[148:149], v250 offset:8704\n"
    "v_cndmask_b32 v190, v190, v254, s[88:89]\n"
    "v_cvt_pk_bf16_f32 v67, v70, v71\n"
    "v_mfma_f32_16x16x32_bf16 v[128:131], a[108:111], a[12:15], v[128:131]\n"
    "v_cndmask_b32 v191, v191, v254, s[90:91]\n"
    "v_permlane16_swap_b32 v64, v66\n"
    "v_mul_f32 v93, v255, v93\n"
    "v_mfma_f32_16x16x32_bf16 v[132:135], a[112:115], a[0:3], 0\n"
    "v_exp_f32 v239, v239\n"
    "v_permlane16_swap_b32 v65, v67\n"
    "v_mul_f32 v79, v255, v79\n"
    "v_mfma_f32_16x16x32_bf16 v[132:135], a[116:119], a[4:7], v[132:135]\n"
    "buffer_store_dwordx4 v[64:67], v253, s[52:55], s99 offen offset:0 sc1 nt\n"
    "v_fma_f32 v240, v188, s36, v245\n"
    "v_fma_f32 v241, v189, s36, v245\n"
    "v_mfma_f32_16x16x32_bf16 v[132:135], a[120:123], a[8:11], v[132:135]\n"
    "v_exp_f32 v240, v240\n"
    "v_mul_f32 v83, v255, v83\n"
    "v_mfma_f32_16x16x32_bf16 v[132:135], a[124:127], a[12:15], v[132:135]\n"
    "v_exp_f32 v241, v241\n"
    "v_fma_f32 v242, v190, s36, v245\n"
    "v_cvt_pk_bf16_f32 v88, v88, v89\n"
    "v_mfma_f32_16x16x32_bf16 v[144:147], a[96:99], a[64:67], 0\n"
    "v_cvt_pk_bf16_f32 v74, v76, v77\n"
    "v_mul_f32 v85, v255, v85\n"
    "v_mul_f32 v92, v255, v92\n"
    "v_mfma_f32_16x16x32_bf16 v[144:147], a[100:103], a[68:71], v[144:147]\n"
    "ds_read_b64_tr_b16 a[150:151], v250 offset:12800\n"
    "v_fma_f32 v243, v191, s36, v245\n"
    "v_fma_f32 v232, v136, s36, v212\n"
    "v_cvt_pk_bf16_f32 v75, v78, v79\n"
    "v_mfma_f32_16x16x32_bf16 v[144:147], a[104:107], a[72:75], v[144:147]\n"
    "v_exp_f32 v242, v242\n"
    "v_mul_f32 v80, v255, v80\n"
    "v_cvt_pk_bf16_f32 v80, v80, v81\n"
    "v_mfma_f32_16x16x32_bf16 v[144:147], a[108:111], a[76:79], v[144:147]\n"
    "v_exp_f32 v243, v243\n"
    "v_permlane16_swap_b32 v72, v74\n"
    "v_permlane16_swap_b32 v73, v75\n"
    "v_mfma_f32_16x16x32_bf16 v[148:151], a[112:115], a[64:67], 0\n"
    "buffer_store_dwordx4 v[72:75], v253, s[52:55], s99 offen offset:64 sc1 nt\n"
    "v_cvt_pk_bf16_f32 v205, v238, v239\n"
    "v_mfma_f32_16x16x32_bf16 v[148:151], a[116:119], a[68:71], v[148:151]\n"
    "v_cvt_pk_bf16_f32 v204, v236, v237\n"
    "v_cvt_pk_bf16_f32 v206, v240, v241\n"
    "v_cvt_pk_bf16_f32 v207, v242, v243\n"
    "v_mfma_f32_16x16x32_bf16 v[148:151], a[120:123], a[72:75], v[148:151]\n"
    "v_cvt_pk_bf16_f32 v81, v82, v83\n"
    "v_cvt_pk_bf16_f32 v82, v84, v85\n"
    "v_cvt_pk_bf16_f32 v83, v86, v87\n"
    "v_mfma_f32_16x16x32_bf16 v[148:151], a[124:127], a[76:79], v[148:151]\n"
    "ds_read_b64_tr_b16 a[152:153], v247 offset:8960\n"
    "ds_read_b64_tr_b16 a[154:155], v247 offset:13056\n"
    "ds_read_b64_tr_b16 a[156:157], v250 offset:8960\n"
    "v_fma_f32 v233, v137, s36, v212\n"
    "v_mfma_f32_16x16x32_bf16 v[228:231], a[172:175], v[204:207], v[228:231]\n"
    "v_permlane16_swap_b32 v80, v82\n"
    "v_cvt_pk_bf16_f32 v89, v90, v91\n"
    "v_cvt_pk_bf16_f32 v90, v92, v93\n"
    "v_mfma_f32_16x16x32_bf16 v[152:155], a[96:99], a[80:83], 0\n"
    "ds_read_b64_tr_b16 a[158:159], v250 offset:13056\n"
    "v_permlane16_swap_b32 v81, v83\n"
    "v_cvt_pk_bf16_f32 v91, v94, v95\n"
    "v_mfma_f32_16x16x32_bf16 v[152:155], a[100:103], a[84:87], v[152:155]\n"
    "buffer_store_dwordx4 v[80:83], v253, s[52:55], s99 offen offset:128 sc1 nt\n"
    "v_mfma_f32_16x16x32_bf16 v[152:155], a[104:107], a[88:91], v[152:155]\n"
    "v_exp_f32 v232, v232\n"
    "v_fma_f32 v234, v138, s36, v212\n"
    "v_mfma_f32_16x16x32_bf16 v[152:155], a[108:111], a[92:95], v[152:155]\n"
    "v_exp_f32 v233, v233\n"
    "v_max3_f32 v241, v228, v229, v230\n"
    "v_mfma_f32_16x16x32_bf16 v[156:159], a[112:115], a[80:83], 0\n"
    "v_exp_f32 v234, v234\n"
    "v_permlane16_swap_b32 v88, v90\n"
    "v_mfma_f32_16x16x32_bf16 v[156:159], a[116:119], a[84:87], v[156:159]\n"
    "v_fma_f32 v235, v139, s36, v212\n"
    "v_max_f32 v241, v241, v231\n"
    "v_permlane16_swap_b32 v89, v91\n"
    "v_mfma_f32_16x16x32_bf16 v[156:159], a[120:123], a[88:91], v[156:159]\n"
    "v_exp_f32 v235, v235\n"
    "v_cmp_nge_f32 vcc, s37, v241\n"
    "v_mfma_f32_16x16x32_bf16 v[156:159], a[124:127], a[92:95], v[156:159]\n"
    "s_waitcnt lgkmcnt(0)\n"
    "buffer_store_dwordx4 v[88:91], v253, s[52:55], s99 offen offset:192 sc1 nt\n"
    "s_cbranch_vccnz L_rc_T1d\n"
    "L_rr_T1d:\n"
    "s_add_u32 s94, s99, s23\n"
    "s_lshl_b32 s62, s39, 1\n"
    "s_sub_u32 s62, 14, s62\n"
    "s_mov_b32 s21, 32768\n"
    "v_mfma_f32_16x16x32_bf16 v[32:35], a[128:131], v[196:199], v[32:35]\n"
    "v_fma_f32 v236, v140, s36, v212\n"
    "v_fma_f32 v237, v141, s36, v212\n"
    "v_fma_f32 v238, v142, s36, v212\n"
    "v_mfma_f32_16x16x32_bf16 v[36:39], a[132:135], v[196:199], v[36:39]\n"
    "v_exp_f32 v236, v236\n"
    "v_fma_f32 v239, v143, s36, v212\n"
    "v_mfma_f32_16x16x32_bf16 v[40:43], a[136:139], v[196:199], v[40:43]\n"
    "s_waitcnt vmcnt(22)\n"
    "v_rcp_f32 v255, v231\n"
    "v_accvgpr_read_b32 v230, a242\n"
    "v_exp_f32 v237, v237\n"
    "v_mfma_f32_16x16x32_bf16 v[44:47], a[140:143], v[196:199], v[44:47]\n"
    "s_barrier\n"
    "v_exp_f32 v238, v238\n"
    "v_fma_f32 v240, v128, s36, v208\n"
    "v_mfma_f32_16x16x32_bf16 v[48:51], a[144:147], v[196:199], v[48:51]\n"
    "s_add_u32 s61, s61, 16384\n"
    "ds_read_b128 a[96:99], v246 offset:16384\n"
    "v_accvgpr_read_b32 v231, a243\n"
    "v_fma_f32 v241, v129, s36, v208\n"
    "v_mfma_f32_16x16x32_bf16 v[52:55], a[148:151], v[196:199], v[52:55]\n"
    "s_add_u32 s61, s61, s63\n"
    "ds_read_b128 a[100:103], v246 offset:16640\n"
    "ds_read_b128 a[104:107], v246 offset:16896\n"
    "v_fma_f32 v242, v130, s36, v208\n"
    "v_mfma_f32_16x16x32_bf16 v[56:59], a[152:155], v[196:199], v[56:59]\n"
    "s_and_b32 s61, s61, 0xffff\n"
    "v_exp_f32 v239, v239\n"
    "v_fma_f32 v243, v131, s36, v208\n"
    "v_mfma_f32_16x16x32_bf16 v[60:63], a[156:159], v[196:199], v[60:63]\n"
    "s_add_u32 s60, s60, 32768\n"
    "ds_read_b128 a[108:111], v246 offset:17152\n"
    "v_cvt_pk_bf16_f32 v198, v236, v237\n"
    "v_cvt_pk_bf16_f32 v199, v238, v239\n"
    "v_mfma_f32_16x16x32_bf16 v[0:3], a[128:131], v[192:195], v[0:3]\n"
    "s_and_b32 s60, s60, 0xffff\n"
    "v_add_u32 v247, s61, v249\n"
    "v_cvt_pk_bf16_f32 v196, v232, v233\n"
    "v_cvt_pk_bf16_f32 v197, v234, v235\n"
    "v_fma_f32 v236, v132, s36, v208\n"
    "v_mfma_f32_16x16x32_bf16 v[96:99], a[128:131], v[204:207], v[96:99]\n"
    "v_xor_b32 v250, 32, v247\n"
    "v_exp_f32 v240, v240\n"
    "v_mfma_f32_16x16x32_bf16 v[4:7], a[132:135], v[192:195], v[4:7]\n"
    "ds_read_b128 a[112:115], v246 offset:20480\n"
    "ds_read_b64_tr_b16 a[128:129], v247 offset:0\n"
    "v_exp_f32 v241, v241\n"
    "v_mfma_f32_16x16x32_bf16 v[100:103], a[132:135], v[204:207], v[100:103]\n"
    "ds_read_b128 a[116:119], v246 offset:20736\n"
    "v_exp_f32 v242, v242\n"
    "v_mfma_f32_16x16x32_bf16 v[224:227], a[164:167], v[196:199], v[228:231]\n"
    "v_fma_f32 v237, v133, s36, v208\n"
    "v_fma_f32 v238, v134, s36, v208\n"
    "v_mul_f32 v98, v255, v98\n"
    "v_mfma_f32_16x16x32_bf16 v[8:11], a[136:139], v[192:195], v[8:11]\n"
    "s_and_b32 m0, s58, 0xffff\n"
    "ds_read_b128 a[120:123], v246 offset:20992\n"
    "ds_read_b64_tr_b16 a[130:131], v247 offset:4096\n"
    "v_exp_f32 v243, v243\n"
    "v_mfma_f32_16x16x32_bf16 v[104:107], a[136:139], v[204:207], v[104:107]\n"
    "buffer_load_dwordx4 v251, s[40:43], s56 offen offset:0 lds\n"
    "v_mfma_f32_16x16x32_bf16 v[12:15], a[140:143], v[192:195], v[12:15]\n"
    "ds_read_b128 a[124:127], v246 offset:21248\n"
    "ds_read_b64_tr_b16 a[132:133], v250 offset:0\n"
    "ds_read_b64_tr_b16 a[134:135], v250 offset:4096\n"
    "v_mul_f32 v100, v255, v100\n"
    "v_mfma_f32_16x16x32_bf16 v[108:111], a[140:143], v[204:207], v[108:111]\n"
    "v_exp_f32 v236, v236\n"
    "v_mul_f32 v97, v255, v97\n"
    "v_mul_f32 v102, v255, v102\n"
    "v_mfma_f32_16x16x32_bf16 v[16:19], a[144:147], v[192:195], v[16:19]\n"
    "ds_read_b64_tr_b16 a[136:137], v247 offset:256\n"
    "v_fma_f32 v239, v135, s36, v208\n"
    "v_mul_f32 v96, v255, v96\n"
    "v_mul_f32 v105, v255, v105\n"
    "v_mfma_f32_16x16x32_bf16 v[112:115], a[144:147], v[204:207], v[112:115]\n"
    "v_mul_f32 v99, v255, v99\n"
    "v_mul_f32 v104, v255, v104\n"
    "v_mul_f32 v106, v255, v106\n"
    "v_mul_f32 v107, v255, v107\n"
    "v_mfma_f32_16x16x32_bf16 v[20:23], a[148:151], v[192:195], v[20:23]\n"
    "buffer_load_dwordx4 v252, s[40:43], s56 offen offset:1024 lds\n"
    "v_mfma_f32_16x16x32_bf16 v[116:119], a[148:151], v[204:207], v[116:119]\n"
    "ds_read_b64_tr_b16 a[138:139], v247 offset:4352\n"
    "v_exp_f32 v237, v237\n"
    "v_mul_f32 v110, v255, v110\n"
    "v_mfma_f32_16x16x32_bf16 v[24:27], a[152:155], v[192:195], v[24:27]\n"
    "ds_read_b64_tr_b16 a[140:141], v250 offset:256\n"
    "ds_read_b64_tr_b16 a[142:143], v250 offset:4352\n"
    "v_cvt_pk_bf16_f32 v96, v96, v97\n"
    "v_mul_f32 v108, v255, v108\n"
    "v_mfma_f32_16x16x32_bf16 v[120:123], a[152:155], v[204:207], v[120:123]\n"
    "v_exp_f32 v238, v238\n"
    "v_mul_f32 v103, v255, v103\n"
    "v_mul_f32 v114, v255, v114\n"
    "v_mfma_f32_16x16x32_bf16 v[28:31], a[156:159], v[192:195], v[28:31]\n"
    "v_cvt_pk_bf16_f32 v193, v242, v243\n"
    "v_exp_f32 v239, v239\n"
    "v_mul_f32 v101, v255, v101\n"
    "v_cvt_pk_bf16_f32 v104, v104, v105\n"
    "v_mfma_f32_16x16x32_bf16 v[124:127], a[156:159], v[204:207], v[124:127]\n"
    "s_waitcnt lgkmcnt(3)\n"
    "buffer_load_dwordx4 v251, s[40:43], s56 offen offset:2048 lds\n"
    "v_fma_f32 v242, v144, s36, v216\n"
    "v_mfma_f32_16x16x32_bf16 v[168:171], a[96:99], a[16:19], 0\n"
    "v_cvt_pk_bf16_f32 v194, v236, v237\n"
    "v_cvt_pk_bf16_f32 v195, v238, v239\n"
    "v_exp_f32 v242, v242\n"
    "v_fma_f32 v243, v145, s36, v216\n"
    "v_mfma_f32_16x16x32_bf16 v[168:171], a[100:103], a[20:23], v[168:171]\n"
    "v_cvt_pk_bf16_f32 v192, v240, v241\n"
    "v_exp_f32 v243, v243\n"
    "v_fma_f32 v236, v146, s36, v216\n"
    "v_fma_f32 v237, v147, s36, v216\n"
    "v_mfma_f32_16x16x32_bf16 v[168:171], a[104:107], a[24:27], v[168:171]\n"
    "v_exp_f32 v236, v236\n"
    "v_fma_f32 v238, v148, s36, v216\n"
    "v_cvt_pk_bf16_f32 v97, v98, v99\n"
    "v_mfma_f32_16x16x32_bf16 v[224:227], a[160:163], v[192:195], v[224:227]\n"
    "v_exp_f32 v237, v237\n"
    "v_fma_f32 v239, v149, s36, v216\n"
    "v_fma_f32 v240, v150, s36, v216\n"
    "v_fma_f32 v241, v151, s36, v216\n"
    "v_mfma_f32_16x16x32_bf16 v[168:171], a[108:111], a[28:31], v[168:171]\n"
    "buffer_load_dwordx4 v252, s[40:43], s56 offen offset:3072 lds\n"
    "v_mfma_f32_16x16x32_bf16 v[172:175], a[112:115], a[16:19], 0\n"
    "s_add_u32 s58, s58, 16384\n"
    "ds_read_b64_tr_b16 a[144:145], v247 offset:512\n"
    "v_cvt_pk_bf16_f32 v201, v236, v237\n"
    "v_exp_f32 v238, v238\n"
    "v_mfma_f32_16x16x32_bf16 v[172:175], a[116:119], a[20:23], v[172:175]\n"
    "v_exp_f32 v239, v239\n"
    "v_fma_f32 v236, v152, s36, v220\n"
    "v_fma_f32 v237, v153, s36, v220\n"
    "v_mfma_f32_16x16x32_bf16 v[172:175], a[120:123], a[24:27], v[172:175]\n"
    "s_and_b32 m0, s59, 0xffff\n"
    "v_cvt_pk_bf16_f32 v200, v242, v243\n"
    "v_exp_f32 v240, v240\n"
    "v_cvt_pk_bf16_f32 v98, v100, v101\n"
    "v_mul_f32 v117, v255, v117\n"
    "v_mfma_f32_16x16x32_bf16 v[172:175], a[124:127], a[28:31], v[172:175]\n"
    "s_bitset1_b32 m0, 16\n"
    "v_exp_f32 v241, v241\n"
    "v_cvt_pk_bf16_f32 v99, v102, v103\n"
    "v_cvt_pk_bf16_f32 v105, v106, v107\n"
    "v_mfma_f32_16x16x32_bf16 v[160:163], a[96:99], a[0:3], 0\n"
    "s_cmp_lt_u32 s58, s17\n"
    "buffer_load_dwordx4 v251, s[44:47], s57 offen offset:0 lds\n"
    "v_mfma_f32_16x16x32_bf16 v[160:163], a[100:103], a[4:7], v[160:163]\n"
    "s_cselect_b32 s56, s18, 0\n"
    "v_exp_f32 v236, v236\n"
    "v_fma_f32 v242, v154, s36, v220\n"
    "v_mfma_f32_16x16x32_bf16 v[160:163], a[104:107], a[8:11], v[160:163]\n"
    "s_add_u32 s56, s56, s58\n"
    "v_cvt_pk_bf16_f32 v202, v238, v239\n"
    "v_cvt_pk_bf16_f32 v203, v240, v241\n"
    "v_exp_f32 v237, v237\n"
    "v_mfma_f32_16x16x32_bf16 v[160:163], a[108:111], a[12:15], v[160:163]\n"
    "v_exp_f32 v242, v242\n"
    "v_mul_f32 v112, v255, v112\n"
    "v_mul_f32 v115, v255, v115\n"
    "v_mfma_f32_16x16x32_bf16 v[224:227], a[168:171], v[200:203], v[224:227]\n"
    "v_fma_f32 v238, v155, s36, v220\n"
    "v_permlane16_swap_b32 v96, v98\n"
    "v_mul_f32 v109, v255, v109\n"
    "v_mul_f32 v111, v255, v111\n"
    "v_mfma_f32_16x16x32_bf16 v[164:167], a[112:115], a[0:3], 0\n"
    "v_exp_f32 v238, v238\n"
    "v_permlane16_swap_b32 v97, v99\n"
    "v_mul_f32 v123, v255, v123\n"
    "v_mfma_f32_16x16x32_bf16 v[164:167], a[116:119], a[4:7], v[164:167]\n"
    "buffer_load_dwordx4 v252, s[44:47], s57 offen offset:1024 lds\n"
    "v_mfma_f32_16x16x32_bf16 v[164:167], a[120:123], a[8:11], v[164:167]\n"
    "s_add_u32 s59, s59, 8192\n"
    "ds_read_b64_tr_b16 a[146:147], v247 offset:4608\n"
    "buffer_store_dwordx4 v[96:99], v253, s[52:55], s94 offen offset:0 sc1 nt\n"
    "v_cvt_pk_bf16_f32 v106, v108, v109\n"
    "v_mfma_f32_16x16x32_bf16 v[164:167], a[124:127], a[12:15], v[164:167]\n"
    "s_add_u32 s59, s59, s93\n"
    "v_cvt_pk_bf16_f32 v107, v110, v111\n"
    "v_mul_f32 v113, v255, v113\n"
    "v_mul_f32 v119, v255, v119\n"
    "v_mul_f32 v125, v255, v125\n"
    "v_mfma_f32_16x16x32_bf16 v[176:179], a[96:99], a[64:67], 0\n"
    "s_cmp_lt_u32 s59, s17\n"
    "v_fma_f32 v239, v156, s36, v220\n"
    "v_fma_f32 v240, v157, s36, v220\n"
    "v_permlane16_swap_b32 v104, v106\n"
    "v_cvt_pk_bf16_f32 v112, v112, v113\n"
    "v_mfma_f32_16x16x32_bf16 v[176:179], a[100:103], a[68:71], v[176:179]\n"
    "s_cselect_b32 s57, s18, 0\n"
    "ds_read_b64_tr_b16 a[148:149], v250 offset:512\n"
    "v_exp_f32 v239, v239\n"
    "v_mul_f32 v120, v255, v120\n"
    "v_mfma_f32_16x16x32_bf16 v[176:179], a[104:107], a[72:75], v[176:179]\n"
    "s_add_u32 s57, s57, s59\n"
    "ds_read_b64_tr_b16 a[150:151], v250 offset:4608\n"
    "v_exp_f32 v240, v240\n"
    "v_permlane16_swap_b32 v105, v107\n"
    "v_mul_f32 v116, v255, v116\n"
    "v_mfma_f32_16x16x32_bf16 v[176:179], a[108:111], a[76:79], v[176:179]\n"
    "ds_read_b64_tr_b16 a[152:153], v247 offset:768\n"
    "ds_read_b64_tr_b16 a[154:155], v247 offset:4864\n"
    "v_fma_f32 v241, v158, s36, v220\n"
    "v_fma_f32 v243, v159, s36, v220\n"
    "v_cvt_pk_bf16_f32 v113, v114, v115\n"
    "v_mfma_f32_16x16x32_bf16 v[180:183], a[112:115], a[64:67], 0\n"
    "buffer_store_dwordx4 v[104:107], v253, s[52:55], s94 offen offset:64 sc1 nt\n"
    "v_cvt_pk_bf16_f32 v205, v242, v238\n"
    "v_cvt_pk_bf16_f32 v114, v116, v117\n"
    "v_mfma_f32_16x16x32_bf16 v[180:183], a[116:119], a[68:71], v[180:183]\n"
    "v_cvt_pk_bf16_f32 v204, v236, v237\n"
    "v_exp_f32 v241, v241\n"
    "v_mul_f32 v121, v255, v121\n"
    "v_mfma_f32_16x16x32_bf16 v[180:183], a[120:123], a[72:75], v[180:183]\n"
    "ds_read_b64_tr_b16 a[156:157], v250 offset:768\n"
    "v_exp_f32 v243, v243\n"
    "v_mul_f32 v118, v255, v118\n"
    "v_mfma_f32_16x16x32_bf16 v[180:183], a[124:127], a[76:79], v[180:183]\n"
    "v_cvt_pk_bf16_f32 v207, v241, v243\n"
    "v_fma_f32 v232, v168, s36, v212\n"
    "v_cvt_pk_bf16_f32 v115, v118, v119\n"
    "v_mul_f32 v126, v255, v126\n"
    "v_mfma_f32_16x16x32_bf16 v[184:187], a[96:99], a[80:83], 0\n"
    "ds_read_b64_tr_b16 a[158:159], v250 offset:4864\n"
    "v_cvt_pk_bf16_f32 v206, v239, v240\n"
    "v_permlane16_swap_b32 v112, v114\n"
    "v_mfma_f32_16x16x32_bf16 v[184:187], a[100:103], a[84:87], v[184:187]\n"
    "v_exp_f32 v232, v232\n"
    "v_permlane16_swap_b32 v113, v115\n"
    "v_mul_f32 v124, v255, v124\n"
    "v_mfma_f32_16x16x32_bf16 v[224:227], a[172:175], v[204:207], v[224:227]\n"
    "buffer_store_dwordx4 v[112:115], v253, s[52:55], s94 offen offset:128 sc1 nt\n"
    "v_mul_f32 v122, v255, v122\n"
    "v_mfma_f32_16x16x32_bf16 v[184:187], a[104:107], a[88:91], v[184:187]\n"
    "v_fma_f32 v233, v169, s36, v212\n"
    "v_mul_f32 v127, v255, v127\n"
    "v_cvt_pk_bf16_f32 v120, v120, v121\n"
    "v_cvt_pk_bf16_f32 v121, v122, v123\n"
    "v_mfma_f32_16x16x32_bf16 v[184:187], a[108:111], a[92:95], v[184:187]\n"
    "v_add_u32 v246, s60, v248\n"
    "v_fma_f32 v234, v170, s36, v212\n"
    "v_cvt_pk_bf16_f32 v122, v124, v125\n"
    "v_mfma_f32_16x16x32_bf16 v[188:191], a[112:115], a[80:83], 0\n"
    "v_exp_f32 v233, v233\n"
    "v_fma_f32 v235, v171, s36, v212\n"
    "v_cvt_pk_bf16_f32 v123, v126, v127\n"
    "v_mfma_f32_16x16x32_bf16 v[188:191], a[116:119], a[84:87], v[188:191]\n"
    "s_sub_u32 s62, s62, s20\n"
    "v_exp_f32 v234, v234\n"
    "v_max3_f32 v241, v224, v225, v226\n"
    "v_max_f32 v241, v241, v227\n"
    "v_mfma_f32_16x16x32_bf16 v[188:191], a[120:123], a[88:91], v[188:191]\n"
    "s_mov_b32 s22, 2\n"
    "v_cmp_nge_f32 vcc, s37, v241\n"
    "v_permlane16_swap_b32 v120, v122\n"
    "v_permlane16_swap_b32 v121, v123\n"
    "v_mfma_f32_16x16x32_bf16 v[188:191], a[124:127], a[92:95], v[188:191]\n"
    "s_waitcnt lgkmcnt(0)\n"
    "buffer_store_dwordx4 v[120:123], v253, s[52:55], s94 offen offset:192 sc1 nt\n"
    "v_exp_f32 v235, v235\n"
    "s_cbranch_vccnz L_rc_T1e\n"
    "L_rr_T1e:\n"
    "s_branch L_L2\n"
    "L_T2b:\n"
    "s_cmp_eq_i32 s95, 0\n"
    "s_cselect_b64 s[72:73], s[64:65], 0\n"
    "s_cselect_b64 s[74:75], s[66:67], 0\n"
    "s_cselect_b64 s[76:77], s[68:69], 0\n"
    "s_cselect_b64 s[78:79], s[70:71], 0\n"
    "s_cselect_b64 s[80:81], -1, s[64:65]\n"
    "s_cselect_b64 s[82:83], -1, s[66:67]\n"
    "s_cselect_b64 s[88:89], -1, s[68:69]\n"
    "s_cselect_b64 s[90:91], -1, s[70:71]\n"
    "v_mfma_f32_16x16x32_bf16 v[32:35], a[128:131], v[196:199], v[32:35]\n"
    "v_fma_f32 v236, v140, s36, v212\n"
    "v_fma_f32 v237, v141, s36, v212\n"
    "v_mfma_f32_16x16x32_bf16 v[36:39], a[132:135], v[196:199], v[36:39]\n"
    "v_exp_f32 v236, v236\n"
    "v_fma_f32 v238, v142, s36, v212\n"
    "v_mfma_f32_16x16x32_bf16 v[40:43], a[136:139], v[196:199], v[40:43]\n"
    "s_waitcnt vmcnt(16)\n"
    "v_exp_f32 v237, v237\n"
    "v_mfma_f32_16x16x32_bf16 v[44:47], a[140:143], v[196:199], v[44:47]\n"
    "s_barrier\n"
    "v_cndmask_b32 v128, v128, v254, s[72:73]\n"
    "v_cndmask_b32 v129, v129, v254, s[74:75]\n"
    "v_cndmask_b32 v130, v130, v254, s[76:77]\n"
    "v_cndmask_b32 v131, v131, v254, s[78:79]\n"
    "v_mfma_f32_16x16x32_bf16 v[48:51], a[144:147], v[196:199], v[48:51]\n"
    "s_add_u32 s61, s61, 32768\n"
    "ds_read_b128 a[96:99], v246 offset:16384\n"
    "v_cndmask_b32 v132, v132, v254, s[80:81]\n"
    "v_cndmask_b32 v133, v133, v254, s[82:83]\n"
    "v_mfma_f32_16x16x32_bf16 v[52:55], a[148:151], v[196:199], v[52:55]\n"
    "s_and_b32 s61, s61, 0xffff\n"
    "v_fma_f32 v239, v143, s36, v212\n"
    "v_fma_f32 v240, v128, s36, v208\n"
    "v_fma_f32 v241, v129, s36, v208\n"
    "v_mfma_f32_16x16x32_bf16 v[56:59], a[152:155], v[196:199], v[56:59]\n"
    "s_cmp_lt_i32 s96, 0\n"
    "ds_read_b128 a[100:103], v246 offset:16640\n"
    "v_exp_f32 v238, v238\n"
    "v_mfma_f32_16x16x32_bf16 v[60:63], a[156:159], v[196:199], v[60:63]\n"
    "s_cselect_b32 s84, s35, 0x7f800000\n"
    "v_cvt_pk_bf16_f32 v198, v236, v237\n"
    "v_exp_f32 v239, v239\n"
    "v_mfma_f32_16x16x32_bf16 v[0:3], a[128:131], v[192:195], v[0:3]\n"
    "s_cmp_lt_i32 s19, 0\n"
    "v_cvt_pk_bf16_f32 v199, v238, v239\n"
    "v_exp_f32 v240, v240\n"
    "v_mfma_f32_16x16x32_bf16 v[4:7], a[132:135], v[192:195], v[4:7]\n"
    "s_cselect_b32 s85, s35, 0x7f800000\n"
    "v_min_f32 v144, s84, v144\n"
    "v_fma_f32 v236, v130, s36, v208\n"
    "v_fma_f32 v237, v131, s36, v208\n"
    "v_mfma_f32_16x16x32_bf16 v[8:11], a[136:139], v[192:195], v[8:11]\n"
    "v_cvt_pk_bf16_f32 v196, v232, v233\n"
    "v_cvt_pk_bf16_f32 v197, v234, v235\n"
    "v_exp_f32 v241, v241\n"
    "v_mfma_f32_16x16x32_bf16 v[12:15], a[140:143], v[192:195], v[12:15]\n"
    "ds_read_b128 a[104:107], v246 offset:16896\n"
    "v_cndmask_b32 v134, v134, v254, s[88:89]\n"
    "v_cndmask_b32 v135, v135, v254, s[90:91]\n"
    "v_mfma_f32_16x16x32_bf16 v[224:227], a[164:167], v[196:199], v[228:231]\n"
    "ds_read_b128 a[108:111], v246 offset:17152\n"
    "v_min_f32 v145, s84, v145\n"
    "v_min_f32 v148, s84, v148\n"
    "v_mfma_f32_16x16x32_bf16 v[16:19], a[144:147], v[192:195], v[16:19]\n"
    "v_exp_f32 v236, v236\n"
    "v_fma_f32 v238, v132, s36, v208\n"
    "v_fma_f32 v239, v133, s36, v208\n"
    "v_mfma_f32_16x16x32_bf16 v[20:23], a[148:151], v[192:195], v[20:23]\n"
    "v_min_f32 v146, s84, v146\n"
    "v_min_f32 v149, s84, v149\n"
    "v_exp_f32 v237, v237\n"
    "v_mfma_f32_16x16x32_bf16 v[24:27], a[152:155], v[192:195], v[24:27]\n"
    "v_exp_f32 v238, v238\n"
    "v_fma_f32 v242, v134, s36, v208\n"
    "v_mfma_f32_16x16x32_bf16 v[28:31], a[156:159], v[192:195], v[28:31]\n"
    "v_cvt_pk_bf16_f32 v193, v236, v237\n"
    "v_exp_f32 v239, v239\n"
    "v_mfma_f32_16x16x32_bf16 a[176:179], a[128:131], v[200:203], a[176:179]\n"
    "v_exp_f32 v242, v242\n"
    "v_fma_f32 v236, v135, s36, v208\n"
    "v_mfma_f32_16x16x32_bf16 a[208:211], a[128:131], v[204:207], a[208:211]\n"
    "v_min_f32 v147, s84, v147\n"
    "v_cvt_pk_bf16_f32 v194, v238, v239\n"
    "v_exp_f32 v236, v236\n"
    "v_mfma_f32_16x16x32_bf16 a[180:183], a[132:135], v[200:203], a[180:183]\n"
    "ds_read_b128 a[112:115], v246 offset:20480\n"
    "v_cvt_pk_bf16_f32 v192, v240, v241\n"
    "v_cvt_pk_bf16_f32 v195, v242, v236\n"
    "v_mfma_f32_16x16x32_bf16 a[212:215], a[132:135], v[204:207], a[212:215]\n"
    "v_add_u32 v247, s61, v249\n"
    "v_xor_b32 v250, 32, v247\n"
    "v_min_f32 v150, s84, v150\n"
    "v_mfma_f32_16x16x32_bf16 v[224:227], a[160:163], v[192:195], v[224:227]\n"
    "ds_read_b64_tr_b16 a[128:129], v247 offset:0\n"
    "ds_read_b64_tr_b16 a[130:131], v247 offset:4096\n"
    "ds_read_b64_tr_b16 a[132:133], v250 offset:0\n"
    "v_fma_f32 v236, v144, s36, v216\n"
    "v_mfma_f32_16x16x32_bf16 a[184:187], a[136:139], v[200:203], a[184:187]\n"
    "ds_read_b64_tr_b16 a[134:135], v250 offset:4096\n"
    "v_min_f32 v151, s84, v151\n"
    "v_fma_f32 v237, v145, s36, v216\n"
    "v_mfma_f32_16x16x32_bf16 a[216:219], a[136:139], v[204:207], a[216:219]\n"
    "v_exp_f32 v236, v236\n"
    "v_fma_f32 v238, v146, s36, v216\n"
    "v_fma_f32 v239, v147, s36, v216\n"
    "v_mfma_f32_16x16x32_bf16 a[188:191], a[140:143], v[200:203], a[188:191]\n"
    "ds_read_b64_tr_b16 a[136:137], v247 offset:256\n"
    "v_exp_f32 v237, v237\n"
    "v_mfma_f32_16x16x32_bf16 a[220:223], a[140:143], v[204:207], a[220:223]\n"
    "v_exp_f32 v238, v238\n"
    "v_fma_f32 v240, v148, s36, v216\n"
    "v_mfma_f32_16x16x32_bf16 a[192:195], a[144:147], v[200:203], a[192:195]\n"
    "ds_read_b128 a[116:119], v246 offset:20736\n"
    "v_exp_f32 v239, v239\n"
    "v_mfma_f32_16x16x32_bf16 a[224:227], a[144:147], v[204:207], a[224:227]\n"
    "ds_read_b64_tr_b16 a[138:139], v247 offset:4352\n"
    "v_fma_f32 v241, v149, s36, v216\n"
    "v_fma_f32 v242, v150, s36, v216\n"
    "v_mfma_f32_16x16x32_bf16 a[196:199], a[148:151], v[200:203], a[196:199]\n"
    "ds_read_b64_tr_b16 a[140:141], v250 offset:256\n"
    "v_exp_f32 v240, v240\n"
    "v_mfma_f32_16x16x32_bf16 a[228:231], a[148:151], v[204:207], a[228:231]\n"
    "v_exp_f32 v241, v241\n"
    "v_fma_f32 v243, v151, s36, v216\n"
    "v_mfma_f32_16x16x32_bf16 a[200:203], a[152:155], v[200:203], a[200:203]\n"
    "ds_read_b128 a[120:123], v246 offset:20992\n"
    "v_exp_f32 v242, v242\n"
    "v_mfma_f32_16x16x32_bf16 a[232:235], a[152:155], v[204:207], a[232:235]\n"
    "ds_read_b64_tr_b16 a[142:143], v250 offset:4352\n"
    "v_exp_f32 v243, v243\n"
    "v_mfma_f32_16x16x32_bf16 a[204:207], a[156:159], v[200:203], a[204:207]\n"
    "ds_read_b128 a[124:127], v246 offset:21248\n"
    "ds_read_b64_tr_b16 a[144:145], v247 offset:512\n"
    "v_cvt_pk_bf16_f32 v200, v236, v237\n"
    "v_mfma_f32_16x16x32_bf16 a[236:239], a[156:159], v[204:207], a[236:239]\n"
    "s_waitcnt lgkmcnt(7)\n"
    "v_cvt_pk_bf16_f32 v202, v240, v241\n"
    "v_cvt_pk_bf16_f32 v203, v242, v243\n"
    "v_fma_f32 v236, v152, s36, v220\n"
    "v_fma_f32 v237, v153, s36, v220\n"
    "v_mfma_f32_16x16x32_bf16 v[168:171], a[96:99], a[16:19], 0\n"
    "v_cvt_pk_bf16_f32 v201, v238, v239\n"
    "v_exp_f32 v236, v236\n"
    "v_mfma_f32_16x16x32_bf16 v[168:171], a[100:103], a[20:23], v[168:171]\n"
    "ds_read_b64_tr_b16 a[146:147], v247 offset:4608\n"
    "v_fma_f32 v238, v154, s36, v220\n"
    "v_fma_f32 v239, v155, s36, v220\n"
    "v_mfma_f32_16x16x32_bf16 v[168:171], a[104:107], a[24:27], v[168:171]\n"
    "ds_read_b64_tr_b16 a[148:149], v250 offset:512\n"
    "v_exp_f32 v237, v237\n"
    "v_mfma_f32_16x16x32_bf16 v[224:227], a[168:171], v[200:203], v[224:227]\n"
    "ds_read_b64_tr_b16 a[150:151], v250 offset:4608\n"
    "v_fma_f32 v240, v156, s36, v220\n"
    "v_fma_f32 v241, v157, s36, v220\n"
    "v_fma_f32 v242, v158, s36, v220\n"
    "v_mfma_f32_16x16x32_bf16 v[168:171], a[108:111], a[28:31], v[168:171]\n"
    "ds_read_b64_tr_b16 a[152:153], v247 offset:768\n"
    "v_exp_f32 v238, v238\n"
    "v_mfma_f32_16x16x32_bf16 v[172:175], a[112:115], a[16:19], 0\n"
    "s_waitcnt lgkmcnt(4)\n"
    "v_exp_f32 v239, v239\n"
    "v_mfma_f32_16x16x32_bf16 v[172:175], a[116:119], a[20:23], v[172:175]\n"
    "v_cvt_pk_bf16_f32 v204, v236, v237\n"
    "v_exp_f32 v240, v240\n"
    "v_fma_f32 v243, v159, s36, v220\n"
    "v_mfma_f32_16x16x32_bf16 v[172:175], a[120:123], a[24:27], v[172:175]\n"
    "v_min_f32 v168, s85, v168\n"
    "v_exp_f32 v241, v241\n"
    "v_mfma_f32_16x16x32_bf16 v[172:175], a[124:127], a[28:31], v[172:175]\n"
    "v_cvt_pk_bf16_f32 v205, v238, v239\n"
    "v_exp_f32 v242, v242\n"
    "v_mfma_f32_16x16x32_bf16 v[184:187], a[96:99], a[80:83], 0\n"
    "ds_read_b64_tr_b16 a[154:155], v247 offset:4864\n"
    "v_exp_f32 v243, v243\n"
    "v_mfma_f32_16x16x32_bf16 v[184:187], a[100:103], a[84:87], v[184:187]\n"
    "ds_read_b64_tr_b16 a[156:157], v250 offset:768\n"
    "v_cvt_pk_bf16_f32 v206, v240, v241\n"
    "v_cvt_pk_bf16_f32 v207, v242, v243\n"
    "v_mfma_f32_16x16x32_bf16 v[184:187], a[104:107], a[88:91], v[184:187]\n"
    "ds_read_b64_tr_b16 a[158:159], v250 offset:4864\n"
    "v_min_f32 v169, s85, v169\n"
    "v_min_f32 v170, s85, v170\n"
    "v_mfma_f32_16x16x32_bf16 v[224:227], a[172:175], v[204:207], v[224:227]\n"
    "v_min_f32 v171, s85, v171\n"
    "v_fma_f32 v232, v168, s36, v212\n"
    "v_fma_f32 v233, v169, s36, v212\n"
    "v_mfma_f32_16x16x32_bf16 v[184:187], a[108:111], a[92:95], v[184:187]\n"
    "v_exp_f32 v232, v232\n"
    "v_mfma_f32_16x16x32_bf16 v[188:191], a[112:115], a[80:83], 0\n"
    "v_exp_f32 v233, v233\n"
    "v_mfma_f32_16x16x32_bf16 v[188:191], a[116:119], a[84:87], v[188:191]\n"
    "v_fma_f32 v234, v170, s36, v212\n"
    "v_fma_f32 v235, v171, s36, v212\n"
    "v_mfma_f32_16x16x32_bf16 v[188:191], a[120:123], a[88:91], v[188:191]\n"
    "v_exp_f32 v234, v234\n"
    "v_max3_f32 v241, v224, v225, v226\n"
    "v_max_f32 v241, v241, v227\n"
    "v_cmp_nge_f32 vcc, s37, v241\n"
    "v_mfma_f32_16x16x32_bf16 v[188:191], a[124:127], a[92:95], v[188:191]\n"
    "s_waitcnt lgkmcnt(0)\n"
    "v_exp_f32 v235, v235\n"
    "s_cbranch_vccnz L_rc_T2b\n"
    "L_rr_T2b:\n"
    "s_lshl_b32 s60, s38, 14\n"
    "s_lshl_b32 s58, s38, 10\n"
    "s_add_u32 s58, s58, 159744\n"
    "s_cmp_lt_i32 s19, 0\n"
    "s_cselect_b32 s85, s35, 0x7f800000\n"
    "v_mfma_f32_16x16x32_bf16 a[176:179], a[128:131], v[200:203], a[176:179]\n"
    "v_accvgpr_read_b32 v253, a246\n"
    "v_mov_b32 v244, v226\n"
    "v_mfma_f32_16x16x32_bf16 a[180:183], a[132:135], v[200:203], a[180:183]\n"
    "v_add_u32 v246, s60, v253\n"
    "v_mov_b32 v245, v216\n"
    "v_mfma_f32_16x16x32_bf16 a[184:187], a[136:139], v[200:203], a[184:187]\n"
    "s_waitcnt vmcnt(0)\n"
    "v_add_u32 v248, s58, v253\n"
    "v_mfma_f32_16x16x32_bf16 a[188:191], a[140:143], v[200:203], a[188:191]\n"
    "s_barrier\n"
    "ds_read_b64_tr_b16 a[96:97], v247 offset:16384\n"
    "ds_write_b128 v246, a[176:179] offset:0\n"
    "v_mfma_f32_16x16x32_bf16 a[192:195], a[144:147], v[200:203], a[192:195]\n"
    "ds_read_b64_tr_b16 a[98:99], v247 offset:20480\n"
    "v_min_f32 v172, s85, v172\n"
    "v_min_f32 v173, s85, v173\n"
    "v_mfma_f32_16x16x32_bf16 a[196:199], a[148:151], v[200:203], a[196:199]\n"
    "v_min_f32 v174, s85, v174\n"
    "v_min_f32 v175, s85, v175\n"
    "v_fma_f32 v236, v172, s36, v212\n"
    "v_mfma_f32_16x16x32_bf16 a[200:203], a[152:155], v[200:203], a[200:203]\n"
    "s_cmp_eq_i32 s97, 0\n"
    "s_cselect_b64 s[72:73], s[64:65], 0\n"
    "s_cselect_b64 s[74:75], s[66:67], 0\n"
    "s_cselect_b64 s[76:77], s[68:69], 0\n"
    "s_cselect_b64 s[78:79], s[70:71], 0\n"
    "s_cselect_b64 s[80:81], -1, s[64:65]\n"
    "s_cselect_b64 s[82:83], -1, s[66:67]\n"
    "s_cselect_b64 s[88:89], -1, s[68:69]\n"
    "s_cselect_b64 s[90:91], -1, s[70:71]\n"
    "v_mfma_f32_16x16x32_bf16 a[204:207], a[156:159], v[200:203], a[204:207]\n"
    "ds_read_b64_tr_b16 a[100:101], v250 offset:16384\n"
    "ds_write_b128 v246, a[180:183] offset:1024\n"
    "v_mfma_f32_16x16x32_bf16 v[32:35], a[128:131], v[196:199], v[32:35]\n"
    "v_cndmask_b32 v188, v188, v254, s[80:81]\n"
    "v_exp_f32 v236, v236\n"
    "v_fma_f32 v237, v173, s36, v212\n"
    "v_mfma_f32_16x16x32_bf16 v[36:39], a[132:135], v[196:199], v[36:39]\n"
    "ds_read_b64_tr_b16 a[102:103], v250 offset:20480\n"
    "v_exp_f32 v237, v237\n"
    "v_mfma_f32_16x16x32_bf16 v[40:43], a[136:139], v[196:199], v[40:43]\n"
    "ds_read_b64_tr_b16 a[104:105], v247 offset:16640\n"
    "ds_write_b128 v246, a[184:187] offset:2048\n"
    "v_mfma_f32_16x16x32_bf16 v[44:47], a[140:143], v[196:199], v[44:47]\n"
    "v_cndmask_b32 v184, v184, v254, s[72:73]\n"
    "v_cndmask_b32 v185, v185, v254, s[74:75]\n"
    "v_cndmask_b32 v189, v189, v254, s[82:83]\n"
    "v_mfma_f32_16x16x32_bf16 v[48:51], a[144:147], v[196:199], v[48:51]\n"
    "ds_read_b64_tr_b16 a[106:107], v247 offset:20736\n"
    "v_cndmask_b32 v186, v186, v254, s[76:77]\n"
    "v_cndmask_b32 v187, v187, v254, s[78:79]\n"
    "v_fma_f32 v238, v174, s36, v212\n"
    "v_mfma_f32_16x16x32_bf16 v[52:55], a[148:151], v[196:199], v[52:55]\n"
    "ds_write_b128 v246, a[188:191] offset:3072\n"
    "v_fma_f32 v239, v175, s36, v212\n"
    "v_fma_f32 v240, v184, s36, v220\n"
    "v_mfma_f32_16x16x32_bf16 v[56:59], a[152:155], v[196:199], v[56:59]\n"
    "ds_read_b64_tr_b16 a[108:109], v250 offset:16640\n"
    "ds_read_b64_tr_b16 a[110:111], v250 offset:20736\n"
    "v_fma_f32 v241, v185, s36, v220\n"
    "v_mfma_f32_16x16x32_bf16 v[60:63], a[156:159], v[196:199], v[60:63]\n"
    "v_exp_f32 v238, v238\n"
    "v_fma_f32 v242, v186, s36, v220\n"
    "v_mfma_f32_16x16x32_bf16 a[208:211], a[128:131], v[204:207], a[208:211]\n"
    "v_cndmask_b32 v190, v190, v254, s[88:89]\n"
    "v_exp_f32 v239, v239\n"
    "s_nop 0\n"
    "v_cvt_pk_bf16_f32 v199, v238, v239\n"
    "v_mfma_f32_16x16x32_bf16 a[212:215], a[132:135], v[204:207], a[212:215]\n"
    "ds_write_b128 v246, a[192:195] offset:4096\n"
    "v_cvt_pk_bf16_f32 v198, v236, v237\n"
    "v_fma_f32 v238, v187, s36, v220\n"
    "v_mfma_f32_16x16x32_bf16 a[216:219], a[136:139], v[204:207], a[216:219]\n"
    "v_cvt_pk_bf16_f32 v196, v232, v233\n"
    "v_exp_f32 v240, v240\n"
    "v_mfma_f32_16x16x32_bf16 a[220:223], a[140:143], v[204:207], a[220:223]\n"
    "ds_read_b64_tr_b16 a[112:113], v247 offset:16896\n"
    "v_cndmask_b32 v191, v191, v254, s[90:91]\n"
    "v_cvt_pk_bf16_f32 v197, v234, v235\n"
    "s_nop 1\n"
    "v_mfma_f32_16x16x32_bf16 v[228:231], a[164:167], v[196:199], v[224:227]\n"
    "ds_write_b128 v246, a[196:199] offset:5120\n"
    "v_exp_f32 v241, v241\n"
    "v_mfma_f32_16x16x32_bf16 a[224:227], a[144:147], v[204:207], a[224:227]\n"
    "v_exp_f32 v242, v242\n"
    "v_exp_f32 v238, v238\n"
    "v_mfma_f32_16x16x32_bf16 a[228:231], a[148:151], v[204:207], a[228:231]\n"
    "ds_read_b64_tr_b16 a[114:115], v247 offset:20992\n"
    "ds_read_b64_tr_b16 a[116:117], v250 offset:16896\n"
    "v_fma_f32 v236, v188, s36, v220\n"
    "v_fma_f32 v237, v189, s36, v220\n"
    "v_mfma_f32_16x16x32_bf16 a[232:235], a[152:155], v[204:207], a[232:235]\n"
    "ds_write_b128 v246, a[200:203] offset:6144\n"
    "v_fma_f32 v239, v190, s36, v220\n"
    "v_mfma_f32_16x16x32_bf16 a[236:239], a[156:159], v[204:207], a[236:239]\n"
    "ds_read_b64_tr_b16 a[118:119], v250 offset:20992\n"
    "v_exp_f32 v236, v236\n"
    "v_mfma_f32_16x16x32_bf16 v[0:3], a[128:131], v[192:195], v[0:3]\n"
    "ds_read_b64_tr_b16 a[120:121], v247 offset:17152\n"
    "ds_read_b64_tr_b16 a[122:123], v247 offset:21248\n"
    "ds_read_b64_tr_b16 a[124:125], v250 offset:17152\n"
    "v_cvt_pk_bf16_f32 v205, v242, v238\n"
    "v_mfma_f32_16x16x32_bf16 v[4:7], a[132:135], v[192:195], v[4:7]\n"
    "v_cvt_pk_bf16_f32 v204, v240, v241\n"
    "v_exp_f32 v237, v237\n"
    "v_fma_f32 v238, v191, s36, v220\n"
    "v_mfma_f32_16x16x32_bf16 v[8:11], a[136:139], v[192:195], v[8:11]\n"
    "ds_read_b64_tr_b16 a[126:127], v250 offset:21248\n"
    "v_cvt_pk_bf16_f32 v206, v236, v237\n"
    "v_exp_f32 v239, v239\n"
    "v_mfma_f32_16x16x32_bf16 v[12:15], a[140:143], v[192:195], v[12:15]\n"
    "v_exp_f32 v238, v238\n"
    "s_nop 0\n"
    "v_cvt_pk_bf16_f32 v207, v239, v238\n"
    "s_nop 1\n"
    "v_mfma_f32_16x16x32_bf16 v[228:231], a[172:175], v[204:207], v[228:231]\n"
    "ds_write_b128 v246, a[204:207] offset:7168\n"
    "v_mfma_f32_16x16x32_bf16 v[16:19], a[144:147], v[192:195], v[16:19]\n"
    "ds_write_b64 v248, v[244:245]\n"
    "v_mfma_f32_16x16x32_bf16 v[20:23], a[148:151], v[192:195], v[20:23]\n"
    "v_mfma_f32_16x16x32_bf16 v[24:27], a[152:155], v[192:195], v[24:27]\n"
    "v_mfma_f32_16x16x32_bf16 v[28:31], a[156:159], v[192:195], v[28:31]\n"
    "s_nop 1\n"
    "v_max3_f32 v241, v228, v229, v230\n"
    "v_max_f32 v241, v241, v231\n"
    "v_cmp_nge_f32 vcc, s37, v241\n"
    "s_cbranch_vccnz L_rc_T2c\n"
    "L_rr_T2c:\n"
    "s_waitcnt lgkmcnt(0)\n"
    "v_xor_b32 v247, 32768, v246\n"
    "v_xor_b32 v249, 2048, v248\n"
    "s_barrier\n"
    "v_mfma_f32_16x16x32_bf16 a[208:211], a[96:99], v[204:207], a[208:211]\n"
    "ds_read_b64 v[92:93], v249 offset:0\n"
    "v_accvgpr_read_b32 v240, a246\n"
    "v_accvgpr_read_b32 v241, a247\n"
    "v_mfma_f32_16x16x32_bf16 a[212:215], a[100:103], v[204:207], a[212:215]\n"
    "ds_read_b128 v[128:131], v247 offset:0\n"
    "v_accvgpr_read_b32 v242, a248\n"
    "v_mfma_f32_16x16x32_bf16 a[216:219], a[104:107], v[204:207], a[216:219]\n"
    "ds_read_b128 v[132:135], v247 offset:1024\n"
    "v_mfma_f32_16x16x32_bf16 a[220:223], a[108:111], v[204:207], a[220:223]\n"
    "ds_read_b128 v[136:139], v247 offset:2048\n"
    "v_mfma_f32_16x16x32_bf16 a[224:227], a[112:115], v[204:207], a[224:227]\n"
    "ds_read_b128 v[140:143], v247 offset:3072\n"
    "v_mfma_f32_16x16x32_bf16 a[228:231], a[116:119], v[204:207], a[228:231]\n"
    "ds_read_b128 v[144:147], v247 offset:4096\n"
    "v_mfma_f32_16x16x32_bf16 a[232:235], a[120:123], v[204:207], a[232:235]\n"
    "ds_read_b128 v[148:151], v247 offset:5120\n"
    "v_mfma_f32_16x16x32_bf16 a[236:239], a[124:127], v[204:207], a[236:239]\n"
    "ds_read_b128 v[152:155], v247 offset:6144\n"
    "ds_read_b128 v[156:159], v247 offset:7168\n"
    "v_mov_b32 v244, v231\n"
    "v_mov_b32 v245, v220\n"
    "ds_write_b64 v248, v[244:245] offset:8\n"
    "v_mfma_f32_16x16x32_bf16 v[32:35], a[96:99], v[196:199], v[32:35]\n"
    "ds_write_b128 v246, a[208:211] offset:8192\n"
    "v_mfma_f32_16x16x32_bf16 v[36:39], a[100:103], v[196:199], v[36:39]\n"
    "v_mfma_f32_16x16x32_bf16 v[40:43], a[104:107], v[196:199], v[40:43]\n"
    "v_mfma_f32_16x16x32_bf16 v[44:47], a[108:111], v[196:199], v[44:47]\n"
    "ds_write_b128 v246, a[212:215] offset:9216\n"
    "v_mfma_f32_16x16x32_bf16 v[48:51], a[112:115], v[196:199], v[48:51]\n"
    "v_mfma_f32_16x16x32_bf16 v[52:55], a[116:119], v[196:199], v[52:55]\n"
    "v_mfma_f32_16x16x32_bf16 v[56:59], a[120:123], v[196:199], v[56:59]\n"
    "ds_write_b128 v246, a[216:219] offset:10240\n"
    "v_mfma_f32_16x16x32_bf16 v[60:63], a[124:127], v[196:199], v[60:63]\n"
    "s_waitcnt lgkmcnt(4)\n"
    "ds_write_b128 v246, a[220:223] offset:11264\n"
    "v_accvgpr_read_b32 v243, a245\n"
    "v_min_f32 v84, v208, v93\n"
    "v_sub_f32 v85, v84, v208\n"
    "v_sub_f32 v86, v84, v93\n"
    "v_exp_f32 v85, v85\n"
    "v_exp_f32 v86, v86\n"
    "s_nop 0\n"
    "v_mul_f32 v87, v85, v228\n"
    "v_fma_f32 v87, v86, v92, v87\n"
    "v_rcp_f32 v87, v87\n"
    "s_nop 0\n"
    "v_mul_f32 v68, v85, v87\n"
    "ds_write_b128 v246, a[224:227] offset:12288\n"
    "v_mul_f32 v76, v86, v87\n"
    "v_pk_mul_f32 v[0:1], v[0:1], v[68:69] op_sel_hi:[1,0]\n"
    "v_pk_mul_f32 v[2:3], v[2:3], v[68:69] op_sel_hi:[1,0]\n"
    "v_pk_mul_f32 v[4:5], v[4:5], v[68:69] op_sel_hi:[1,0]\n"
    "v_pk_mul_f32 v[6:7], v[6:7], v[68:69] op_sel_hi:[1,0]\n"
    "v_pk_mul_f32 v[8:9], v[8:9], v[68:69] op_sel_hi:[1,0]\n"
    "v_pk_mul_f32 v[10:11], v[10:11], v[68:69] op_sel_hi:[1,0]\n"
    "v_pk_mul_f32 v[12:13], v[12:13], v[68:69] op_sel_hi:[1,0]\n"
    "v_pk_mul_f32 v[14:15], v[14:15], v[68:69] op_sel_hi:[1,0]\n"
    "v_pk_mul_f32 v[16:17], v[16:17], v[68:69] op_sel_hi:[1,0]\n"
    "v_pk_mul_f32 v[18:19], v[18:19], v[68:69] op_sel_hi:[1,0]\n"
    "v_pk_mul_f32 v[20:21], v[20:21], v[68:69] op_sel_hi:[1,0]\n"
    "ds_write_b128 v246, a[228:231] offset:13312\n"
    "v_pk_mul_f32 v[22:23], v[22:23], v[68:69] op_sel_hi:[1,0]\n"
    "v_pk_mul_f32 v[24:25], v[24:25], v[68:69] op_sel_hi:[1,0]\n"
    "v_pk_mul_f32 v[26:27], v[26:27], v[68:69] op_sel_hi:[1,0]\n"
    "v_pk_mul_f32 v[28:29], v[28:29], v[68:69] op_sel_hi:[1,0]\n"
    "v_pk_mul_f32 v[30:31], v[30:31], v[68:69] op_sel_hi:[1,0]\n"
    "v_pk_fma_f32 v[0:1], v[128:129], v[76:77], v[0:1] op_sel_hi:[1,0,1]\n"
    "v_pk_fma_f32 v[2:3], v[130:131], v[76:77], v[2:3] op_sel_hi:[1,0,1]\n"
    "v_pk_fma_f32 v[4:5], v[132:133], v[76:77], v[4:5] op_sel_hi:[1,0,1]\n"
    "v_pk_fma_f32 v[6:7], v[134:135], v[76:77], v[6:7] op_sel_hi:[1,0,1]\n"
    "v_pk_fma_f32 v[8:9], v[136:137], v[76:77], v[8:9] op_sel_hi:[1,0,1]\n"
    "v_pk_fma_f32 v[10:11], v[138:139], v[76:77], v[10:11] op_sel_hi:[1,0,1]\n"
    "v_pk_fma_f32 v[12:13], v[140:141], v[76:77], v[12:13] op_sel_hi:[1,0,1]\n"
    "ds_write_b128 v246, a[232:235] offset:14336\n"
    "v_pk_fma_f32 v[14:15], v[142:143], v[76:77], v[14:15] op_sel_hi:[1,0,1]\n"
    "v_pk_fma_f32 v[16:17], v[144:145], v[76:77], v[16:17] op_sel_hi:[1,0,1]\n"
    "v_pk_fma_f32 v[18:19], v[146:147], v[76:77], v[18:19] op_sel_hi:[1,0,1]\n"
    "v_pk_fma_f32 v[20:21], v[148:149], v[76:77], v[20:21] op_sel_hi:[1,0,1]\n"
    "v_pk_fma_f32 v[22:23], v[150:151], v[76:77], v[22:23] op_sel_hi:[1,0,1]\n"
    "v_pk_fma_f32 v[24:25], v[152:153], v[76:77], v[24:25] op_sel_hi:[1,0,1]\n"
    "v_pk_fma_f32 v[26:27], v[154:155], v[76:77], v[26:27] op_sel_hi:[1,0,1]\n"
    "v_pk_fma_f32 v[28:29], v[156:157], v[76:77], v[28:29] op_sel_hi:[1,0,1]\n"
    "v_pk_fma_f32 v[30:31], v[158:159], v[76:77], v[30:31] op_sel_hi:[1,0,1]\n"
    "v_cvt_pk_bf16_f32 v0, v0, v1\n"
    "v_cvt_pk_bf16_f32 v1, v2, v3\n"
    "v_cvt_pk_bf16_f32 v2, v4, v5\n"
    "ds_write_b128 v246, a[236:239] offset:15360\n"
    "v_cvt_pk_bf16_f32 v3, v6, v7\n"
    "v_cvt_pk_bf16_f32 v8, v8, v9\n"
    "v_cvt_pk_bf16_f32 v9, v10, v11\n"
    "v_cvt_pk_bf16_f32 v10, v12, v13\n"
    "v_cvt_pk_bf16_f32 v11, v14, v15\n"
    "v_cvt_pk_bf16_f32 v16, v16, v17\n"
    "v_cvt_pk_bf16_f32 v17, v18, v19\n"
    "v_cvt_pk_bf16_f32 v18, v20, v21\n"
    "v_cvt_pk_bf16_f32 v19, v22, v23\n"
    "v_cvt_pk_bf16_f32 v24, v24, v25\n"
    "v_cvt_pk_bf16_f32 v25, v26, v27\n"
    "v_cvt_pk_bf16_f32 v26, v28, v29\n"
    "v_cvt_pk_bf16_f32 v27, v30, v31\n"
    "v_permlane16_swap_b32 v0, v2\n"
    "v_permlane16_swap_b32 v1, v3\n"
    "v_permlane16_swap_b32 v8, v10\n"
    "v_permlane16_swap_b32 v9, v11\n"
    "v_permlane16_swap_b32 v16, v18\n"
    "v_permlane16_swap_b32 v17, v19\n"
    "v_permlane16_swap_b32 v24, v26\n"
    "v_permlane16_swap_b32 v25, v27\n"
    "L_epi:\n"
    "s_add_u32 s91, s98, s23\n"
    "buffer_store_dwordx4 v[0:3], v243, s[52:55], s98 offen offset:0 sc1 nt\n"
    "buffer_store_dwordx4 v[8:11], v243, s[52:55], s98 offen offset:64 sc1 nt\n"
    "s_waitcnt lgkmcnt(0)\n"
    "s_barrier\n"
    "ds_read_b64 v[94:95], v249 offset:8\n"
    "ds_read_b128 v[160:163], v247 offset:8192\n"
    "ds_read_b128 v[164:167], v247 offset:9216\n"
    "ds_read_b128 v[168:171], v247 offset:10240\n"
    "ds_read_b128 v[172:175], v247 offset:11264\n"
    "ds_read_b128 v[176:179], v247 offset:12288\n"
    "ds_read_b128 v[180:183], v247 offset:13312\n"
    "ds_read_b128 v[184:187], v247 offset:14336\n"
    "ds_read_b128 v[188:191], v247 offset:15360\n"
    "buffer_store_dwordx4 v[16:19], v243, s[52:55], s98 offen offset:128 sc1 nt\n"
    "s_waitcnt lgkmcnt(8)\n"
    "v_min_f32 v232, v212, v95\n"
    "v_sub_f32 v233, v232, v212\n"
    "v_sub_f32 v234, v232, v95\n"
    "v_exp_f32 v233, v233\n"
    "v_exp_f32 v234, v234\n"
    "s_nop 0\n"
    "v_mul_f32 v235, v233, v229\n"
    "v_fma_f32 v235, v234, v94, v235\n"
    "v_rcp_f32 v235, v235\n"
    "s_nop 0\n"
    "v_mul_f32 v70, v233, v235\n"
    "v_mul_f32 v78, v234, v235\n"
    "v_pk_mul_f32 v[32:33], v[32:33], v[70:71] op_sel_hi:[1,0]\n"
    "v_pk_mul_f32 v[34:35], v[34:35], v[70:71] op_sel_hi:[1,0]\n"
    "v_pk_mul_f32 v[36:37], v[36:37], v[70:71] op_sel_hi:[1,0]\n"
    "v_pk_mul_f32 v[38:39], v[38:39], v[70:71] op_sel_hi:[1,0]\n"
    "v_pk_mul_f32 v[40:41], v[40:41], v[70:71] op_sel_hi:[1,0]\n"
    "v_pk_mul_f32 v[42:43], v[42:43], v[70:71] op_sel_hi:[1,0]\n"
    "v_pk_mul_f32 v[44:45], v[44:45], v[70:71] op_sel_hi:[1,0]\n"
    "v_pk_mul_f32 v[46:47], v[46:47], v[70:71] op_sel_hi:[1,0]\n"
    "v_pk_mul_f32 v[48:49], v[48:49], v[70:71] op_sel_hi:[1,0]\n"
    "v_pk_mul_f32 v[50:51], v[50:51], v[70:71] op_sel_hi:[1,0]\n"
    "v_pk_mul_f32 v[52:53], v[52:53], v[70:71] op_sel_hi:[1,0]\n"
    "v_pk_mul_f32 v[54:55], v[54:55], v[70:71] op_sel_hi:[1,0]\n"
    "v_pk_mul_f32 v[56:57], v[56:57], v[70:71] op_sel_hi:[1,0]\n"
    "v_pk_mul_f32 v[58:59], v[58:59], v[70:71] op_sel_hi:[1,0]\n"
    "v_pk_mul_f32 v[60:61], v[60:61], v[70:71] op_sel_hi:[1,0]\n"
    "v_pk_mul_f32 v[62:63], v[62:63], v[70:71] op_sel_hi:[1,0]\n"
    "buffer_store_dwordx4 v[24:27], v243, s[52:55], s98 offen offset:192 sc1 nt\n"
    "s_waitcnt lgkmcnt(0)\n"
    "v_pk_fma_f32 v[32:33], v[160:161], v[78:79], v[32:33] op_sel_hi:[1,0,1]\n"
    "v_pk_fma_f32 v[34:35], v[162:163], v[78:79], v[34:35] op_sel_hi:[1,0,1]\n"
    "v_pk_fma_f32 v[36:37], v[164:165], v[78:79], v[36:37] op_sel_hi:[1,0,1]\n"
    "v_pk_fma_f32 v[38:39], v[166:167], v[78:79], v[38:39] op_sel_hi:[1,0,1]\n"
    "v_pk_fma_f32 v[40:41], v[168:169], v[78:79], v[40:41] op_sel_hi:[1,0,1]\n"
    "v_pk_fma_f32 v[42:43], v[170:171], v[78:79], v[42:43] op_sel_hi:[1,0,1]\n"
    "v_pk_fma_f32 v[44:45], v[172:173], v[78:79], v[44:45] op_sel_hi:[1,0,1]\n"
    "v_pk_fma_f32 v[46:47], v[174:175], v[78:79], v[46:47] op_sel_hi:[1,0,1]\n"
    "v_pk_fma_f32 v[48:49], v[176:177], v[78:79], v[48:49] op_sel_hi:[1,0,1]\n"
    "v_pk_fma_f32 v[50:51], v[178:179], v[78:79], v[50:51] op_sel_hi:[1,0,1]\n"
    "v_pk_fma_f32 v[52:53], v[180:181], v[78:79], v[52:53] op_sel_hi:[1,0,1]\n"
    "v_pk_fma_f32 v[54:55], v[182:183], v[78:79], v[54:55] op_sel_hi:[1,0,1]\n"
    "v_pk_fma_f32 v[56:57], v[184:185], v[78:79], v[56:57] op_sel_hi:[1,0,1]\n"
    "v_pk_fma_f32 v[58:59], v[186:187], v[78:79], v[58:59] op_sel_hi:[1,0,1]\n"
    "v_pk_fma_f32 v[60:61], v[188:189], v[78:79], v[60:61] op_sel_hi:[1,0,1]\n"
    "v_pk_fma_f32 v[62:63], v[190:191], v[78:79], v[62:63] op_sel_hi:[1,0,1]\n"
    "v_cvt_pk_bf16_f32 v32, v32, v33\n"
    "v_cvt_pk_bf16_f32 v33, v34, v35\n"
    "v_xor_b32 v34, 0, v241\n"
    "v_cvt_pk_bf16_f32 v36, v36, v37\n"
    "v_cvt_pk_bf16_f32 v37, v38, v39\n"
    "v_xor_b32 v38, 32, v241\n"
    "v_cvt_pk_bf16_f32 v40, v40, v41\n"
    "v_cvt_pk_bf16_f32 v41, v42, v43\n"
    "v_xor_b32 v42, 64, v241\n"
    "v_cvt_pk_bf16_f32 v44, v44, v45\n"
    "v_cvt_pk_bf16_f32 v45, v46, v47\n"
    "v_xor_b32 v46, 96, v241\n"
    "v_cvt_pk_bf16_f32 v48, v48, v49\n"
    "v_cvt_pk_bf16_f32 v49, v50, v51\n"
    "v_xor_b32 v50, 128, v241\n"
    "v_cvt_pk_bf16_f32 v52, v52, v53\n"
    "v_cvt_pk_bf16_f32 v53, v54, v55\n"
    "v_xor_b32 v54, 160, v241\n"
    "v_cvt_pk_bf16_f32 v56, v56, v57\n"
    "v_cvt_pk_bf16_f32 v57, v58, v59\n"
    "v_xor_b32 v58, 192, v241\n"
    "v_cvt_pk_bf16_f32 v60, v60, v61\n"
    "v_cvt_pk_bf16_f32 v61, v62, v63\n"
    "v_xor_b32 v62, 224, v241\n"
    "ds_write_b64 v34, v[32:33] offset:4096\n"
    "ds_write_b64 v38, v[36:37] offset:4096\n"
    "ds_write_b64 v42, v[40:41] offset:4096\n"
    "ds_write_b64 v46, v[44:45] offset:4096\n"
    "ds_write_b64 v50, v[48:49] offset:4096\n"
    "ds_write_b64 v54, v[52:53] offset:4096\n"
    "ds_write_b64 v58, v[56:57] offset:4096\n"
    "ds_write_b64 v62, v[60:61] offset:4096\n"
    "s_waitcnt lgkmcnt(0)\n"
    "v_xor_b32 v48, 0, v242\n"
    "v_xor_b32 v52, 64, v242\n"
    "v_xor_b32 v56, 128, v242\n"
    "v_xor_b32 v60, 192, v242\n"
    "ds_read_b128 v[32:35], v48 offset:4096\n"
    "ds_read_b128 v[36:39], v52 offset:5120\n"
    "ds_read_b128 v[40:43], v56 offset:6144\n"
    "ds_read_b128 v[44:47], v60 offset:7168\n"
    "s_waitcnt lgkmcnt(3)\n"
    "buffer_store_dwordx4 v[32:35], v240, s[52:55], s91 offen offset:0 sc1 nt\n"
    "s_waitcnt lgkmcnt(2)\n"
    "buffer_store_dwordx4 v[36:39], v240, s[52:55], s91 offen offset:1024 sc1 nt\n"
    "s_waitcnt lgkmcnt(1)\n"
    "buffer_store_dwordx4 v[40:43], v240, s[52:55], s91 offen offset:2048 sc1 nt\n"
    "s_waitcnt lgkmcnt(0)\n"
    "buffer_store_dwordx4 v[44:47], v240, s[52:55], s91 offen offset:3072 sc1 nt\n"
    "s_endpgm\n"
    "L_qs:\n"
    "s_lshl_b32 s89, s38, 13\n"
    "s_add_u32 m0, s89, 131072\n"
    "s_nop 0\n"
    "buffer_load_dwordx4 v251, s[48:51], s99 offen offset:0 lds\n"
    "buffer_load_dwordx4 v252, s[48:51], s99 offen offset:1024 lds\n"
    "buffer_load_dwordx4 v251, s[48:51], s99 offen offset:2048 lds\n"
    "buffer_load_dwordx4 v252, s[48:51], s99 offen offset:3072 lds\n"
    "s_add_u32 s91, s99, s23\n"
    "s_add_u32 m0, s89, 135168\n"
    "s_nop 0\n"
    "buffer_load_dwordx4 v251, s[48:51], s91 offen offset:0 lds\n"
    "buffer_load_dwordx4 v252, s[48:51], s91 offen offset:1024 lds\n"
    "buffer_load_dwordx4 v251, s[48:51], s91 offen offset:2048 lds\n"
    "buffer_load_dwordx4 v252, s[48:51], s91 offen offset:3072 lds\n"
    "s_branch L_qsr\n"
    "L_qs0:\n"
    "s_lshl_b32 s89, s38, 13\n"
    "s_add_u32 m0, s89, 131072\n"
    "s_nop 0\n"
    "buffer_load_dwordx4 v251, s[48:51], s99 offen offset:0 lds\n"
    "buffer_load_dwordx4 v252, s[48:51], s99 offen offset:1024 lds\n"
    "buffer_load_dwordx4 v251, s[48:51], s99 offen offset:2048 lds\n"
    "buffer_load_dwordx4 v252, s[48:51], s99 offen offset:3072 lds\n"
    "s_add_u32 s91, s99, s23\n"
    "s_add_u32 m0, s89, 135168\n"
    "s_nop 0\n"
    "buffer_load_dwordx4 v251, s[48:51], s91 offen offset:0 lds\n"
    "buffer_load_dwordx4 v252, s[48:51], s91 offen offset:1024 lds\n"
    "buffer_load_dwordx4 v251, s[48:51], s91 offen offset:2048 lds\n"
    "buffer_load_dwordx4 v252, s[48:51], s91 offen offset:3072 lds\n"
    "s_branch L_noqs\n"
    "L_mo_pro:\n"
    "s_mov_b32 s92, s95\n"
    "s_cmp_lt_i32 s92, 0\n"
    "s_cselect_b64 s[84:85], -1, 0\n"
    "s_cmp_eq_i32 s92, 0\n"
    "s_cselect_b64 s[72:73], s[64:65], s[84:85]\n"
    "s_cselect_b64 s[74:75], s[66:67], s[84:85]\n"
    "s_cselect_b64 s[76:77], s[68:69], s[84:85]\n"
    "s_cselect_b64 s[78:79], s[70:71], s[84:85]\n"
    "s_cmp_lt_i32 s92, 1\n"
    "s_cselect_b64 s[84:85], -1, 0\n"
    "s_cmp_eq_i32 s92, 1\n"
    "s_cselect_b64 s[80:81], s[64:65], s[84:85]\n"
    "s_cselect_b64 s[82:83], s[66:67], s[84:85]\n"
    "s_cselect_b64 s[88:89], s[68:69], s[84:85]\n"
    "s_cselect_b64 s[90:91], s[70:71], s[84:85]\n"
    "v_cndmask_b32 v128, v128, v254, s[72:73]\n"
    "v_cndmask_b32 v129, v129, v254, s[74:75]\n"
    "v_cndmask_b32 v130, v130, v254, s[76:77]\n"
    "v_cndmask_b32 v131, v131, v254, s[78:79]\n"
    "v_cndmask_b32 v132, v132, v254, s[80:81]\n"
    "v_cndmask_b32 v133, v133, v254, s[82:83]\n"
    "v_cndmask_b32 v134, v134, v254, s[88:89]\n"
    "v_cndmask_b32 v135, v135, v254, s[90:91]\n"
    "s_mov_b32 s92, s96\n"
    "s_cmp_lt_i32 s92, 0\n"
    "s_cselect_b64 s[84:85], -1, 0\n"
    "s_cmp_eq_i32 s92, 0\n"
    "s_cselect_b64 s[72:73], s[64:65], s[84:85]\n"
    "s_cselect_b64 s[74:75], s[66:67], s[84:85]\n"
    "s_cselect_b64 s[76:77], s[68:69], s[84:85]\n"
    "s_cselect_b64 s[78:79], s[70:71], s[84:85]\n"
    "s_cmp_lt_i32 s92, 1\n"
    "s_cselect_b64 s[84:85], -1, 0\n"
    "s_cmp_eq_i32 s92, 1\n"
    "s_cselect_b64 s[80:81], s[64:65], s[84:85]\n"
    "s_cselect_b64 s[82:83], s[66:67], s[84:85]\n"
    "s_cselect_b64 s[88:89], s[68:69], s[84:85]\n"
    "s_cselect_b64 s[90:91], s[70:71], s[84:85]\n"
    "v_cndmask_b32 v144, v144, v254, s[72:73]\n"
    "v_cndmask_b32 v145, v145, v254, s[74:75]\n"
    "v_cndmask_b32 v146, v146, v254, s[76:77]\n"
    "v_cndmask_b32 v147, v147, v254, s[78:79]\n"
    "v_cndmask_b32 v148, v148, v254, s[80:81]\n"
    "v_cndmask_b32 v149, v149, v254, s[82:83]\n"
    "v_cndmask_b32 v150, v150, v254, s[88:89]\n"
    "v_cndmask_b32 v151, v151, v254, s[90:91]\n"
    "s_branch L_ms_pro\n"
    "L_mo_T0b:\n"
    "s_nop 7\n"
    "s_mov_b32 s92, s19\n"
    "s_cmp_lt_i32 s92, 1\n"
    "s_cselect_b64 s[84:85], -1, 0\n"
    "s_cmp_eq_i32 s92, 1\n"
    "s_cselect_b64 s[80:81], s[64:65], s[84:85]\n"
    "s_cselect_b64 s[82:83], s[66:67], s[84:85]\n"
    "s_cselect_b64 s[88:89], s[68:69], s[84:85]\n"
    "s_cselect_b64 s[90:91], s[70:71], s[84:85]\n"
    "v_cndmask_b32 v172, v172, v254, s[80:81]\n"
    "v_cndmask_b32 v173, v173, v254, s[82:83]\n"
    "v_cndmask_b32 v174, v174, v254, s[88:89]\n"
    "v_cndmask_b32 v175, v175, v254, s[90:91]\n"
    "s_mov_b32 s92, s97\n"
    "s_cmp_lt_i32 s92, 0\n"
    "s_cselect_b64 s[84:85], -1, 0\n"
    "s_cmp_eq_i32 s92, 0\n"
    "s_cselect_b64 s[72:73], s[64:65], s[84:85]\n"
    "s_cselect_b64 s[74:75], s[66:67], s[84:85]\n"
    "s_cselect_b64 s[76:77], s[68:69], s[84:85]\n"
    "s_cselect_b64 s[78:79], s[70:71], s[84:85]\n"
    "s_cmp_lt_i32 s92, 1\n"
    "s_cselect_b64 s[84:85], -1, 0\n"
    "s_cmp_eq_i32 s92, 1\n"
    "s_cselect_b64 s[80:81], s[64:65], s[84:85]\n"
    "s_cselect_b64 s[82:83], s[66:67], s[84:85]\n"
    "s_cselect_b64 s[88:89], s[68:69], s[84:85]\n"
    "s_cselect_b64 s[90:91], s[70:71], s[84:85]\n"
    "v_cndmask_b32 v184, v184, v254, s[72:73]\n"
    "v_cndmask_b32 v185, v185, v254, s[74:75]\n"
    "v_cndmask_b32 v186, v186, v254, s[76:77]\n"
    "v_cndmask_b32 v187, v187, v254, s[78:79]\n"
    "v_cndmask_b32 v188, v188, v254, s[80:81]\n"
    "v_cndmask_b32 v189, v189, v254, s[82:83]\n"
    "v_cndmask_b32 v190, v190, v254, s[88:89]\n"
    "v_cndmask_b32 v191, v191, v254, s[90:91]\n"
    "v_mov_b32 v160, v254\n"
    "v_mov_b32 v161, v254\n"
    "v_mov_b32 v162, v254\n"
    "v_mov_b32 v163, v254\n"
    "v_mov_b32 v164, v254\n"
    "v_mov_b32 v165, v254\n"
    "v_mov_b32 v166, v254\n"
    "v_mov_b32 v167, v254\n"
    "v_mov_b32 v176, v254\n"
    "v_mov_b32 v177, v254\n"
    "v_mov_b32 v178, v254\n"
    "v_mov_b32 v179, v254\n"
    "v_mov_b32 v180, v254\n"
    "v_mov_b32 v181, v254\n"
    "v_mov_b32 v182, v254\n"
    "v_mov_b32 v183, v254\n"
    "s_branch L_ms_T0b\n"
    "L_mo_T0c:\n"
    "s_nop 7\n"
    "s_mov_b32 s92, s38\n"
    "s_cmp_lt_i32 s92, 0\n"
    "s_cselect_b64 s[84:85], -1, 0\n"
    "s_cmp_eq_i32 s92, 0\n"
    "s_cselect_b64 s[72:73], s[64:65], s[84:85]\n"
    "s_cselect_b64 s[74:75], s[66:67], s[84:85]\n"
    "s_cselect_b64 s[76:77], s[68:69], s[84:85]\n"
    "s_cselect_b64 s[78:79], s[70:71], s[84:85]\n"
    "s_cmp_lt_i32 s92, 1\n"
    "s_cselect_b64 s[84:85], -1, 0\n"
    "s_cmp_eq_i32 s92, 1\n"
    "s_cselect_b64 s[80:81], s[64:65], s[84:85]\n"
    "s_cselect_b64 s[82:83], s[66:67], s[84:85]\n"
    "s_cselect_b64 s[88:89], s[68:69], s[84:85]\n"
    "s_cselect_b64 s[90:91], s[70:71], s[84:85]\n"
    "v_cndmask_b32 v144, v144, v254, s[72:73]\n"
    "v_cndmask_b32 v145, v145, v254, s[74:75]\n"
    "v_cndmask_b32 v146, v146, v254, s[76:77]\n"
    "v_cndmask_b32 v147, v147, v254, s[78:79]\n"
    "v_cndmask_b32 v148, v148, v254, s[80:81]\n"
    "v_cndmask_b32 v149, v149, v254, s[82:83]\n"
    "v_cndmask_b32 v150, v150, v254, s[88:89]\n"
    "v_cndmask_b32 v151, v151, v254, s[90:91]\n"
    "s_branch L_ms_T0c\n"
    "L_mo_L1a:\n"
    "s_nop 7\n"
    "s_mov_b32 s92, s38\n"
    "s_cmp_lt_i32 s92, 0\n"
    "s_cselect_b64 s[84:85], -1, 0\n"
    "s_cmp_eq_i32 s92, 0\n"
    "s_cselect_b64 s[72:73], s[64:65], s[84:85]\n"
    "s_cselect_b64 s[74:75], s[66:67], s[84:85]\n"
    "s_cselect_b64 s[76:77], s[68:69], s[84:85]\n"
    "s_cselect_b64 s[78:79], s[70:71], s[84:85]\n"
    "s_cmp_lt_i32 s92, 1\n"
    "s_cselect_b64 s[84:85], -1, 0\n"
    "s_cmp_eq_i32 s92, 1\n"
    "s_cselect_b64 s[80:81], s[64:65], s[84:85]\n"
    "s_cselect_b64 s[82:83], s[66:67], s[84:85]\n"
    "s_cselect_b64 s[88:89], s[68:69], s[84:85]\n"
    "s_cselect_b64 s[90:91], s[70:71], s[84:85]\n"
    "v_cndmask_b32 v144, v144, v254, s[72:73]\n"
    "v_cndmask_b32 v145, v145, v254, s[74:75]\n"
    "v_cndmask_b32 v146, v146, v254, s[76:77]\n"
    "v_cndmask_b32 v147, v147, v254, s[78:79]\n"
    "v_cndmask_b32 v148, v148, v254, s[80:81]\n"
    "v_cndmask_b32 v149, v149, v254, s[82:83]\n"
    "v_cndmask_b32 v150, v150, v254, s[88:89]\n"
    "v_cndmask_b32 v151, v151, v254, s[90:91]\n"
    "s_branch L_ms_L1a\n"
    "L_X7:\n"
    "s_lshl_b32 s60, s38, 14\n"
    "s_lshl_b32 s58, s38, 10\n"
    "s_add_u32 s58, s58, 159744\n"
    "s_add_u32 s94, s99, s23\n"
    "v_mov_b32 v217, v216\n"
    "v_mov_b32 v219, v220\n"
    "s_sub_i32 s92, 1, s38\n"
    "s_cmp_lt_i32 s92, 0\n"
    "s_cselect_b64 s[84:85], -1, 0\n"
    "s_cmp_eq_i32 s92, 0\n"
    "s_cselect_b64 s[72:73], s[64:65], s[84:85]\n"
    "s_cselect_b64 s[74:75], s[66:67], s[84:85]\n"
    "s_cselect_b64 s[76:77], s[68:69], s[84:85]\n"
    "s_cselect_b64 s[78:79], s[70:71], s[84:85]\n"
    "s_cmp_lt_i32 s92, 1\n"
    "s_cselect_b64 s[84:85], -1, 0\n"
    "s_cmp_eq_i32 s92, 1\n"
    "s_cselect_b64 s[80:81], s[64:65], s[84:85]\n"
    "s_cselect_b64 s[82:83], s[66:67], s[84:85]\n"
    "s_cselect_b64 s[88:89], s[68:69], s[84:85]\n"
    "s_cselect_b64 s[90:91], s[70:71], s[84:85]\n"
    "v_mfma_f32_16x16x32_bf16 v[32:35], a[128:131], v[196:199], v[32:35]\n"
    "v_fma_f32 v236, v172, s36, v212\n"
    "v_fma_f32 v237, v173, s36, v212\n"
    "v_fma_f32 v238, v174, s36, v212\n"
    "v_fma_f32 v239, v175, s36, v212\n"
    "v_fma_f32 v240, v160, s36, v208\n"
    "v_accvgpr_read_b32 v249, a246\n"
    "v_rcp_f32 v255, v226\n"
    "v_mfma_f32_16x16x32_bf16 v[36:39], a[132:135], v[196:199], v[36:39]\n"
    "v_exp_f32 v236, v236\n"
    "v_add_u32 v246, s60, v249\n"
    "v_add_u32 v248, s58, v249\n"
    "v_mul_f32 v68, v255, v68\n"
    "v_mul_f32 v69, v255, v69\n"
    "v_mul_f32 v71, v255, v71\n"
    "v_mul_f32 v91, v255, v91\n"
    "v_mfma_f32_16x16x32_bf16 v[40:43], a[136:139], v[196:199], v[40:43]\n"
    "s_waitcnt vmcnt(0)\n"
    "v_exp_f32 v237, v237\n"
    "v_mul_f32 v64, v255, v64\n"
    "v_mul_f32 v65, v255, v65\n"
    "v_mul_f32 v70, v255, v70\n"
    "v_cvt_pk_bf16_f32 v64, v64, v65\n"
    "v_mul_f32 v74, v255, v74\n"
    "v_mfma_f32_16x16x32_bf16 v[44:47], a[140:143], v[196:199], v[44:47]\n"
    "s_barrier\n"
    "ds_read_b64_tr_b16 a[96:97], v247 offset:8192\n"
    "ds_read_b64_tr_b16 a[98:99], v247 offset:12288\n"
    "ds_write_b128 v246, a[176:179] offset:0\n"
    "v_cndmask_b32 v184, v184, v254, s[72:73]\n"
    "v_mul_f32 v67, v255, v67\n"
    "v_mul_f32 v72, v255, v72\n"
    "v_mul_f32 v86, v255, v86\n"
    "v_mfma_f32_16x16x32_bf16 v[48:51], a[144:147], v[196:199], v[48:51]\n"
    "ds_write_b128 v246, a[180:183] offset:1024\n"
    "v_accvgpr_read_b32 v218, a243\n"
    "v_accvgpr_read_b32 v253, a245\n"
    "v_mul_f32 v66, v255, v66\n"
    "v_mul_f32 v78, v255, v78\n"
    "v_mul_f32 v79, v255, v79\n"
    "v_mul_f32 v80, v255, v80\n"
    "v_mfma_f32_16x16x32_bf16 v[52:55], a[148:151], v[196:199], v[52:55]\n"
    "ds_read_b64_tr_b16 a[100:101], v250 offset:8192\n"
    "v_exp_f32 v238, v238\n"
    "v_exp_f32 v239, v239\n"
    "v_fma_f32 v241, v161, s36, v208\n"
    "v_mul_f32 v73, v255, v73\n"
    "v_mul_f32 v87, v255, v87\n"
    "v_mfma_f32_16x16x32_bf16 v[56:59], a[152:155], v[196:199], v[56:59]\n"
    "v_exp_f32 v240, v240\n"
    "v_cvt_pk_bf16_f32 v65, v66, v67\n"
    "v_cvt_pk_bf16_f32 v66, v68, v69\n"
    "v_cvt_pk_bf16_f32 v67, v70, v71\n"
    "v_mul_f32 v77, v255, v77\n"
    "v_mul_f32 v82, v255, v82\n"
    "v_mfma_f32_16x16x32_bf16 v[60:63], a[156:159], v[196:199], v[60:63]\n"
    "v_cvt_pk_bf16_f32 v197, v234, v235\n"
    "v_permlane16_swap_b32 v64, v66\n"
    "v_permlane16_swap_b32 v65, v67\n"
    "v_mul_f32 v76, v255, v76\n"
    "v_mul_f32 v81, v255, v81\n"
    "v_mul_f32 v83, v255, v83\n"
    "v_mul_f32 v84, v255, v84\n"
    "v_cvt_pk_bf16_f32 v80, v80, v81\n"
    "v_mfma_f32_16x16x32_bf16 v[0:3], a[128:131], v[192:195], v[0:3]\n"
    "ds_write_b128 v246, a[184:187] offset:2048\n"
    "buffer_store_dwordx4 v[64:67], v253, s[52:55], s99 offen offset:0 sc1 nt\n"
    "v_cndmask_b32 v188, v188, v254, s[80:81]\n"
    "v_mul_f32 v75, v255, v75\n"
    "v_cvt_pk_bf16_f32 v72, v72, v73\n"
    "v_mfma_f32_16x16x32_bf16 v[4:7], a[132:135], v[192:195], v[4:7]\n"
    "ds_write_b128 v246, a[188:191] offset:3072\n"
    "v_cndmask_b32 v185, v185, v254, s[74:75]\n"
    "v_cndmask_b32 v189, v189, v254, s[82:83]\n"
    "v_fma_f32 v242, v162, s36, v208\n"
    "v_cvt_pk_bf16_f32 v73, v74, v75\n"
    "v_mul_f32 v93, v255, v93\n"
    "v_mfma_f32_16x16x32_bf16 v[8:11], a[136:139], v[192:195], v[8:11]\n"
    "ds_write_b128 v246, a[192:195] offset:4096\n"
    "v_cvt_pk_bf16_f32 v196, v232, v233\n"
    "v_cvt_pk_bf16_f32 v198, v236, v237\n"
    "v_exp_f32 v241, v241\n"
    "v_cvt_pk_bf16_f32 v74, v76, v77\n"
    "v_mul_f32 v85, v255, v85\n"
    "v_mfma_f32_16x16x32_bf16 v[12:15], a[140:143], v[192:195], v[12:15]\n"
    "ds_write_b128 v246, a[196:199] offset:5120\n"
    "v_cvt_pk_bf16_f32 v199, v238, v239\n"
    "v_fma_f32 v236, v163, s36, v208\n"
    "v_fma_f32 v237, v164, s36, v208\n"
    "v_cvt_pk_bf16_f32 v75, v78, v79\n"
    "v_mul_f32 v89, v255, v89\n"
    "v_mfma_f32_16x16x32_bf16 v[228:231], a[164:167], v[196:199], v[224:227]\n"
    "ds_read_b64_tr_b16 a[102:103], v250 offset:12288\n"
    "v_exp_f32 v242, v242\n"
    "v_fma_f32 v238, v165, s36, v208\n"
    "v_fma_f32 v239, v166, s36, v208\n"
    "v_permlane16_swap_b32 v72, v74\n"
    "v_permlane16_swap_b32 v73, v75\n"
    "v_cvt_pk_bf16_f32 v81, v82, v83\n"
    "v_mfma_f32_16x16x32_bf16 v[16:19], a[144:147], v[192:195], v[16:19]\n"
    "ds_read_b64_tr_b16 a[104:105], v247 offset:8448\n"
    "ds_read_b64_tr_b16 a[106:107], v247 offset:12544\n"
    "ds_read_b64_tr_b16 a[108:109], v250 offset:8448\n"
    "ds_read_b64_tr_b16 a[110:111], v250 offset:12544\n"
    "ds_read_b64_tr_b16 a[112:113], v247 offset:8704\n"
    "ds_write_b128 v246, a[200:203] offset:6144\n"
    "v_fma_f32 v243, v167, s36, v208\n"
    "v_mfma_f32_16x16x32_bf16 v[20:23], a[148:151], v[192:195], v[20:23]\n"
    "ds_write_b128 v246, a[204:207] offset:7168\n"
    "buffer_store_dwordx4 v[72:75], v253, s[52:55], s99 offen offset:64 sc1 nt\n"
    "v_cndmask_b32 v186, v186, v254, s[76:77]\n"
    "v_cndmask_b32 v187, v187, v254, s[78:79]\n"
    "v_mul_f32 v94, v255, v94\n"
    "v_mfma_f32_16x16x32_bf16 v[24:27], a[152:155], v[192:195], v[24:27]\n"
    "ds_write_b128 v246, a[208:211] offset:8192\n"
    "v_exp_f32 v236, v236\n"
    "v_exp_f32 v237, v237\n"
    "v_exp_f32 v238, v238\n"
    "v_mfma_f32_16x16x32_bf16 v[28:31], a[156:159], v[192:195], v[28:31]\n"
    "ds_write_b128 v246, a[212:215] offset:9216\n"
    "v_exp_f32 v239, v239\n"
    "v_exp_f32 v243, v243\n"
    "s_nop 0\n"
    "v_cvt_pk_bf16_f32 v195, v239, v243\n"
    "v_mul_f32 v88, v255, v88\n"
    "v_mfma_f32_16x16x32_bf16 v[96:99], a[128:131], v[204:207], v[96:99]\n"
    "ds_read_b64_tr_b16 a[114:115], v247 offset:12800\n"
    "ds_read_b64_tr_b16 a[116:117], v250 offset:8704\n"
    "ds_write_b128 v246, a[216:219] offset:10240\n"
    "v_cvt_pk_bf16_f32 v193, v242, v236\n"
    "v_fma_f32 v239, v184, s36, v245\n"
    "v_cvt_pk_bf16_f32 v82, v84, v85\n"
    "v_mfma_f32_16x16x32_bf16 v[100:103], a[132:135], v[204:207], v[100:103]\n"
    "v_cndmask_b32 v190, v190, v254, s[88:89]\n"
    "v_cvt_pk_bf16_f32 v192, v240, v241\n"
    "v_exp_f32 v239, v239\n"
    "v_fma_f32 v236, v185, s36, v245\n"
    "v_cvt_pk_bf16_f32 v83, v86, v87\n"
    "v_mul_f32 v90, v255, v90\n"
    "v_cvt_pk_bf16_f32 v88, v88, v89\n"
    "v_mfma_f32_16x16x32_bf16 v[104:107], a[136:139], v[204:207], v[104:107]\n"
    "ds_read_b64_tr_b16 a[118:119], v250 offset:12800\n"
    "ds_write_b128 v246, a[220:223] offset:11264\n"
    "v_cndmask_b32 v191, v191, v254, s[90:91]\n"
    "v_fma_f32 v240, v186, s36, v245\n"
    "v_permlane16_swap_b32 v80, v82\n"
    "v_permlane16_swap_b32 v81, v83\n"
    "v_mul_f32 v92, v255, v92\n"
    "v_mfma_f32_16x16x32_bf16 v[108:111], a[140:143], v[204:207], v[108:111]\n"
    "ds_read_b64_tr_b16 a[120:121], v247 offset:8960\n"
    "ds_read_b64_tr_b16 a[122:123], v247 offset:13056\n"
    "ds_read_b64_tr_b16 a[124:125], v250 offset:8960\n"
    "ds_read_b64_tr_b16 a[126:127], v250 offset:13056\n"
    "buffer_store_dwordx4 v[80:83], v253, s[52:55], s99 offen offset:128 sc1 nt\n"
    "v_cvt_pk_bf16_f32 v194, v237, v238\n"
    "v_fma_f32 v241, v187, s36, v245\n"
    "s_nop 0\n"
    "v_mfma_f32_16x16x32_bf16 v[228:231], a[160:163], v[192:195], v[228:231]\n"
    "ds_write_b128 v246, a[224:227] offset:12288\n"
    "v_exp_f32 v236, v236\n"
    "v_fma_f32 v237, v188, s36, v245\n"
    "v_fma_f32 v238, v189, s36, v245\n"
    "v_fma_f32 v242, v190, s36, v245\n"
    "v_mfma_f32_16x16x32_bf16 v[112:115], a[144:147], v[204:207], v[112:115]\n"
    "v_exp_f32 v240, v240\n"
    "v_exp_f32 v241, v241\n"
    "v_accvgpr_read_b32 v216, a242\n"
    "v_cvt_pk_bf16_f32 v89, v90, v91\n"
    "v_cvt_pk_bf16_f32 v90, v92, v93\n"
    "v_mfma_f32_16x16x32_bf16 v[116:119], a[148:151], v[204:207], v[116:119]\n"
    "ds_write_b128 v248, v[216:219]\n"
    "v_exp_f32 v237, v237\n"
    "v_exp_f32 v238, v238\n"
    "v_mul_f32 v95, v255, v95\n"
    "v_cvt_pk_bf16_f32 v91, v94, v95\n"
    "v_mfma_f32_16x16x32_bf16 v[120:123], a[152:155], v[204:207], v[120:123]\n"
    "ds_write_b128 v246, a[228:231] offset:13312\n"
    "v_exp_f32 v242, v242\n"
    "v_fma_f32 v243, v191, s36, v245\n"
    "v_exp_f32 v243, v243\n"
    "v_mfma_f32_16x16x32_bf16 v[124:127], a[156:159], v[204:207], v[124:127]\n"
    "ds_write_b128 v246, a[232:235] offset:14336\n"
    "v_cvt_pk_bf16_f32 v204, v239, v236\n"
    "v_cvt_pk_bf16_f32 v205, v240, v241\n"
    "v_cvt_pk_bf16_f32 v206, v237, v238\n"
    "v_cvt_pk_bf16_f32 v207, v242, v243\n"
    "v_permlane16_swap_b32 v88, v90\n"
    "v_permlane16_swap_b32 v89, v91\n"
    "v_mfma_f32_16x16x32_bf16 v[228:231], a[172:175], v[204:207], v[228:231]\n"
    "ds_write_b128 v246, a[236:239] offset:15360\n"
    "buffer_store_dwordx4 v[88:91], v253, s[52:55], s99 offen offset:192 sc1 nt\n"
    "s_nop 5\n"
    "v_max3_f32 v241, v228, v229, v230\n"
    "v_max_f32 v241, v241, v231\n"
    "v_cmp_nge_f32 vcc, s37, v241\n"
    "s_cbranch_vccnz L_rc_X7a\n"
    "L_rr_X7a:\n"
    "s_waitcnt lgkmcnt(0)\n"
    "v_xor_b32 v247, 32768, v246\n"
    "v_xor_b32 v249, 2048, v248\n"
    "s_barrier\n"
    "v_mfma_f32_16x16x32_bf16 v[96:99], a[96:99], v[204:207], v[96:99]\n"
    "ds_read_b64 v[92:93], v249 offset:0\n"
    "ds_read_b128 v[128:131], v247 offset:0\n"
    "v_mfma_f32_16x16x32_bf16 v[100:103], a[100:103], v[204:207], v[100:103]\n"
    "ds_read_b128 v[132:135], v247 offset:1024\n"
    "ds_read_b128 v[136:139], v247 offset:2048\n"
    "v_mfma_f32_16x16x32_bf16 v[104:107], a[104:107], v[204:207], v[104:107]\n"
    "ds_read_b128 v[140:143], v247 offset:3072\n"
    "ds_read_b128 v[144:147], v247 offset:4096\n"
    "v_mfma_f32_16x16x32_bf16 v[108:111], a[108:111], v[204:207], v[108:111]\n"
    "ds_read_b128 v[148:151], v247 offset:5120\n"
    "ds_read_b128 v[152:155], v247 offset:6144\n"
    "v_mfma_f32_16x16x32_bf16 v[112:115], a[112:115], v[204:207], v[112:115]\n"
    "ds_read_b128 v[156:159], v247 offset:7168\n"
    "v_rcp_f32 v255, v231\n"
    "v_mfma_f32_16x16x32_bf16 v[116:119], a[116:119], v[204:207], v[116:119]\n"
    "v_accvgpr_read_b32 v240, a246\n"
    "v_accvgpr_read_b32 v241, a247\n"
    "v_mfma_f32_16x16x32_bf16 v[120:123], a[120:123], v[204:207], v[120:123]\n"
    "v_accvgpr_read_b32 v242, a248\n"
    "v_mfma_f32_16x16x32_bf16 v[124:127], a[124:127], v[204:207], v[124:127]\n"
    "v_mfma_f32_16x16x32_bf16 v[0:3], a[96:99], v[192:195], v[0:3]\n"
    "v_mul_f32 v96, v255, v96\n"
    "v_mul_f32 v97, v255, v97\n"
    "v_mul_f32 v98, v255, v98\n"
    "v_mul_f32 v99, v255, v99\n"
    "v_mfma_f32_16x16x32_bf16 v[4:7], a[100:103], v[192:195], v[4:7]\n"
    "v_mul_f32 v100, v255, v100\n"
    "v_mul_f32 v101, v255, v101\n"
    "v_mul_f32 v102, v255, v102\n"
    "v_mul_f32 v103, v255, v103\n"
    "v_mfma_f32_16x16x32_bf16 v[8:11], a[104:107], v[192:195], v[8:11]\n"
    "v_cvt_pk_bf16_f32 v96, v96, v97\n"
    "v_cvt_pk_bf16_f32 v97, v98, v99\n"
    "v_cvt_pk_bf16_f32 v98, v100, v101\n"
    "v_cvt_pk_bf16_f32 v99, v102, v103\n"
    "v_mfma_f32_16x16x32_bf16 v[12:15], a[108:111], v[192:195], v[12:15]\n"
    "s_nop 0\n"
    "v_permlane16_swap_b32 v96, v98\n"
    "v_permlane16_swap_b32 v97, v99\n"
    "buffer_store_dwordx4 v[96:99], v253, s[52:55], s94 offen offset:0 sc1 nt\n"
    "v_mfma_f32_16x16x32_bf16 v[16:19], a[112:115], v[192:195], v[16:19]\n"
    "v_mul_f32 v104, v255, v104\n"
    "v_mul_f32 v105, v255, v105\n"
    "v_mul_f32 v106, v255, v106\n"
    "v_mul_f32 v107, v255, v107\n"
    "v_mfma_f32_16x16x32_bf16 v[20:23], a[116:119], v[192:195], v[20:23]\n"
    "v_mul_f32 v108, v255, v108\n"
    "v_mul_f32 v109, v255, v109\n"
    "v_mul_f32 v110, v255, v110\n"
    "v_mul_f32 v111, v255, v111\n"
    "v_mfma_f32_16x16x32_bf16 v[24:27], a[120:123], v[192:195], v[24:27]\n"
    "v_cvt_pk_bf16_f32 v104, v104, v105\n"
    "v_cvt_pk_bf16_f32 v105, v106, v107\n"
    "v_cvt_pk_bf16_f32 v106, v108, v109\n"
    "v_cvt_pk_bf16_f32 v107, v110, v111\n"
    "v_mfma_f32_16x16x32_bf16 v[28:31], a[124:127], v[192:195], v[28:31]\n"
    "s_nop 0\n"
    "v_permlane16_swap_b32 v104, v106\n"
    "v_permlane16_swap_b32 v105, v107\n"
    "buffer_store_dwordx4 v[104:107], v253, s[52:55], s94 offen offset:64 sc1 nt\n"
    "v_mfma_f32_16x16x32_bf16 v[32:35], a[96:99], v[196:199], v[32:35]\n"
    "v_mul_f32 v112, v255, v112\n"
    "v_mul_f32 v113, v255, v113\n"
    "v_mul_f32 v114, v255, v114\n"
    "v_mul_f32 v115, v255, v115\n"
    "v_mfma_f32_16x16x32_bf16 v[36:39], a[100:103], v[196:199], v[36:39]\n"
    "v_mul_f32 v116, v255, v116\n"
    "v_mul_f32 v117, v255, v117\n"
    "v_mul_f32 v118, v255, v118\n"
    "v_mul_f32 v119, v255, v119\n"
    "v_mfma_f32_16x16x32_bf16 v[40:43], a[104:107], v[196:199], v[40:43]\n"
    "v_cvt_pk_bf16_f32 v112, v112, v113\n"
    "v_cvt_pk_bf16_f32 v113, v114, v115\n"
    "v_cvt_pk_bf16_f32 v114, v116, v117\n"
    "v_cvt_pk_bf16_f32 v115, v118, v119\n"
    "v_mfma_f32_16x16x32_bf16 v[44:47], a[108:111], v[196:199], v[44:47]\n"
    "s_nop 0\n"
    "v_permlane16_swap_b32 v112, v114\n"
    "v_permlane16_swap_b32 v113, v115\n"
    "buffer_store_dwordx4 v[112:115], v253, s[52:55], s94 offen offset:128 sc1 nt\n"
    "v_mfma_f32_16x16x32_bf16 v[48:51], a[112:115], v[196:199], v[48:51]\n"
    "v_mul_f32 v120, v255, v120\n"
    "v_mul_f32 v121, v255, v121\n"
    "v_mul_f32 v122, v255, v122\n"
    "v_mul_f32 v123, v255, v123\n"
    "v_mfma_f32_16x16x32_bf16 v[52:55], a[116:119], v[196:199], v[52:55]\n"
    "v_mul_f32 v124, v255, v124\n"
    "v_mul_f32 v125, v255, v125\n"
    "v_mul_f32 v126, v255, v126\n"
    "v_mul_f32 v127, v255, v127\n"
    "v_mfma_f32_16x16x32_bf16 v[56:59], a[120:123], v[196:199], v[56:59]\n"
    "v_cvt_pk_bf16_f32 v120, v120, v121\n"
    "v_cvt_pk_bf16_f32 v121, v122, v123\n"
    "v_cvt_pk_bf16_f32 v122, v124, v125\n"
    "v_cvt_pk_bf16_f32 v123, v126, v127\n"
    "v_mfma_f32_16x16x32_bf16 v[60:63], a[124:127], v[196:199], v[60:63]\n"
    "s_nop 0\n"
    "v_permlane16_swap_b32 v120, v122\n"
    "v_permlane16_swap_b32 v121, v123\n"
    "buffer_store_dwordx4 v[120:123], v253, s[52:55], s94 offen offset:192 sc1 nt\n"
    "s_waitcnt lgkmcnt(0)\n"
    "v_accvgpr_read_b32 v243, a245\n"
    "v_min_f32 v84, v208, v93\n"
    "v_sub_f32 v85, v84, v208\n"
    "v_sub_f32 v86, v84, v93\n"
    "v_exp_f32 v85, v85\n"
    "v_exp_f32 v86, v86\n"
    "s_nop 0\n"
    "v_mul_f32 v87, v85, v228\n"
    "v_fma_f32 v87, v86, v92, v87\n"
    "v_rcp_f32 v87, v87\n"
    "s_nop 0\n"
    "v_mul_f32 v68, v85, v87\n"
    "v_mul_f32 v76, v86, v87\n"
    "v_pk_mul_f32 v[0:1], v[0:1], v[68:69] op_sel_hi:[1,0]\n"
    "v_pk_mul_f32 v[2:3], v[2:3], v[68:69] op_sel_hi:[1,0]\n"
    "v_pk_mul_f32 v[4:5], v[4:5], v[68:69] op_sel_hi:[1,0]\n"
    "v_pk_mul_f32 v[6:7], v[6:7], v[68:69] op_sel_hi:[1,0]\n"
    "v_pk_mul_f32 v[8:9], v[8:9], v[68:69] op_sel_hi:[1,0]\n"
    "v_pk_mul_f32 v[10:11], v[10:11], v[68:69] op_sel_hi:[1,0]\n"
    "v_pk_mul_f32 v[12:13], v[12:13], v[68:69] op_sel_hi:[1,0]\n"
    "v_pk_mul_f32 v[14:15], v[14:15], v[68:69] op_sel_hi:[1,0]\n"
    "v_pk_mul_f32 v[16:17], v[16:17], v[68:69] op_sel_hi:[1,0]\n"
    "v_pk_mul_f32 v[18:19], v[18:19], v[68:69] op_sel_hi:[1,0]\n"
    "v_pk_mul_f32 v[20:21], v[20:21], v[68:69] op_sel_hi:[1,0]\n"
    "v_pk_mul_f32 v[22:23], v[22:23], v[68:69] op_sel_hi:[1,0]\n"
    "v_pk_mul_f32 v[24:25], v[24:25], v[68:69] op_sel_hi:[1,0]\n"
    "v_pk_mul_f32 v[26:27], v[26:27], v[68:69] op_sel_hi:[1,0]\n"
    "v_pk_mul_f32 v[28:29], v[28:29], v[68:69] op_sel_hi:[1,0]\n"
    "v_pk_mul_f32 v[30:31], v[30:31], v[68:69] op_sel_hi:[1,0]\n"
    "v_pk_fma_f32 v[0:1], v[128:129], v[76:77], v[0:1] op_sel_hi:[1,0,1]\n"
    "v_pk_fma_f32 v[2:3], v[130:131], v[76:77], v[2:3] op_sel_hi:[1,0,1]\n"
    "v_pk_fma_f32 v[4:5], v[132:133], v[76:77], v[4:5] op_sel_hi:[1,0,1]\n"
    "v_pk_fma_f32 v[6:7], v[134:135], v[76:77], v[6:7] op_sel_hi:[1,0,1]\n"
    "v_pk_fma_f32 v[8:9], v[136:137], v[76:77], v[8:9] op_sel_hi:[1,0,1]\n"
    "v_pk_fma_f32 v[10:11], v[138:139], v[76:77], v[10:11] op_sel_hi:[1,0,1]\n"
    "v_pk_fma_f32 v[12:13], v[140:141], v[76:77], v[12:13] op_sel_hi:[1,0,1]\n"
    "v_pk_fma_f32 v[14:15], v[142:143], v[76:77], v[14:15] op_sel_hi:[1,0,1]\n"
    "v_pk_fma_f32 v[16:17], v[144:145], v[76:77], v[16:17] op_sel_hi:[1,0,1]\n"
    "v_pk_fma_f32 v[18:19], v[146:147], v[76:77], v[18:19] op_sel_hi:[1,0,1]\n"
    "v_pk_fma_f32 v[20:21], v[148:149], v[76:77], v[20:21] op_sel_hi:[1,0,1]\n"
    "v_pk_fma_f32 v[22:23], v[150:151], v[76:77], v[22:23] op_sel_hi:[1,0,1]\n"
    "v_pk_fma_f32 v[24:25], v[152:153], v[76:77], v[24:25] op_sel_hi:[1,0,1]\n"
    "v_pk_fma_f32 v[26:27], v[154:155], v[76:77], v[26:27] op_sel_hi:[1,0,1]\n"
    "v_pk_fma_f32 v[28:29], v[156:157], v[76:77], v[28:29] op_sel_hi:[1,0,1]\n"
    "v_pk_fma_f32 v[30:31], v[158:159], v[76:77], v[30:31] op_sel_hi:[1,0,1]\n"
    "v_cvt_pk_bf16_f32 v0, v0, v1\n"
    "v_cvt_pk_bf16_f32 v1, v2, v3\n"
    "v_cvt_pk_bf16_f32 v2, v4, v5\n"
    "v_cvt_pk_bf16_f32 v3, v6, v7\n"
    "v_cvt_pk_bf16_f32 v8, v8, v9\n"
    "v_cvt_pk_bf16_f32 v9, v10, v11\n"
    "v_cvt_pk_bf16_f32 v10, v12, v13\n"
    "v_cvt_pk_bf16_f32 v11, v14, v15\n"
    "v_cvt_pk_bf16_f32 v16, v16, v17\n"
    "v_cvt_pk_bf16_f32 v17, v18, v19\n"
    "v_cvt_pk_bf16_f32 v18, v20, v21\n"
    "v_cvt_pk_bf16_f32 v19, v22, v23\n"
    "v_cvt_pk_bf16_f32 v24, v24, v25\n"
    "v_cvt_pk_bf16_f32 v25, v26, v27\n"
    "v_cvt_pk_bf16_f32 v26, v28, v29\n"
    "v_cvt_pk_bf16_f32 v27, v30, v31\n"
    "v_permlane16_swap_b32 v0, v2\n"
    "v_permlane16_swap_b32 v1, v3\n"
    "v_permlane16_swap_b32 v8, v10\n"
    "v_permlane16_swap_b32 v9, v11\n"
    "v_permlane16_swap_b32 v16, v18\n"
    "v_permlane16_swap_b32 v17, v19\n"
    "v_permlane16_swap_b32 v24, v26\n"
    "v_permlane16_swap_b32 v25, v27\n"
    "s_branch L_epi\n"
    "L_hot_end:\n"
    "L_rc_T0a:\n"
    "s_getpc_b64 s[86:87]\n"
    "s_branch L_rare_0PP_0123_\n"
    "s_branch L_rr_T0a\n"
    "L_rc_T0b:\n"
    "s_getpc_b64 s[86:87]\n"
    "s_branch L_rare_1PP_0123_\n"
    "s_branch L_rr_T0b\n"
    "L_rc_T0c:\n"
    "s_getpc_b64 s[86:87]\n"
    "s_branch L_rare_0SP_0123_\n"
    "s_branch L_rr_T0c\n"
    "L_rc_L1b:\n"
    "s_getpc_b64 s[86:87]\n"
    "s_branch L_rare_1SP_0123_\n"
    "s_branch L_rr_L1b\n"
    "L_rc_L1a:\n"
    "s_getpc_b64 s[86:87]\n"
    "s_branch L_rare_0SP_0123_\n"
    "s_branch L_rr_L1a\n"
    "L_rc_T1b:\n"
    "s_getpc_b64 s[86:87]\n"
    "s_branch L_rare_1SP_0123_\n"
    "s_branch L_rr_T1b\n"
    "L_rc_T1c:\n"
    "s_getpc_b64 s[86:87]\n"
    "s_branch L_rare_0SP_013_\n"
    "s_branch L_rr_T1c\n"
    "L_rc_T1d:\n"
    "s_getpc_b64 s[86:87]\n"
    "s_branch L_rare_1SP_013_\n"
    "s_branch L_rr_T1d\n"
    "L_rc_T1e:\n"
    "s_getpc_b64 s[86:87]\n"
    "s_branch L_rare_0PP_0123_\n"
    "s_branch L_rr_T1e\n"
    "L_rc_L2a:\n"
    "s_getpc_b64 s[86:87]\n"
    "s_branch L_rare_1PP_0123_\n"
    "s_branch L_rr_L2a\n"
    "L_rc_L2b:\n"
    "s_getpc_b64 s[86:87]\n"
    "s_branch L_rare_0PP_0123_\n"
    "s_branch L_rr_L2b\n"
    "L_rc_T2b:\n"
    "s_getpc_b64 s[86:87]\n"
    "s_branch L_rare_0PP_0123_\n"
    "s_branch L_rr_T2b\n"
    "L_rc_T2c:\n"
    "s_getpc_b64 s[86:87]\n"
    "s_branch L_rare_1Pn_13_\n"
    "s_branch L_rr_T2c\n"
    "L_rc_X7a:\n"
    "s_getpc_b64 s[86:87]\n"
    "s_branch L_rare_1Sn_013_\n"
    "s_branch L_rr_X7a\n"
    "L_rare_0PP_0123_:\n"
    "s_cselect_b32 s34, 1, 0\n"
    "s_nop 7\n"
    "s_nop 7\n"
    "v_max3_f32 v236, v128, v129, v130\n"
    "v_max3_f32 v236, v236, v131, v132\n"
    "v_max3_f32 v236, v236, v133, v134\n"
    "v_max_f32 v236, v236, v135\n"
    "v_mov_b32 v237, v236\n"
    "s_nop 1\n"
    "v_permlane16_swap_b32 v236, v237\n"
    "v_max_f32 v236, v236, v237\n"
    "v_mov_b32 v237, v236\n"
    "s_nop 1\n"
    "v_permlane32_swap_b32 v236, v237\n"
    "v_max_f32 v236, v236, v237\n"
    "v_fma_f32 v236, v236, s36, v208\n"
    "v_log_f32 v237, v228\n"
    "s_nop 0\n"
    "v_add_f32 v237, 0xc1300000, v237\n"
    "v_max3_f32 v236, v236, v237, 0\n"
    "v_exp_f32 v238, -v236\n"
    "s_nop 0\n"
    "v_mul_f32 v228, v238, v228\n"
    "v_sub_f32 v208, v208, v236\n"
    "v_sub_f32 v209, v209, v236\n"
    "v_sub_f32 v210, v210, v236\n"
    "v_sub_f32 v211, v211, v236\n"
    "v_mul_f32 v0, v238, v0\n"
    "v_mul_f32 v1, v238, v1\n"
    "v_mul_f32 v2, v238, v2\n"
    "v_mul_f32 v3, v238, v3\n"
    "v_mul_f32 v4, v238, v4\n"
    "v_mul_f32 v5, v238, v5\n"
    "v_mul_f32 v6, v238, v6\n"
    "v_mul_f32 v7, v238, v7\n"
    "v_mul_f32 v8, v238, v8\n"
    "v_mul_f32 v9, v238, v9\n"
    "v_mul_f32 v10, v238, v10\n"
    "v_mul_f32 v11, v238, v11\n"
    "v_mul_f32 v12, v238, v12\n"
    "v_mul_f32 v13, v238, v13\n"
    "v_mul_f32 v14, v238, v14\n"
    "v_mul_f32 v15, v238, v15\n"
    "v_mul_f32 v16, v238, v16\n"
    "v_mul_f32 v17, v238, v17\n"
    "v_mul_f32 v18, v238, v18\n"
    "v_mul_f32 v19, v238, v19\n"
    "v_mul_f32 v20, v238, v20\n"
    "v_mul_f32 v21, v238, v21\n"
    "v_mul_f32 v22, v238, v22\n"
    "v_mul_f32 v23, v238, v23\n"
    "v_mul_f32 v24, v238, v24\n"
    "v_mul_f32 v25, v238, v25\n"
    "v_mul_f32 v26, v238, v26\n"
    "v_mul_f32 v27, v238, v27\n"
    "v_mul_f32 v28, v238, v28\n"
    "v_mul_f32 v29, v238, v29\n"
    "v_mul_f32 v30, v238, v30\n"
    "v_mul_f32 v31, v238, v31\n"
    "v_fma_f32 v237, v128, s36, v208\n"
    "v_fma_f32 v239, v129, s36, v208\n"
    "v_exp_f32 v237, v237\n"
    "v_exp_f32 v239, v239\n"
    "s_nop 0\n"
    "v_cvt_pk_bf16_f32 v192, v237, v239\n"
    "v_fma_f32 v237, v130, s36, v208\n"
    "v_fma_f32 v239, v131, s36, v208\n"
    "v_exp_f32 v237, v237\n"
    "v_exp_f32 v239, v239\n"
    "s_nop 0\n"
    "v_cvt_pk_bf16_f32 v193, v237, v239\n"
    "v_fma_f32 v237, v132, s36, v208\n"
    "v_fma_f32 v239, v133, s36, v208\n"
    "v_exp_f32 v237, v237\n"
    "v_exp_f32 v239, v239\n"
    "s_nop 0\n"
    "v_cvt_pk_bf16_f32 v194, v237, v239\n"
    "v_fma_f32 v237, v134, s36, v208\n"
    "v_fma_f32 v239, v135, s36, v208\n"
    "v_exp_f32 v237, v237\n"
    "v_exp_f32 v239, v239\n"
    "s_nop 0\n"
    "v_cvt_pk_bf16_f32 v195, v237, v239\n"
    "v_max3_f32 v236, v136, v137, v138\n"
    "v_max3_f32 v236, v236, v139, v140\n"
    "v_max3_f32 v236, v236, v141, v142\n"
    "v_max_f32 v236, v236, v143\n"
    "v_mov_b32 v237, v236\n"
    "s_nop 1\n"
    "v_permlane16_swap_b32 v236, v237\n"
    "v_max_f32 v236, v236, v237\n"
    "v_mov_b32 v237, v236\n"
    "s_nop 1\n"
    "v_permlane32_swap_b32 v236, v237\n"
    "v_max_f32 v236, v236, v237\n"
    "v_fma_f32 v236, v236, s36, v212\n"
    "v_log_f32 v237, v229\n"
    "s_nop 0\n"
    "v_add_f32 v237, 0xc1300000, v237\n"
    "v_max3_f32 v236, v236, v237, 0\n"
    "v_exp_f32 v238, -v236\n"
    "s_nop 0\n"
    "v_mul_f32 v229, v238, v229\n"
    "v_sub_f32 v212, v212, v236\n"
    "v_sub_f32 v213, v213, v236\n"
    "v_sub_f32 v214, v214, v236\n"
    "v_sub_f32 v215, v215, v236\n"
    "v_mul_f32 v32, v238, v32\n"
    "v_mul_f32 v33, v238, v33\n"
    "v_mul_f32 v34, v238, v34\n"
    "v_mul_f32 v35, v238, v35\n"
    "v_mul_f32 v36, v238, v36\n"
    "v_mul_f32 v37, v238, v37\n"
    "v_mul_f32 v38, v238, v38\n"
    "v_mul_f32 v39, v238, v39\n"
    "v_mul_f32 v40, v238, v40\n"
    "v_mul_f32 v41, v238, v41\n"
    "v_mul_f32 v42, v238, v42\n"
    "v_mul_f32 v43, v238, v43\n"
    "v_mul_f32 v44, v238, v44\n"
    "v_mul_f32 v45, v238, v45\n"
    "v_mul_f32 v46, v238, v46\n"
    "v_mul_f32 v47, v238, v47\n"
    "v_mul_f32 v48, v238, v48\n"
    "v_mul_f32 v49, v238, v49\n"
    "v_mul_f32 v50, v238, v50\n"
    "v_mul_f32 v51, v238, v51\n"
    "v_mul_f32 v52, v238, v52\n"
    "v_mul_f32 v53, v238, v53\n"
    "v_mul_f32 v54, v238, v54\n"
    "v_mul_f32 v55, v238, v55\n"
    "v_mul_f32 v56, v238, v56\n"
    "v_mul_f32 v57, v238, v57\n"
    "v_mul_f32 v58, v238, v58\n"
    "v_mul_f32 v59, v238, v59\n"
    "v_mul_f32 v60, v238, v60\n"
    "v_mul_f32 v61, v238, v61\n"
    "v_mul_f32 v62, v238, v62\n"
    "v_mul_f32 v63, v238, v63\n"
    "v_fma_f32 v237, v136, s36, v212\n"
    "v_fma_f32 v239, v137, s36, v212\n"
    "v_exp_f32 v237, v237\n"
    "v_exp_f32 v239, v239\n"
    "s_nop 0\n"
    "v_cvt_pk_bf16_f32 v196, v237, v239\n"
    "v_fma_f32 v237, v138, s36, v212\n"
    "v_fma_f32 v239, v139, s36, v212\n"
    "v_exp_f32 v237, v237\n"
    "v_exp_f32 v239, v239\n"
    "s_nop 0\n"
    "v_cvt_pk_bf16_f32 v197, v237, v239\n"
    "v_fma_f32 v237, v140, s36, v212\n"
    "v_fma_f32 v239, v141, s36, v212\n"
    "v_exp_f32 v237, v237\n"
    "v_exp_f32 v239, v239\n"
    "s_nop 0\n"
    "v_cvt_pk_bf16_f32 v198, v237, v239\n"
    "v_fma_f32 v237, v142, s36, v212\n"
    "v_fma_f32 v239, v143, s36, v212\n"
    "v_exp_f32 v237, v237\n"
    "v_exp_f32 v239, v239\n"
    "s_nop 0\n"
    "v_cvt_pk_bf16_f32 v199, v237, v239\n"
    "v_max3_f32 v236, v144, v145, v146\n"
    "v_max3_f32 v236, v236, v147, v148\n"
    "v_max3_f32 v236, v236, v149, v150\n"
    "v_max_f32 v236, v236, v151\n"
    "v_mov_b32 v237, v236\n"
    "s_nop 1\n"
    "v_permlane16_swap_b32 v236, v237\n"
    "v_max_f32 v236, v236, v237\n"
    "v_mov_b32 v237, v236\n"
    "s_nop 1\n"
    "v_permlane32_swap_b32 v236, v237\n"
    "v_max_f32 v236, v236, v237\n"
    "v_fma_f32 v236, v236, s36, v216\n"
    "v_log_f32 v237, v230\n"
    "s_nop 0\n"
    "v_add_f32 v237, 0xc1300000, v237\n"
    "v_max3_f32 v236, v236, v237, 0\n"
    "v_exp_f32 v238, -v236\n"
    "s_nop 0\n"
    "v_mul_f32 v230, v238, v230\n"
    "v_sub_f32 v216, v216, v236\n"
    "v_sub_f32 v217, v217, v236\n"
    "v_sub_f32 v218, v218, v236\n"
    "v_sub_f32 v219, v219, v236\n"
    "v_accvgpr_read_b32 v239, a176\n"
    "v_mul_f32 v239, v238, v239\n"
    "v_accvgpr_write_b32 a176, v239\n"
    "v_accvgpr_read_b32 v239, a177\n"
    "v_mul_f32 v239, v238, v239\n"
    "v_accvgpr_write_b32 a177, v239\n"
    "v_accvgpr_read_b32 v239, a178\n"
    "v_mul_f32 v239, v238, v239\n"
    "v_accvgpr_write_b32 a178, v239\n"
    "v_accvgpr_read_b32 v239, a179\n"
    "v_mul_f32 v239, v238, v239\n"
    "v_accvgpr_write_b32 a179, v239\n"
    "v_accvgpr_read_b32 v239, a180\n"
    "v_mul_f32 v239, v238, v239\n"
    "v_accvgpr_write_b32 a180, v239\n"
    "v_accvgpr_read_b32 v239, a181\n"
    "v_mul_f32 v239, v238, v239\n"
    "v_accvgpr_write_b32 a181, v239\n"
    "v_accvgpr_read_b32 v239, a182\n"
    "v_mul_f32 v239, v238, v239\n"
    "v_accvgpr_write_b32 a182, v239\n"
    "v_accvgpr_read_b32 v239, a183\n"
    "v_mul_f32 v239, v238, v239\n"
    "v_accvgpr_write_b32 a183, v239\n"
    "v_accvgpr_read_b32 v239, a184\n"
    "v_mul_f32 v239, v238, v239\n"
    "v_accvgpr_write_b32 a184, v239\n"
    "v_accvgpr_read_b32 v239, a185\n"
    "v_mul_f32 v239, v238, v239\n"
    "v_accvgpr_write_b32 a185, v239\n"
    "v_accvgpr_read_b32 v239, a186\n"
    "v_mul_f32 v239, v238, v239\n"
    "v_accvgpr_write_b32 a186, v239\n"
    "v_accvgpr_read_b32 v239, a187\n"
    "v_mul_f32 v239, v238, v239\n"
    "v_accvgpr_write_b32 a187, v239\n"
    "v_accvgpr_read_b32 v239, a188\n"
    "v_mul_f32 v239, v238, v239\n"
    "v_accvgpr_write_b32 a188, v239\n"
    "v_accvgpr_read_b32 v239, a189\n"
    "v_mul_f32 v239, v238, v239\n"
    "v_accvgpr_write_b32 a189, v239\n"
    "v_accvgpr_read_b32 v239, a190\n"
    "v_mul_f32 v239, v238, v239\n"
    "v_accvgpr_write_b32 a190, v239\n"
    "v_accvgpr_read_b32 v239, a191\n"
    "v_mul_f32 v239, v238, v239\n"
    "v_accvgpr_write_b32 a191, v239\n"
    "v_accvgpr_read_b32 v239, a192\n"
    "v_mul_f32 v239, v238, v239\n"
    "v_accvgpr_write_b32 a192, v239\n"
    "v_accvgpr_read_b32 v239, a193\n"
    "v_mul_f32 v239, v238, v239\n"
    "v_accvgpr_write_b32 a193, v239\n"
    "v_accvgpr_read_b32 v239, a194\n"
    "v_mul_f32 v239, v238, v239\n"
    "v_accvgpr_write_b32 a194, v239\n"
    "v_accvgpr_read_b32 v239, a195\n"
    "v_mul_f32 v239, v238, v239\n"
    "v_accvgpr_write_b32 a195, v239\n"
    "v_accvgpr_read_b32 v239, a196\n"
    "v_mul_f32 v239, v238, v239\n"
    "v_accvgpr_write_b32 a196, v239\n"
    "v_accvgpr_read_b32 v239, a197\n"
    "v_mul_f32 v239, v238, v239\n"
    "v_accvgpr_write_b32 a197, v239\n"
    "v_accvgpr_read_b32 v239, a198\n"
    "v_mul_f32 v239, v238, v239\n"
    "v_accvgpr_write_b32 a198, v239\n"
    "v_accvgpr_read_b32 v239, a199\n"
    "v_mul_f32 v239, v238, v239\n"
    "v_accvgpr_write_b32 a199, v239\n"
    "v_accvgpr_read_b32 v239, a200\n"
    "v_mul_f32 v239, v238, v239\n"
    "v_accvgpr_write_b32 a200, v239\n"
    "v_accvgpr_read_b32 v239, a201\n"
    "v_mul_f32 v239, v238, v239\n"
    "v_accvgpr_write_b32 a201, v239\n"
    "v_accvgpr_read_b32 v239, a202\n"
    "v_mul_f32 v239, v238, v239\n"
    "v_accvgpr_write_b32 a202, v239\n"
    "v_accvgpr_read_b32 v239, a203\n"
    "v_mul_f32 v239, v238, v239\n"
    "v_accvgpr_write_b32 a203, v239\n"
    "v_accvgpr_read_b32 v239, a204\n"
    "v_mul_f32 v239, v238, v239\n"
    "v_accvgpr_write_b32 a204, v239\n"
    "v_accvgpr_read_b32 v239, a205\n"
    "v_mul_f32 v239, v238, v239\n"
    "v_accvgpr_write_b32 a205, v239\n"
    "v_accvgpr_read_b32 v239, a206\n"
    "v_mul_f32 v239, v238, v239\n"
    "v_accvgpr_write_b32 a206, v239\n"
    "v_accvgpr_read_b32 v239, a207\n"
    "v_mul_f32 v239, v238, v239\n"
    "v_accvgpr_write_b32 a207, v239\n"
    "v_fma_f32 v237, v144, s36, v216\n"
    "v_fma_f32 v239, v145, s36, v216\n"
    "v_exp_f32 v237, v237\n"
    "v_exp_f32 v239, v239\n"
    "s_nop 0\n"
    "v_cvt_pk_bf16_f32 v200, v237, v239\n"
    "v_fma_f32 v237, v146, s36, v216\n"
    "v_fma_f32 v239, v147, s36, v216\n"
    "v_exp_f32 v237, v237\n"
    "v_exp_f32 v239, v239\n"
    "s_nop 0\n"
    "v_cvt_pk_bf16_f32 v201, v237, v239\n"
    "v_fma_f32 v237, v148, s36, v216\n"
    "v_fma_f32 v239, v149, s36, v216\n"
    "v_exp_f32 v237, v237\n"
    "v_exp_f32 v239, v239\n"
    "s_nop 0\n"
    "v_cvt_pk_bf16_f32 v202, v237, v239\n"
    "v_fma_f32 v237, v150, s36, v216\n"
    "v_fma_f32 v239, v151, s36, v216\n"
    "v_exp_f32 v237, v237\n"
    "v_exp_f32 v239, v239\n"
    "s_nop 0\n"
    "v_cvt_pk_bf16_f32 v203, v237, v239\n"
    "v_max3_f32 v236, v152, v153, v154\n"
    "v_max3_f32 v236, v236, v155, v156\n"
    "v_max3_f32 v236, v236, v157, v158\n"
    "v_max_f32 v236, v236, v159\n"
    "v_mov_b32 v237, v236\n"
    "s_nop 1\n"
    "v_permlane16_swap_b32 v236, v237\n"
    "v_max_f32 v236, v236, v237\n"
    "v_mov_b32 v237, v236\n"
    "s_nop 1\n"
    "v_permlane32_swap_b32 v236, v237\n"
    "v_max_f32 v236, v236, v237\n"
    "v_fma_f32 v236, v236, s36, v220\n"
    "v_log_f32 v237, v231\n"
    "s_nop 0\n"
    "v_add_f32 v237, 0xc1300000, v237\n"
    "v_max3_f32 v236, v236, v237, 0\n"
    "v_exp_f32 v238, -v236\n"
    "s_nop 0\n"
    "v_mul_f32 v231, v238, v231\n"
    "v_sub_f32 v220, v220, v236\n"
    "v_sub_f32 v221, v221, v236\n"
    "v_sub_f32 v222, v222, v236\n"
    "v_sub_f32 v223, v223, v236\n"
    "v_accvgpr_read_b32 v239, a208\n"
    "v_mul_f32 v239, v238, v239\n"
    "v_accvgpr_write_b32 a208, v239\n"
    "v_accvgpr_read_b32 v239, a209\n"
    "v_mul_f32 v239, v238, v239\n"
    "v_accvgpr_write_b32 a209, v239\n"
    "v_accvgpr_read_b32 v239, a210\n"
    "v_mul_f32 v239, v238, v239\n"
    "v_accvgpr_write_b32 a210, v239\n"
    "v_accvgpr_read_b32 v239, a211\n"
    "v_mul_f32 v239, v238, v239\n"
    "v_accvgpr_write_b32 a211, v239\n"
    "v_accvgpr_read_b32 v239, a212\n"
    "v_mul_f32 v239, v238, v239\n"
    "v_accvgpr_write_b32 a212, v239\n"
    "v_accvgpr_read_b32 v239, a213\n"
    "v_mul_f32 v239, v238, v239\n"
    "v_accvgpr_write_b32 a213, v239\n"
    "v_accvgpr_read_b32 v239, a214\n"
    "v_mul_f32 v239, v238, v239\n"
    "v_accvgpr_write_b32 a214, v239\n"
    "v_accvgpr_read_b32 v239, a215\n"
    "v_mul_f32 v239, v238, v239\n"
    "v_accvgpr_write_b32 a215, v239\n"
    "v_accvgpr_read_b32 v239, a216\n"
    "v_mul_f32 v239, v238, v239\n"
    "v_accvgpr_write_b32 a216, v239\n"
    "v_accvgpr_read_b32 v239, a217\n"
    "v_mul_f32 v239, v238, v239\n"
    "v_accvgpr_write_b32 a217, v239\n"
    "v_accvgpr_read_b32 v239, a218\n"
    "v_mul_f32 v239, v238, v239\n"
    "v_accvgpr_write_b32 a218, v239\n"
    "v_accvgpr_read_b32 v239, a219\n"
    "v_mul_f32 v239, v238, v239\n"
    "v_accvgpr_write_b32 a219, v239\n"
    "v_accvgpr_read_b32 v239, a220\n"
    "v_mul_f32 v239, v238, v239\n"
    "v_accvgpr_write_b32 a220, v239\n"
    "v_accvgpr_read_b32 v239, a221\n"
    "v_mul_f32 v239, v238, v239\n"
    "v_accvgpr_write_b32 a221, v239\n"
    "v_accvgpr_read_b32 v239, a222\n"
    "v_mul_f32 v239, v238, v239\n"
    "v_accvgpr_write_b32 a222, v239\n"
    "v_accvgpr_read_b32 v239, a223\n"
    "v_mul_f32 v239, v238, v239\n"
    "v_accvgpr_write_b32 a223, v239\n"
    "v_accvgpr_read_b32 v239, a224\n"
    "v_mul_f32 v239, v238, v239\n"
    "v_accvgpr_write_b32 a224, v239\n"
    "v_accvgpr_read_b32 v239, a225\n"
    "v_mul_f32 v239, v238, v239\n"
    "v_accvgpr_write_b32 a225, v239\n"
    "v_accvgpr_read_b32 v239, a226\n"
    "v_mul_f32 v239, v238, v239\n"
    "v_accvgpr_write_b32 a226, v239\n"
    "v_accvgpr_read_b32 v239, a227\n"
    "v_mul_f32 v239, v238, v239\n"
    "v_accvgpr_write_b32 a227, v239\n"
    "v_accvgpr_read_b32 v239, a228\n"
    "v_mul_f32 v239, v238, v239\n"
    "v_accvgpr_write_b32 a228, v239\n"
    "v_accvgpr_read_b32 v239, a229\n"
    "v_mul_f32 v239, v238, v239\n"
    "v_accvgpr_write_b32 a229, v239\n"
    "v_accvgpr_read_b32 v239, a230\n"
    "v_mul_f32 v239, v238, v239\n"
    "v_accvgpr_write_b32 a230, v239\n"
    "v_accvgpr_read_b32 v239, a231\n"
    "v_mul_f32 v239, v238, v239\n"
    "v_accvgpr_write_b32 a231, v239\n"
    "v_accvgpr_read_b32 v239, a232\n"
    "v_mul_f32 v239, v238, v239\n"
    "v_accvgpr_write_b32 a232, v239\n"
    "v_accvgpr_read_b32 v239, a233\n"
    "v_mul_f32 v239, v238, v239\n"
    "v_accvgpr_write_b32 a233, v239\n"
    "v_accvgpr_read_b32 v239, a234\n"
    "v_mul_f32 v239, v238, v239\n"
    "v_accvgpr_write_b32 a234, v239\n"
    "v_accvgpr_read_b32 v239, a235\n"
    "v_mul_f32 v239, v238, v239\n"
    "v_accvgpr_write_b32 a235, v239\n"
    "v_accvgpr_read_b32 v239, a236\n"
    "v_mul_f32 v239, v238, v239\n"
    "v_accvgpr_write_b32 a236, v239\n"
    "v_accvgpr_read_b32 v239, a237\n"
    "v_mul_f32 v239, v238, v239\n"
    "v_accvgpr_write_b32 a237, v239\n"
    "v_accvgpr_read_b32 v239, a238\n"
    "v_mul_f32 v239, v238, v239\n"
    "v_accvgpr_write_b32 a238, v239\n"
    "v_accvgpr_read_b32 v239, a239\n"
    "v_mul_f32 v239, v238, v239\n"
    "v_accvgpr_write_b32 a239, v239\n"
    "v_fma_f32 v237, v152, s36, v220\n"
    "v_fma_f32 v239, v153, s36, v220\n"
    "v_exp_f32 v237, v237\n"
    "v_exp_f32 v239, v239\n"
    "s_nop 0\n"
    "v_cvt_pk_bf16_f32 v204, v237, v239\n"
    "v_fma_f32 v237, v154, s36, v220\n"
    "v_fma_f32 v239, v155, s36, v220\n"
    "v_exp_f32 v237, v237\n"
    "v_exp_f32 v239, v239\n"
    "s_nop 0\n"
    "v_cvt_pk_bf16_f32 v205, v237, v239\n"
    "v_fma_f32 v237, v156, s36, v220\n"
    "v_fma_f32 v239, v157, s36, v220\n"
    "v_exp_f32 v237, v237\n"
    "v_exp_f32 v239, v239\n"
    "s_nop 0\n"
    "v_cvt_pk_bf16_f32 v206, v237, v239\n"
    "v_fma_f32 v237, v158, s36, v220\n"
    "v_fma_f32 v239, v159, s36, v220\n"
    "v_exp_f32 v237, v237\n"
    "v_exp_f32 v239, v239\n"
    "s_nop 0\n"
    "v_cvt_pk_bf16_f32 v207, v237, v239\n"
    "s_nop 1\n"
    "v_mfma_f32_16x16x32_bf16 v[224:227], a[160:163], v[192:195], v[228:231]\n"
    "v_mfma_f32_16x16x32_bf16 v[224:227], a[164:167], v[196:199], v[224:227]\n"
    "v_mfma_f32_16x16x32_bf16 v[224:227], a[168:171], v[200:203], v[224:227]\n"
    "v_mfma_f32_16x16x32_bf16 v[224:227], a[172:175], v[204:207], v[224:227]\n"
    "v_fma_f32 v232, v168, s36, v212\n"
    "v_fma_f32 v233, v169, s36, v212\n"
    "v_fma_f32 v234, v170, s36, v212\n"
    "v_fma_f32 v235, v171, s36, v212\n"
    "v_exp_f32 v232, v232\n"
    "v_exp_f32 v233, v233\n"
    "v_exp_f32 v234, v234\n"
    "v_exp_f32 v235, v235\n"
    "s_nop 7\n"
    "s_nop 7\n"
    "s_add_u32 s86, s86, 4\n"
    "s_addc_u32 s87, s87, 0\n"
    "s_cmp_lg_u32 s34, 0\n"
    "s_setpc_b64 s[86:87]\n"
    "L_rare_0SP_0123_:\n"
    "s_cselect_b32 s34, 1, 0\n"
    "s_nop 7\n"
    "s_nop 7\n"
    "v_max3_f32 v236, v128, v129, v130\n"
    "v_max3_f32 v236, v236, v131, v132\n"
    "v_max3_f32 v236, v236, v133, v134\n"
    "v_max_f32 v236, v236, v135\n"
    "v_mov_b32 v237, v236\n"
    "s_nop 1\n"
    "v_permlane16_swap_b32 v236, v237\n"
    "v_max_f32 v236, v236, v237\n"
    "v_mov_b32 v237, v236\n"
    "s_nop 1\n"
    "v_permlane32_swap_b32 v236, v237\n"
    "v_max_f32 v236, v236, v237\n"
    "v_fma_f32 v236, v236, s36, v208\n"
    "v_log_f32 v237, v228\n"
    "s_nop 0\n"
    "v_add_f32 v237, 0xc1300000, v237\n"
    "v_max3_f32 v236, v236, v237, 0\n"
    "v_exp_f32 v238, -v236\n"
    "s_nop 0\n"
    "v_mul_f32 v228, v238, v228\n"
    "v_sub_f32 v208, v208, v236\n"
    "v_sub_f32 v209, v209, v236\n"
    "v_sub_f32 v210, v210, v236\n"
    "v_sub_f32 v211, v211, v236\n"
    "v_mul_f32 v0, v238, v0\n"
    "v_mul_f32 v1, v238, v1\n"
    "v_mul_f32 v2, v238, v2\n"
    "v_mul_f32 v3, v238, v3\n"
    "v_mul_f32 v4, v238, v4\n"
    "v_mul_f32 v5, v238, v5\n"
    "v_mul_f32 v6, v238, v6\n"
    "v_mul_f32 v7, v238, v7\n"
    "v_mul_f32 v8, v238, v8\n"
    "v_mul_f32 v9, v238, v9\n"
    "v_mul_f32 v10, v238, v10\n"
    "v_mul_f32 v11, v238, v11\n"
    "v_mul_f32 v12, v238, v12\n"
    "v_mul_f32 v13, v238, v13\n"
    "v_mul_f32 v14, v238, v14\n"
    "v_mul_f32 v15, v238, v15\n"
    "v_mul_f32 v16, v238, v16\n"
    "v_mul_f32 v17, v238, v17\n"
    "v_mul_f32 v18, v238, v18\n"
    "v_mul_f32 v19, v238, v19\n"
    "v_mul_f32 v20, v238, v20\n"
    "v_mul_f32 v21, v238, v21\n"
    "v_mul_f32 v22, v238, v22\n"
    "v_mul_f32 v23, v238, v23\n"
    "v_mul_f32 v24, v238, v24\n"
    "v_mul_f32 v25, v238, v25\n"
    "v_mul_f32 v26, v238, v26\n"
    "v_mul_f32 v27, v238, v27\n"
    "v_mul_f32 v28, v238, v28\n"
    "v_mul_f32 v29, v238, v29\n"
    "v_mul_f32 v30, v238, v30\n"
    "v_mul_f32 v31, v238, v31\n"
    "v_fma_f32 v237, v128, s36, v208\n"
    "v_fma_f32 v239, v129, s36, v208\n"
    "v_exp_f32 v237, v237\n"
    "v_exp_f32 v239, v239\n"
    "s_nop 0\n"
    "v_cvt_pk_bf16_f32 v192, v237, v239\n"
    "v_fma_f32 v237, v130, s36, v208\n"
    "v_fma_f32 v239, v131, s36, v208\n"
    "v_exp_f32 v237, v237\n"
    "v_exp_f32 v239, v239\n"
    "s_nop 0\n"
    "v_cvt_pk_bf16_f32 v193, v237, v239\n"
    "v_fma_f32 v237, v132, s36, v208\n"
    "v_fma_f32 v239, v133, s36, v208\n"
    "v_exp_f32 v237, v237\n"
    "v_exp_f32 v239, v239\n"
    "s_nop 0\n"
    "v_cvt_pk_bf16_f32 v194, v237, v239\n"
    "v_fma_f32 v237, v134, s36, v208\n"
    "v_fma_f32 v239, v135, s36, v208\n"
    "v_exp_f32 v237, v237\n"
    "v_exp_f32 v239, v239\n"
    "s_nop 0\n"
    "v_cvt_pk_bf16_f32 v195, v237, v239\n"
    "v_max3_f32 v236, v136, v137, v138\n"
    "v_max3_f32 v236, v236, v139, v140\n"
    "v_max3_f32 v236, v236, v141, v142\n"
    "v_max_f32 v236, v236, v143\n"
    "v_mov_b32 v237, v236\n"
    "s_nop 1\n"
    "v_permlane16_swap_b32 v236, v237\n"
    "v_max_f32 v236, v236, v237\n"
    "v_mov_b32 v237, v236\n"
    "s_nop 1\n"
    "v_permlane32_swap_b32 v236, v237\n"
    "v_max_f32 v236, v236, v237\n"
    "v_fma_f32 v236, v236, s36, v212\n"
    "v_log_f32 v237, v229\n"
    "s_nop 0\n"
    "v_add_f32 v237, 0xc1300000, v237\n"
    "v_max3_f32 v236, v236, v237, 0\n"
    "v_exp_f32 v238, -v236\n"
    "s_nop 0\n"
    "v_mul_f32 v229, v238, v229\n"
    "v_sub_f32 v212, v212, v236\n"
    "v_sub_f32 v213, v213, v236\n"
    "v_sub_f32 v214, v214, v236\n"
    "v_sub_f32 v215, v215, v236\n"
    "v_mul_f32 v32, v238, v32\n"
    "v_mul_f32 v33, v238, v33\n"
    "v_mul_f32 v34, v238, v34\n"
    "v_mul_f32 v35, v238, v35\n"
    "v_mul_f32 v36, v238, v36\n"
    "v_mul_f32 v37, v238, v37\n"
    "v_mul_f32 v38, v238, v38\n"
    "v_mul_f32 v39, v238, v39\n"
    "v_mul_f32 v40, v238, v40\n"
    "v_mul_f32 v41, v238, v41\n"
    "v_mul_f32 v42, v238, v42\n"
    "v_mul_f32 v43, v238, v43\n"
    "v_mul_f32 v44, v238, v44\n"
    "v_mul_f32 v45, v238, v45\n"
    "v_mul_f32 v46, v238, v46\n"
    "v_mul_f32 v47, v238, v47\n"
    "v_mul_f32 v48, v238, v48\n"
    "v_mul_f32 v49, v238, v49\n"
    "v_mul_f32 v50, v238, v50\n"
    "v_mul_f32 v51, v238, v51\n"
    "v_mul_f32 v52, v238, v52\n"
    "v_mul_f32 v53, v238, v53\n"
    "v_mul_f32 v54, v238, v54\n"
    "v_mul_f32 v55, v238, v55\n"
    "v_mul_f32 v56, v238, v56\n"
    "v_mul_f32 v57, v238, v57\n"
    "v_mul_f32 v58, v238, v58\n"
    "v_mul_f32 v59, v238, v59\n"
    "v_mul_f32 v60, v238, v60\n"
    "v_mul_f32 v61, v238, v61\n"
    "v_mul_f32 v62, v238, v62\n"
    "v_mul_f32 v63, v238, v63\n"
    "v_fma_f32 v237, v136, s36, v212\n"
    "v_fma_f32 v239, v137, s36, v212\n"
    "v_exp_f32 v237, v237\n"
    "v_exp_f32 v239, v239\n"
    "s_nop 0\n"
    "v_cvt_pk_bf16_f32 v196, v237, v239\n"
    "v_fma_f32 v237, v138, s36, v212\n"
    "v_fma_f32 v239, v139, s36, v212\n"
    "v_exp_f32 v237, v237\n"
    "v_exp_f32 v239, v239\n"
    "s_nop 0\n"
    "v_cvt_pk_bf16_f32 v197, v237, v239\n"
    "v_fma_f32 v237, v140, s36, v212\n"
    "v_fma_f32 v239, v141, s36, v212\n"
    "v_exp_f32 v237, v237\n"
    "v_exp_f32 v239, v239\n"
    "s_nop 0\n"
    "v_cvt_pk_bf16_f32 v198, v237, v239\n"
    "v_fma_f32 v237, v142, s36, v212\n"
    "v_fma_f32 v239, v143, s36, v212\n"
    "v_exp_f32 v237, v237\n"
    "v_exp_f32 v239, v239\n"
    "s_nop 0\n"
    "v_cvt_pk_bf16_f32 v199, v237, v239\n"
    "v_max3_f32 v236, v144, v145, v146\n"
    "v_max3_f32 v236, v236, v147, v148\n"
    "v_max3_f32 v236, v236, v149, v150\n"
    "v_max_f32 v236, v236, v151\n"
    "v_mov_b32 v237, v236\n"
    "s_nop 1\n"
    "v_permlane16_swap_b32 v236, v237\n"
    "v_max_f32 v236, v236, v237\n"
    "v_mov_b32 v237, v236\n"
    "s_nop 1\n"
    "v_permlane32_swap_b32 v236, v237\n"
    "v_max_f32 v236, v236, v237\n"
    "v_fma_f32 v236, v236, s36, v244\n"
    "v_log_f32 v237, v230\n"
    "s_nop 0\n"
    "v_add_f32 v237, 0xc1300000, v237\n"
    "v_max3_f32 v236, v236, v237, 0\n"
    "v_exp_f32 v238, -v236\n"
    "s_nop 0\n"
    "v_mul_f32 v230, v238, v230\n"
    "v_sub_f32 v244, v244, v236\n"
    "v_mul_f32 v64, v238, v64\n"
    "v_mul_f32 v65, v238, v65\n"
    "v_mul_f32 v66, v238, v66\n"
    "v_mul_f32 v67, v238, v67\n"
    "v_mul_f32 v68, v238, v68\n"
    "v_mul_f32 v69, v238, v69\n"
    "v_mul_f32 v70, v238, v70\n"
    "v_mul_f32 v71, v238, v71\n"
    "v_mul_f32 v72, v238, v72\n"
    "v_mul_f32 v73, v238, v73\n"
    "v_mul_f32 v74, v238, v74\n"
    "v_mul_f32 v75, v238, v75\n"
    "v_mul_f32 v76, v238, v76\n"
    "v_mul_f32 v77, v238, v77\n"
    "v_mul_f32 v78, v238, v78\n"
    "v_mul_f32 v79, v238, v79\n"
    "v_mul_f32 v80, v238, v80\n"
    "v_mul_f32 v81, v238, v81\n"
    "v_mul_f32 v82, v238, v82\n"
    "v_mul_f32 v83, v238, v83\n"
    "v_mul_f32 v84, v238, v84\n"
    "v_mul_f32 v85, v238, v85\n"
    "v_mul_f32 v86, v238, v86\n"
    "v_mul_f32 v87, v238, v87\n"
    "v_mul_f32 v88, v238, v88\n"
    "v_mul_f32 v89, v238, v89\n"
    "v_mul_f32 v90, v238, v90\n"
    "v_mul_f32 v91, v238, v91\n"
    "v_mul_f32 v92, v238, v92\n"
    "v_mul_f32 v93, v238, v93\n"
    "v_mul_f32 v94, v238, v94\n"
    "v_mul_f32 v95, v238, v95\n"
    "v_fma_f32 v237, v144, s36, v244\n"
    "v_fma_f32 v239, v145, s36, v244\n"
    "v_exp_f32 v237, v237\n"
    "v_exp_f32 v239, v239\n"
    "s_nop 0\n"
    "v_cvt_pk_bf16_f32 v200, v237, v239\n"
    "v_fma_f32 v237, v146, s36, v244\n"
    "v_fma_f32 v239, v147, s36, v244\n"
    "v_exp_f32 v237, v237\n"
    "v_exp_f32 v239, v239\n"
    "s_nop 0\n"
    "v_cvt_pk_bf16_f32 v201, v237, v239\n"
    "v_fma_f32 v237, v148, s36, v244\n"
    "v_fma_f32 v239, v149, s36, v244\n"
    "v_exp_f32 v237, v237\n"
    "v_exp_f32 v239, v239\n"
    "s_nop 0\n"
    "v_cvt_pk_bf16_f32 v202, v237, v239\n"
    "v_fma_f32 v237, v150, s36, v244\n"
    "v_fma_f32 v239, v151, s36, v244\n"
    "v_exp_f32 v237, v237\n"
    "v_exp_f32 v239, v239\n"
    "s_nop 0\n"
    "v_cvt_pk_bf16_f32 v203, v237, v239\n"
    "v_max3_f32 v236, v152, v153, v154\n"
    "v_max3_f32 v236, v236, v155, v156\n"
    "v_max3_f32 v236, v236, v157, v158\n"
    "v_max_f32 v236, v236, v159\n"
    "v_mov_b32 v237, v236\n"
    "s_nop 1\n"
    "v_permlane16_swap_b32 v236, v237\n"
    "v_max_f32 v236, v236, v237\n"
    "v_mov_b32 v237, v236\n"
    "s_nop 1\n"
    "v_permlane32_swap_b32 v236, v237\n"
    "v_max_f32 v236, v236, v237\n"
    "v_fma_f32 v236, v236, s36, v245\n"
    "v_log_f32 v237, v231\n"
    "s_nop 0\n"
    "v_add_f32 v237, 0xc1300000, v237\n"
    "v_max3_f32 v236, v236, v237, 0\n"
    "v_exp_f32 v238, -v236\n"
    "s_nop 0\n"
    "v_mul_f32 v231, v238, v231\n"
    "v_sub_f32 v245, v245, v236\n"
    "v_mul_f32 v96, v238, v96\n"
    "v_mul_f32 v97, v238, v97\n"
    "v_mul_f32 v98, v238, v98\n"
    "v_mul_f32 v99, v238, v99\n"
    "v_mul_f32 v100, v238, v100\n"
    "v_mul_f32 v101, v238, v101\n"
    "v_mul_f32 v102, v238, v102\n"
    "v_mul_f32 v103, v238, v103\n"
    "v_mul_f32 v104, v238, v104\n"
    "v_mul_f32 v105, v238, v105\n"
    "v_mul_f32 v106, v238, v106\n"
    "v_mul_f32 v107, v238, v107\n"
    "v_mul_f32 v108, v238, v108\n"
    "v_mul_f32 v109, v238, v109\n"
    "v_mul_f32 v110, v238, v110\n"
    "v_mul_f32 v111, v238, v111\n"
    "v_mul_f32 v112, v238, v112\n"
    "v_mul_f32 v113, v238, v113\n"
    "v_mul_f32 v114, v238, v114\n"
    "v_mul_f32 v115, v238, v115\n"
    "v_mul_f32 v116, v238, v116\n"
    "v_mul_f32 v117, v238, v117\n"
    "v_mul_f32 v118, v238, v118\n"
    "v_mul_f32 v119, v238, v119\n"
    "v_mul_f32 v120, v238, v120\n"
    "v_mul_f32 v121, v238, v121\n"
    "v_mul_f32 v122, v238, v122\n"
    "v_mul_f32 v123, v238, v123\n"
    "v_mul_f32 v124, v238, v124\n"
    "v_mul_f32 v125, v238, v125\n"
    "v_mul_f32 v126, v238, v126\n"
    "v_mul_f32 v127, v238, v127\n"
    "v_fma_f32 v237, v152, s36, v245\n"
    "v_fma_f32 v239, v153, s36, v245\n"
    "v_exp_f32 v237, v237\n"
    "v_exp_f32 v239, v239\n"
    "s_nop 0\n"
    "v_cvt_pk_bf16_f32 v204, v237, v239\n"
    "v_fma_f32 v237, v154, s36, v245\n"
    "v_fma_f32 v239, v155, s36, v245\n"
    "v_exp_f32 v237, v237\n"
    "v_exp_f32 v239, v239\n"
    "s_nop 0\n"
    "v_cvt_pk_bf16_f32 v205, v237, v239\n"
    "v_fma_f32 v237, v156, s36, v245\n"
    "v_fma_f32 v239, v157, s36, v245\n"
    "v_exp_f32 v237, v237\n"
    "v_exp_f32 v239, v239\n"
    "s_nop 0\n"
    "v_cvt_pk_bf16_f32 v206, v237, v239\n"
    "v_fma_f32 v237, v158, s36, v245\n"
    "v_fma_f32 v239, v159, s36, v245\n"
    "v_exp_f32 v237, v237\n"
    "v_exp_f32 v239, v239\n"
    "s_nop 0\n"
    "v_cvt_pk_bf16_f32 v207, v237, v239\n"
    "s_nop 1\n"
    "v_mfma_f32_16x16x32_bf16 v[224:227], a[160:163], v[192:195], v[228:231]\n"
    "v_mfma_f32_16x16x32_bf16 v[224:227], a[164:167], v[196:199], v[224:227]\n"
    "v_mfma_f32_16x16x32_bf16 v[224:227], a[168:171], v[200:203], v[224:227]\n"
    "v_mfma_f32_16x16x32_bf16 v[224:227], a[172:175], v[204:207], v[224:227]\n"
    "v_fma_f32 v232, v168, s36, v212\n"
    "v_fma_f32 v233, v169, s36, v212\n"
    "v_fma_f32 v234, v170, s36, v212\n"
    "v_fma_f32 v235, v171, s36, v212\n"
    "v_exp_f32 v232, v232\n"
    "v_exp_f32 v233, v233\n"
    "v_exp_f32 v234, v234\n"
    "v_exp_f32 v235, v235\n"
    "s_nop 7\n"
    "s_nop 7\n"
    "s_add_u32 s86, s86, 4\n"
    "s_addc_u32 s87, s87, 0\n"
    "s_cmp_lg_u32 s34, 0\n"
    "s_setpc_b64 s[86:87]\n"
    "L_rare_0SP_013_:\n"
    "s_cselect_b32 s34, 1, 0\n"
    "s_nop 7\n"
    "s_nop 7\n"
    "v_max3_f32 v236, v128, v129, v130\n"
    "v_max3_f32 v236, v236, v131, v132\n"
    "v_max3_f32 v236, v236, v133, v134\n"
    "v_max_f32 v236, v236, v135\n"
    "v_mov_b32 v237, v236\n"
    "s_nop 1\n"
    "v_permlane16_swap_b32 v236, v237\n"
    "v_max_f32 v236, v236, v237\n"
    "v_mov_b32 v237, v236\n"
    "s_nop 1\n"
    "v_permlane32_swap_b32 v236, v237\n"
    "v_max_f32 v236, v236, v237\n"
    "v_fma_f32 v236, v236, s36, v208\n"
    "v_log_f32 v237, v228\n"
    "s_nop 0\n"
    "v_add_f32 v237, 0xc1300000, v237\n"
    "v_max3_f32 v236, v236, v237, 0\n"
    "v_exp_f32 v238, -v236\n"
    "s_nop 0\n"
    "v_mul_f32 v228, v238, v228\n"
    "v_sub_f32 v208, v208, v236\n"
    "v_sub_f32 v209, v209, v236\n"
    "v_sub_f32 v210, v210, v236\n"
    "v_sub_f32 v211, v211, v236\n"
    "v_mul_f32 v0, v238, v0\n"
    "v_mul_f32 v1, v238, v1\n"
    "v_mul_f32 v2, v238, v2\n"
    "v_mul_f32 v3, v238, v3\n"
    "v_mul_f32 v4, v238, v4\n"
    "v_mul_f32 v5, v238, v5\n"
    "v_mul_f32 v6, v238, v6\n"
    "v_mul_f32 v7, v238, v7\n"
    "v_mul_f32 v8, v238, v8\n"
    "v_mul_f32 v9, v238, v9\n"
    "v_mul_f32 v10, v238, v10\n"
    "v_mul_f32 v11, v238, v11\n"
    "v_mul_f32 v12, v238, v12\n"
    "v_mul_f32 v13, v238, v13\n"
    "v_mul_f32 v14, v238, v14\n"
    "v_mul_f32 v15, v238, v15\n"
    "v_mul_f32 v16, v238, v16\n"
    "v_mul_f32 v17, v238, v17\n"
    "v_mul_f32 v18, v238, v18\n"
    "v_mul_f32 v19, v238, v19\n"
    "v_mul_f32 v20, v238, v20\n"
    "v_mul_f32 v21, v238, v21\n"
    "v_mul_f32 v22, v238, v22\n"
    "v_mul_f32 v23, v238, v23\n"
    "v_mul_f32 v24, v238, v24\n"
    "v_mul_f32 v25, v238, v25\n"
    "v_mul_f32 v26, v238, v26\n"
    "v_mul_f32 v27, v238, v27\n"
    "v_mul_f32 v28, v238, v28\n"
    "v_mul_f32 v29, v238, v29\n"
    "v_mul_f32 v30, v238, v30\n"
    "v_mul_f32 v31, v238, v31\n"
    "v_fma_f32 v237, v128, s36, v208\n"
    "v_fma_f32 v239, v129, s36, v208\n"
    "v_exp_f32 v237, v237\n"
    "v_exp_f32 v239, v239\n"
    "s_nop 0\n"
    "v_cvt_pk_bf16_f32 v192, v237, v239\n"
    "v_fma_f32 v237, v130, s36, v208\n"
    "v_fma_f32 v239, v131, s36, v208\n"
    "v_exp_f32 v237, v237\n"
    "v_exp_f32 v239, v239\n"
    "s_nop 0\n"
    "v_cvt_pk_bf16_f32 v193, v237, v239\n"
    "v_fma_f32 v237, v132, s36, v208\n"
    "v_fma_f32 v239, v133, s36, v208\n"
    "v_exp_f32 v237, v237\n"
    "v_exp_f32 v239, v239\n"
    "s_nop 0\n"
    "v_cvt_pk_bf16_f32 v194, v237, v239\n"
    "v_fma_f32 v237, v134, s36, v208\n"
    "v_fma_f32 v239, v135, s36, v208\n"
    "v_exp_f32 v237, v237\n"
    "v_exp_f32 v239, v239\n"
    "s_nop 0\n"
    "v_cvt_pk_bf16_f32 v195, v237, v239\n"
    "v_max3_f32 v236, v136, v137, v138\n"
    "v_max3_f32 v236, v236, v139, v140\n"
    "v_max3_f32 v236, v236, v141, v142\n"
    "v_max_f32 v236, v236, v143\n"
    "v_mov_b32 v237, v236\n"
    "s_nop 1\n"
    "v_permlane16_swap_b32 v236, v237\n"
    "v_max_f32 v236, v236, v237\n"
    "v_mov_b32 v237, v236\n"
    "s_nop 1\n"
    "v_permlane32_swap_b32 v236, v237\n"
    "v_max_f32 v236, v236, v237\n"
    "v_fma_f32 v236, v236, s36, v212\n"
    "v_log_f32 v237, v229\n"
    "s_nop 0\n"
    "v_add_f32 v237, 0xc1300000, v237\n"
    "v_max3_f32 v236, v236, v237, 0\n"
    "v_exp_f32 v238, -v236\n"
    "s_nop 0\n"
    "v_mul_f32 v229, v238, v229\n"
    "v_sub_f32 v212, v212, v236\n"
    "v_sub_f32 v213, v213, v236\n"
    "v_sub_f32 v214, v214, v236\n"
    "v_sub_f32 v215, v215, v236\n"
    "v_mul_f32 v32, v238, v32\n"
    "v_mul_f32 v33, v238, v33\n"
    "v_mul_f32 v34, v238, v34\n"
    "v_mul_f32 v35, v238, v35\n"
    "v_mul_f32 v36, v238, v36\n"
    "v_mul_f32 v37, v238, v37\n"
    "v_mul_f32 v38, v238, v38\n"
    "v_mul_f32 v39, v238, v39\n"
    "v_mul_f32 v40, v238, v40\n"
    "v_mul_f32 v41, v238, v41\n"
    "v_mul_f32 v42, v238, v42\n"
    "v_mul_f32 v43, v238, v43\n"
    "v_mul_f32 v44, v238, v44\n"
    "v_mul_f32 v45, v238, v45\n"
    "v_mul_f32 v46, v238, v46\n"
    "v_mul_f32 v47, v238, v47\n"
    "v_mul_f32 v48, v238, v48\n"
    "v_mul_f32 v49, v238, v49\n"
    "v_mul_f32 v50, v238, v50\n"
    "v_mul_f32 v51, v238, v51\n"
    "v_mul_f32 v52, v238, v52\n"
    "v_mul_f32 v53, v238, v53\n"
    "v_mul_f32 v54, v238, v54\n"
    "v_mul_f32 v55, v238, v55\n"
    "v_mul_f32 v56, v238, v56\n"
    "v_mul_f32 v57, v238, v57\n"
    "v_mul_f32 v58, v238, v58\n"
    "v_mul_f32 v59, v238, v59\n"
    "v_mul_f32 v60, v238, v60\n"
    "v_mul_f32 v61, v238, v61\n"
    "v_mul_f32 v62, v238, v62\n"
    "v_mul_f32 v63, v238, v63\n"
    "v_fma_f32 v237, v136, s36, v212\n"
    "v_fma_f32 v239, v137, s36, v212\n"
    "v_exp_f32 v237, v237\n"
    "v_exp_f32 v239, v239\n"
    "s_nop 0\n"
    "v_cvt_pk_bf16_f32 v196, v237, v239\n"
    "v_fma_f32 v237, v138, s36, v212\n"
    "v_fma_f32 v239, v139, s36, v212\n"
    "v_exp_f32 v237, v237\n"
    "v_exp_f32 v239, v239\n"
    "s_nop 0\n"
    "v_cvt_pk_bf16_f32 v197, v237, v239\n"
    "v_fma_f32 v237, v140, s36, v212\n"
    "v_fma_f32 v239, v141, s36, v212\n"
    "v_exp_f32 v237, v237\n"
    "v_exp_f32 v239, v239\n"
    "s_nop 0\n"
    "v_cvt_pk_bf16_f32 v198, v237, v239\n"
    "v_fma_f32 v237, v142, s36, v212\n"
    "v_fma_f32 v239, v143, s36, v212\n"
    "v_exp_f32 v237, v237\n"
    "v_exp_f32 v239, v239\n"
    "s_nop 0\n"
    "v_cvt_pk_bf16_f32 v199, v237, v239\n"
    "v_max3_f32 v236, v152, v153, v154\n"
    "v_max3_f32 v236, v236, v155, v156\n"
    "v_max3_f32 v236, v236, v157, v158\n"
    "v_max_f32 v236, v236, v159\n"
    "v_mov_b32 v237, v236\n"
    "s_nop 1\n"
    "v_permlane16_swap_b32 v236, v237\n"
    "v_max_f32 v236, v236, v237\n"
    "v_mov_b32 v237, v236\n"
    "s_nop 1\n"
    "v_permlane32_swap_b32 v236, v237\n"
    "v_max_f32 v236, v236, v237\n"
    "v_fma_f32 v236, v236, s36, v245\n"
    "v_log_f32 v237, v231\n"
    "s_nop 0\n"
    "v_add_f32 v237, 0xc1300000, v237\n"
    "v_max3_f32 v236, v236, v237, 0\n"
    "v_exp_f32 v238, -v236\n"
    "s_nop 0\n"
    "v_mul_f32 v231, v238, v231\n"
    "v_sub_f32 v245, v245, v236\n"
    "v_mul_f32 v96, v238, v96\n"
    "v_mul_f32 v97, v238, v97\n"
    "v_mul_f32 v98, v238, v98\n"
    "v_mul_f32 v99, v238, v99\n"
    "v_mul_f32 v100, v238, v100\n"
    "v_mul_f32 v101, v238, v101\n"
    "v_mul_f32 v102, v238, v102\n"
    "v_mul_f32 v103, v238, v103\n"
    "v_mul_f32 v104, v238, v104\n"
    "v_mul_f32 v105, v238, v105\n"
    "v_mul_f32 v106, v238, v106\n"
    "v_mul_f32 v107, v238, v107\n"
    "v_mul_f32 v108, v238, v108\n"
    "v_mul_f32 v109, v238, v109\n"
    "v_mul_f32 v110, v238, v110\n"
    "v_mul_f32 v111, v238, v111\n"
    "v_mul_f32 v112, v238, v112\n"
    "v_mul_f32 v113, v238, v113\n"
    "v_mul_f32 v114, v238, v114\n"
    "v_mul_f32 v115, v238, v115\n"
    "v_mul_f32 v116, v238, v116\n"
    "v_mul_f32 v117, v238, v117\n"
    "v_mul_f32 v118, v238, v118\n"
    "v_mul_f32 v119, v238, v119\n"
    "v_mul_f32 v120, v238, v120\n"
    "v_mul_f32 v121, v238, v121\n"
    "v_mul_f32 v122, v238, v122\n"
    "v_mul_f32 v123, v238, v123\n"
    "v_mul_f32 v124, v238, v124\n"
    "v_mul_f32 v125, v238, v125\n"
    "v_mul_f32 v126, v238, v126\n"
    "v_mul_f32 v127, v238, v127\n"
    "v_fma_f32 v237, v152, s36, v245\n"
    "v_fma_f32 v239, v153, s36, v245\n"
    "v_exp_f32 v237, v237\n"
    "v_exp_f32 v239, v239\n"
    "s_nop 0\n"
    "v_cvt_pk_bf16_f32 v204, v237, v239\n"
    "v_fma_f32 v237, v154, s36, v245\n"
    "v_fma_f32 v239, v155, s36, v245\n"
    "v_exp_f32 v237, v237\n"
    "v_exp_f32 v239, v239\n"
    "s_nop 0\n"
    "v_cvt_pk_bf16_f32 v205, v237, v239\n"
    "v_fma_f32 v237, v156, s36, v245\n"
    "v_fma_f32 v239, v157, s36, v245\n"
    "v_exp_f32 v237, v237\n"
    "v_exp_f32 v239, v239\n"
    "s_nop 0\n"
    "v_cvt_pk_bf16_f32 v206, v237, v239\n"
    "v_fma_f32 v237, v158, s36, v245\n"
    "v_fma_f32 v239, v159, s36, v245\n"
    "v_exp_f32 v237, v237\n"
    "v_exp_f32 v239, v239\n"
    "s_nop 0\n"
    "v_cvt_pk_bf16_f32 v207, v237, v239\n"
    "s_nop 1\n"
    "v_mfma_f32_16x16x32_bf16 v[224:227], a[160:163], v[192:195], v[228:231]\n"
    "v_mfma_f32_16x16x32_bf16 v[224:227], a[164:167], v[196:199], v[224:227]\n"
    "v_mfma_f32_16x16x32_bf16 v[224:227], a[172:175], v[204:207], v[224:227]\n"
    "v_fma_f32 v232, v168, s36, v212\n"
    "v_fma_f32 v233, v169, s36, v212\n"
    "v_fma_f32 v234, v170, s36, v212\n"
    "v_fma_f32 v235, v171, s36, v212\n"
    "v_exp_f32 v232, v232\n"
    "v_exp_f32 v233, v233\n"
    "v_exp_f32 v234, v234\n"
    "v_exp_f32 v235, v235\n"
    "s_nop 7\n"
    "s_nop 7\n"
    "s_add_u32 s86, s86, 4\n"
    "s_addc_u32 s87, s87, 0\n"
    "s_cmp_lg_u32 s34, 0\n"
    "s_setpc_b64 s[86:87]\n"
    "L_rare_1PP_0123_:\n"
    "s_cselect_b32 s34, 1, 0\n"
    "s_nop 7\n"
    "s_nop 7\n"
    "v_max3_f32 v236, v160, v161, v162\n"
    "v_max3_f32 v236, v236, v163, v164\n"
    "v_max3_f32 v236, v236, v165, v166\n"
    "v_max_f32 v236, v236, v167\n"
    "v_mov_b32 v237, v236\n"
    "s_nop 1\n"
    "v_permlane16_swap_b32 v236, v237\n"
    "v_max_f32 v236, v236, v237\n"
    "v_mov_b32 v237, v236\n"
    "s_nop 1\n"
    "v_permlane32_swap_b32 v236, v237\n"
    "v_max_f32 v236, v236, v237\n"
    "v_fma_f32 v236, v236, s36, v208\n"
    "v_log_f32 v237, v224\n"
    "s_nop 0\n"
    "v_add_f32 v237, 0xc1300000, v237\n"
    "v_max3_f32 v236, v236, v237, 0\n"
    "v_exp_f32 v238, -v236\n"
    "s_nop 0\n"
    "v_mul_f32 v224, v238, v224\n"
    "v_sub_f32 v208, v208, v236\n"
    "v_sub_f32 v209, v209, v236\n"
    "v_sub_f32 v210, v210, v236\n"
    "v_sub_f32 v211, v211, v236\n"
    "v_mul_f32 v0, v238, v0\n"
    "v_mul_f32 v1, v238, v1\n"
    "v_mul_f32 v2, v238, v2\n"
    "v_mul_f32 v3, v238, v3\n"
    "v_mul_f32 v4, v238, v4\n"
    "v_mul_f32 v5, v238, v5\n"
    "v_mul_f32 v6, v238, v6\n"
    "v_mul_f32 v7, v238, v7\n"
    "v_mul_f32 v8, v238, v8\n"
    "v_mul_f32 v9, v238, v9\n"
    "v_mul_f32 v10, v238, v10\n"
    "v_mul_f32 v11, v238, v11\n"
    "v_mul_f32 v12, v238, v12\n"
    "v_mul_f32 v13, v238, v13\n"
    "v_mul_f32 v14, v238, v14\n"
    "v_mul_f32 v15, v238, v15\n"
    "v_mul_f32 v16, v238, v16\n"
    "v_mul_f32 v17, v238, v17\n"
    "v_mul_f32 v18, v238, v18\n"
    "v_mul_f32 v19, v238, v19\n"
    "v_mul_f32 v20, v238, v20\n"
    "v_mul_f32 v21, v238, v21\n"
    "v_mul_f32 v22, v238, v22\n"
    "v_mul_f32 v23, v238, v23\n"
    "v_mul_f32 v24, v238, v24\n"
    "v_mul_f32 v25, v238, v25\n"
    "v_mul_f32 v26, v238, v26\n"
    "v_mul_f32 v27, v238, v27\n"
    "v_mul_f32 v28, v238, v28\n"
    "v_mul_f32 v29, v238, v29\n"
    "v_mul_f32 v30, v238, v30\n"
    "v_mul_f32 v31, v238, v31\n"
    "v_fma_f32 v237, v160, s36, v208\n"
    "v_fma_f32 v239, v161, s36, v208\n"
    "v_exp_f32 v237, v237\n"
    "v_exp_f32 v239, v239\n"
    "s_nop 0\n"
    "v_cvt_pk_bf16_f32 v192, v237, v239\n"
    "v_fma_f32 v237, v162, s36, v208\n"
    "v_fma_f32 v239, v163, s36, v208\n"
    "v_exp_f32 v237, v237\n"
    "v_exp_f32 v239, v239\n"
    "s_nop 0\n"
    "v_cvt_pk_bf16_f32 v193, v237, v239\n"
    "v_fma_f32 v237, v164, s36, v208\n"
    "v_fma_f32 v239, v165, s36, v208\n"
    "v_exp_f32 v237, v237\n"
    "v_exp_f32 v239, v239\n"
    "s_nop 0\n"
    "v_cvt_pk_bf16_f32 v194, v237, v239\n"
    "v_fma_f32 v237, v166, s36, v208\n"
    "v_fma_f32 v239, v167, s36, v208\n"
    "v_exp_f32 v237, v237\n"
    "v_exp_f32 v239, v239\n"
    "s_nop 0\n"
    "v_cvt_pk_bf16_f32 v195, v237, v239\n"
    "v_max3_f32 v236, v168, v169, v170\n"
    "v_max3_f32 v236, v236, v171, v172\n"
    "v_max3_f32 v236, v236, v173, v174\n"
    "v_max_f32 v236, v236, v175\n"
    "v_mov_b32 v237, v236\n"
    "s_nop 1\n"
    "v_permlane16_swap_b32 v236, v237\n"
    "v_max_f32 v236, v236, v237\n"
    "v_mov_b32 v237, v236\n"
    "s_nop 1\n"
    "v_permlane32_swap_b32 v236, v237\n"
    "v_max_f32 v236, v236, v237\n"
    "v_fma_f32 v236, v236, s36, v212\n"
    "v_log_f32 v237, v225\n"
    "s_nop 0\n"
    "v_add_f32 v237, 0xc1300000, v237\n"
    "v_max3_f32 v236, v236, v237, 0\n"
    "v_exp_f32 v238, -v236\n"
    "s_nop 0\n"
    "v_mul_f32 v225, v238, v225\n"
    "v_sub_f32 v212, v212, v236\n"
    "v_sub_f32 v213, v213, v236\n"
    "v_sub_f32 v214, v214, v236\n"
    "v_sub_f32 v215, v215, v236\n"
    "v_mul_f32 v32, v238, v32\n"
    "v_mul_f32 v33, v238, v33\n"
    "v_mul_f32 v34, v238, v34\n"
    "v_mul_f32 v35, v238, v35\n"
    "v_mul_f32 v36, v238, v36\n"
    "v_mul_f32 v37, v238, v37\n"
    "v_mul_f32 v38, v238, v38\n"
    "v_mul_f32 v39, v238, v39\n"
    "v_mul_f32 v40, v238, v40\n"
    "v_mul_f32 v41, v238, v41\n"
    "v_mul_f32 v42, v238, v42\n"
    "v_mul_f32 v43, v238, v43\n"
    "v_mul_f32 v44, v238, v44\n"
    "v_mul_f32 v45, v238, v45\n"
    "v_mul_f32 v46, v238, v46\n"
    "v_mul_f32 v47, v238, v47\n"
    "v_mul_f32 v48, v238, v48\n"
    "v_mul_f32 v49, v238, v49\n"
    "v_mul_f32 v50, v238, v50\n"
    "v_mul_f32 v51, v238, v51\n"
    "v_mul_f32 v52, v238, v52\n"
    "v_mul_f32 v53, v238, v53\n"
    "v_mul_f32 v54, v238, v54\n"
    "v_mul_f32 v55, v238, v55\n"
    "v_mul_f32 v56, v238, v56\n"
    "v_mul_f32 v57, v238, v57\n"
    "v_mul_f32 v58, v238, v58\n"
    "v_mul_f32 v59, v238, v59\n"
    "v_mul_f32 v60, v238, v60\n"
    "v_mul_f32 v61, v238, v61\n"
    "v_mul_f32 v62, v238, v62\n"
    "v_mul_f32 v63, v238, v63\n"
    "v_fma_f32 v237, v168, s36, v212\n"
    "v_fma_f32 v239, v169, s36, v212\n"
    "v_exp_f32 v237, v237\n"
    "v_exp_f32 v239, v239\n"
    "s_nop 0\n"
    "v_cvt_pk_bf16_f32 v196, v237, v239\n"
    "v_fma_f32 v237, v170, s36, v212\n"
    "v_fma_f32 v239, v171, s36, v212\n"
    "v_exp_f32 v237, v237\n"
    "v_exp_f32 v239, v239\n"
    "s_nop 0\n"
    "v_cvt_pk_bf16_f32 v197, v237, v239\n"
    "v_fma_f32 v237, v172, s36, v212\n"
    "v_fma_f32 v239, v173, s36, v212\n"
    "v_exp_f32 v237, v237\n"
    "v_exp_f32 v239, v239\n"
    "s_nop 0\n"
    "v_cvt_pk_bf16_f32 v198, v237, v239\n"
    "v_fma_f32 v237, v174, s36, v212\n"
    "v_fma_f32 v239, v175, s36, v212\n"
    "v_exp_f32 v237, v237\n"
    "v_exp_f32 v239, v239\n"
    "s_nop 0\n"
    "v_cvt_pk_bf16_f32 v199, v237, v239\n"
    "v_max3_f32 v236, v176, v177, v178\n"
    "v_max3_f32 v236, v236, v179, v180\n"
    "v_max3_f32 v236, v236, v181, v182\n"
    "v_max_f32 v236, v236, v183\n"
    "v_mov_b32 v237, v236\n"
    "s_nop 1\n"
    "v_permlane16_swap_b32 v236, v237\n"
    "v_max_f32 v236, v236, v237\n"
    "v_mov_b32 v237, v236\n"
    "s_nop 1\n"
    "v_permlane32_swap_b32 v236, v237\n"
    "v_max_f32 v236, v236, v237\n"
    "v_fma_f32 v236, v236, s36, v216\n"
    "v_log_f32 v237, v226\n"
    "s_nop 0\n"
    "v_add_f32 v237, 0xc1300000, v237\n"
    "v_max3_f32 v236, v236, v237, 0\n"
    "v_exp_f32 v238, -v236\n"
    "s_nop 0\n"
    "v_mul_f32 v226, v238, v226\n"
    "v_sub_f32 v216, v216, v236\n"
    "v_sub_f32 v217, v217, v236\n"
    "v_sub_f32 v218, v218, v236\n"
    "v_sub_f32 v219, v219, v236\n"
    "v_accvgpr_read_b32 v239, a176\n"
    "v_mul_f32 v239, v238, v239\n"
    "v_accvgpr_write_b32 a176, v239\n"
    "v_accvgpr_read_b32 v239, a177\n"
    "v_mul_f32 v239, v238, v239\n"
    "v_accvgpr_write_b32 a177, v239\n"
    "v_accvgpr_read_b32 v239, a178\n"
    "v_mul_f32 v239, v238, v239\n"
    "v_accvgpr_write_b32 a178, v239\n"
    "v_accvgpr_read_b32 v239, a179\n"
    "v_mul_f32 v239, v238, v239\n"
    "v_accvgpr_write_b32 a179, v239\n"
    "v_accvgpr_read_b32 v239, a180\n"
    "v_mul_f32 v239, v238, v239\n"
    "v_accvgpr_write_b32 a180, v239\n"
    "v_accvgpr_read_b32 v239, a181\n"
    "v_mul_f32 v239, v238, v239\n"
    "v_accvgpr_write_b32 a181, v239\n"
    "v_accvgpr_read_b32 v239, a182\n"
    "v_mul_f32 v239, v238, v239\n"
    "v_accvgpr_write_b32 a182, v239\n"
    "v_accvgpr_read_b32 v239, a183\n"
    "v_mul_f32 v239, v238, v239\n"
    "v_accvgpr_write_b32 a183, v239\n"
    "v_accvgpr_read_b32 v239, a184\n"
    "v_mul_f32 v239, v238, v239\n"
    "v_accvgpr_write_b32 a184, v239\n"
    "v_accvgpr_read_b32 v239, a185\n"
    "v_mul_f32 v239, v238, v239\n"
    "v_accvgpr_write_b32 a185, v239\n"
    "v_accvgpr_read_b32 v239, a186\n"
    "v_mul_f32 v239, v238, v239\n"
    "v_accvgpr_write_b32 a186, v239\n"
    "v_accvgpr_read_b32 v239, a187\n"
    "v_mul_f32 v239, v238, v239\n"
    "v_accvgpr_write_b32 a187, v239\n"
    "v_accvgpr_read_b32 v239, a188\n"
    "v_mul_f32 v239, v238, v239\n"
    "v_accvgpr_write_b32 a188, v239\n"
    "v_accvgpr_read_b32 v239, a189\n"
    "v_mul_f32 v239, v238, v239\n"
    "v_accvgpr_write_b32 a189, v239\n"
    "v_accvgpr_read_b32 v239, a190\n"
    "v_mul_f32 v239, v238, v239\n"
    "v_accvgpr_write_b32 a190, v239\n"
    "v_accvgpr_read_b32 v239, a191\n"
    "v_mul_f32 v239, v238, v239\n"
    "v_accvgpr_write_b32 a191, v239\n"
    "v_accvgpr_read_b32 v239, a192\n"
    "v_mul_f32 v239, v238, v239\n"
    "v_accvgpr_write_b32 a192, v239\n"
    "v_accvgpr_read_b32 v239, a193\n"
    "v_mul_f32 v239, v238, v239\n"
    "v_accvgpr_write_b32 a193, v239\n"
    "v_accvgpr_read_b32 v239, a194\n"
    "v_mul_f32 v239, v238, v239\n"
    "v_accvgpr_write_b32 a194, v239\n"
    "v_accvgpr_read_b32 v239, a195\n"
    "v_mul_f32 v239, v238, v239\n"
    "v_accvgpr_write_b32 a195, v239\n"
    "v_accvgpr_read_b32 v239, a196\n"
    "v_mul_f32 v239, v238, v239\n"
    "v_accvgpr_write_b32 a196, v239\n"
    "v_accvgpr_read_b32 v239, a197\n"
    "v_mul_f32 v239, v238, v239\n"
    "v_accvgpr_write_b32 a197, v239\n"
    "v_accvgpr_read_b32 v239, a198\n"
    "v_mul_f32 v239, v238, v239\n"
    "v_accvgpr_write_b32 a198, v239\n"
    "v_accvgpr_read_b32 v239, a199\n"
    "v_mul_f32 v239, v238, v239\n"
    "v_accvgpr_write_b32 a199, v239\n"
    "v_accvgpr_read_b32 v239, a200\n"
    "v_mul_f32 v239, v238, v239\n"
    "v_accvgpr_write_b32 a200, v239\n"
    "v_accvgpr_read_b32 v239, a201\n"
    "v_mul_f32 v239, v238, v239\n"
    "v_accvgpr_write_b32 a201, v239\n"
    "v_accvgpr_read_b32 v239, a202\n"
    "v_mul_f32 v239, v238, v239\n"
    "v_accvgpr_write_b32 a202, v239\n"
    "v_accvgpr_read_b32 v239, a203\n"
    "v_mul_f32 v239, v238, v239\n"
    "v_accvgpr_write_b32 a203, v239\n"
    "v_accvgpr_read_b32 v239, a204\n"
    "v_mul_f32 v239, v238, v239\n"
    "v_accvgpr_write_b32 a204, v239\n"
    "v_accvgpr_read_b32 v239, a205\n"
    "v_mul_f32 v239, v238, v239\n"
    "v_accvgpr_write_b32 a205, v239\n"
    "v_accvgpr_read_b32 v239, a206\n"
    "v_mul_f32 v239, v238, v239\n"
    "v_accvgpr_write_b32 a206, v239\n"
    "v_accvgpr_read_b32 v239, a207\n"
    "v_mul_f32 v239, v238, v239\n"
    "v_accvgpr_write_b32 a207, v239\n"
    "v_fma_f32 v237, v176, s36, v216\n"
    "v_fma_f32 v239, v177, s36, v216\n"
    "v_exp_f32 v237, v237\n"
    "v_exp_f32 v239, v239\n"
    "s_nop 0\n"
    "v_cvt_pk_bf16_f32 v200, v237, v239\n"
    "v_fma_f32 v237, v178, s36, v216\n"
    "v_fma_f32 v239, v179, s36, v216\n"
    "v_exp_f32 v237, v237\n"
    "v_exp_f32 v239, v239\n"
    "s_nop 0\n"
    "v_cvt_pk_bf16_f32 v201, v237, v239\n"
    "v_fma_f32 v237, v180, s36, v216\n"
    "v_fma_f32 v239, v181, s36, v216\n"
    "v_exp_f32 v237, v237\n"
    "v_exp_f32 v239, v239\n"
    "s_nop 0\n"
    "v_cvt_pk_bf16_f32 v202, v237, v239\n"
    "v_fma_f32 v237, v182, s36, v216\n"
    "v_fma_f32 v239, v183, s36, v216\n"
    "v_exp_f32 v237, v237\n"
    "v_exp_f32 v239, v239\n"
    "s_nop 0\n"
    "v_cvt_pk_bf16_f32 v203, v237, v239\n"
    "v_max3_f32 v236, v184, v185, v186\n"
    "v_max3_f32 v236, v236, v187, v188\n"
    "v_max3_f32 v236, v236, v189, v190\n"
    "v_max_f32 v236, v236, v191\n"
    "v_mov_b32 v237, v236\n"
    "s_nop 1\n"
    "v_permlane16_swap_b32 v236, v237\n"
    "v_max_f32 v236, v236, v237\n"
    "v_mov_b32 v237, v236\n"
    "s_nop 1\n"
    "v_permlane32_swap_b32 v236, v237\n"
    "v_max_f32 v236, v236, v237\n"
    "v_fma_f32 v236, v236, s36, v220\n"
    "v_log_f32 v237, v227\n"
    "s_nop 0\n"
    "v_add_f32 v237, 0xc1300000, v237\n"
    "v_max3_f32 v236, v236, v237, 0\n"
    "v_exp_f32 v238, -v236\n"
    "s_nop 0\n"
    "v_mul_f32 v227, v238, v227\n"
    "v_sub_f32 v220, v220, v236\n"
    "v_sub_f32 v221, v221, v236\n"
    "v_sub_f32 v222, v222, v236\n"
    "v_sub_f32 v223, v223, v236\n"
    "v_accvgpr_read_b32 v239, a208\n"
    "v_mul_f32 v239, v238, v239\n"
    "v_accvgpr_write_b32 a208, v239\n"
    "v_accvgpr_read_b32 v239, a209\n"
    "v_mul_f32 v239, v238, v239\n"
    "v_accvgpr_write_b32 a209, v239\n"
    "v_accvgpr_read_b32 v239, a210\n"
    "v_mul_f32 v239, v238, v239\n"
    "v_accvgpr_write_b32 a210, v239\n"
    "v_accvgpr_read_b32 v239, a211\n"
    "v_mul_f32 v239, v238, v239\n"
    "v_accvgpr_write_b32 a211, v239\n"
    "v_accvgpr_read_b32 v239, a212\n"
    "v_mul_f32 v239, v238, v239\n"
    "v_accvgpr_write_b32 a212, v239\n"
    "v_accvgpr_read_b32 v239, a213\n"
    "v_mul_f32 v239, v238, v239\n"
    "v_accvgpr_write_b32 a213, v239\n"
    "v_accvgpr_read_b32 v239, a214\n"
    "v_mul_f32 v239, v238, v239\n"
    "v_accvgpr_write_b32 a214, v239\n"
    "v_accvgpr_read_b32 v239, a215\n"
    "v_mul_f32 v239, v238, v239\n"
    "v_accvgpr_write_b32 a215, v239\n"
    "v_accvgpr_read_b32 v239, a216\n"
    "v_mul_f32 v239, v238, v239\n"
    "v_accvgpr_write_b32 a216, v239\n"
    "v_accvgpr_read_b32 v239, a217\n"
    "v_mul_f32 v239, v238, v239\n"
    "v_accvgpr_write_b32 a217, v239\n"
    "v_accvgpr_read_b32 v239, a218\n"
    "v_mul_f32 v239, v238, v239\n"
    "v_accvgpr_write_b32 a218, v239\n"
    "v_accvgpr_read_b32 v239, a219\n"
    "v_mul_f32 v239, v238, v239\n"
    "v_accvgpr_write_b32 a219, v239\n"
    "v_accvgpr_read_b32 v239, a220\n"
    "v_mul_f32 v239, v238, v239\n"
    "v_accvgpr_write_b32 a220, v239\n"
    "v_accvgpr_read_b32 v239, a221\n"
    "v_mul_f32 v239, v238, v239\n"
    "v_accvgpr_write_b32 a221, v239\n"
    "v_accvgpr_read_b32 v239, a222\n"
    "v_mul_f32 v239, v238, v239\n"
    "v_accvgpr_write_b32 a222, v239\n"
    "v_accvgpr_read_b32 v239, a223\n"
    "v_mul_f32 v239, v238, v239\n"
    "v_accvgpr_write_b32 a223, v239\n"
    "v_accvgpr_read_b32 v239, a224\n"
    "v_mul_f32 v239, v238, v239\n"
    "v_accvgpr_write_b32 a224, v239\n"
    "v_accvgpr_read_b32 v239, a225\n"
    "v_mul_f32 v239, v238, v239\n"
    "v_accvgpr_write_b32 a225, v239\n"
    "v_accvgpr_read_b32 v239, a226\n"
    "v_mul_f32 v239, v238, v239\n"
    "v_accvgpr_write_b32 a226, v239\n"
    "v_accvgpr_read_b32 v239, a227\n"
    "v_mul_f32 v239, v238, v239\n"
    "v_accvgpr_write_b32 a227, v239\n"
    "v_accvgpr_read_b32 v239, a228\n"
    "v_mul_f32 v239, v238, v239\n"
    "v_accvgpr_write_b32 a228, v239\n"
    "v_accvgpr_read_b32 v239, a229\n"
    "v_mul_f32 v239, v238, v239\n"
    "v_accvgpr_write_b32 a229, v239\n"
    "v_accvgpr_read_b32 v239, a230\n"
    "v_mul_f32 v239, v238, v239\n"
    "v_accvgpr_write_b32 a230, v239\n"
    "v_accvgpr_read_b32 v239, a231\n"
    "v_mul_f32 v239, v238, v239\n"
    "v_accvgpr_write_b32 a231, v239\n"
    "v_accvgpr_read_b32 v239, a232\n"
    "v_mul_f32 v239, v238, v239\n"
    "v_accvgpr_write_b32 a232, v239\n"
    "v_accvgpr_read_b32 v239, a233\n"
    "v_mul_f32 v239, v238, v239\n"
    "v_accvgpr_write_b32 a233, v239\n"
    "v_accvgpr_read_b32 v239, a234\n"
    "v_mul_f32 v239, v238, v239\n"
    "v_accvgpr_write_b32 a234, v239\n"
    "v_accvgpr_read_b32 v239, a235\n"
    "v_mul_f32 v239, v238, v239\n"
    "v_accvgpr_write_b32 a235, v239\n"
    "v_accvgpr_read_b32 v239, a236\n"
    "v_mul_f32 v239, v238, v239\n"
    "v_accvgpr_write_b32 a236, v239\n"
    "v_accvgpr_read_b32 v239, a237\n"
    "v_mul_f32 v239, v238, v239\n"
    "v_accvgpr_write_b32 a237, v239\n"
    "v_accvgpr_read_b32 v239, a238\n"
    "v_mul_f32 v239, v238, v239\n"
    "v_accvgpr_write_b32 a238, v239\n"
    "v_accvgpr_read_b32 v239, a239\n"
    "v_mul_f32 v239, v238, v239\n"
    "v_accvgpr_write_b32 a239, v239\n"
    "v_fma_f32 v237, v184, s36, v220\n"
    "v_fma_f32 v239, v185, s36, v220\n"
    "v_exp_f32 v237, v237\n"
    "v_exp_f32 v239, v239\n"
    "s_nop 0\n"
    "v_cvt_pk_bf16_f32 v204, v237, v239\n"
    "v_fma_f32 v237, v186, s36, v220\n"
    "v_fma_f32 v239, v187, s36, v220\n"
    "v_exp_f32 v237, v237\n"
    "v_exp_f32 v239, v239\n"
    "s_nop 0\n"
    "v_cvt_pk_bf16_f32 v205, v237, v239\n"
    "v_fma_f32 v237, v188, s36, v220\n"
    "v_fma_f32 v239, v189, s36, v220\n"
    "v_exp_f32 v237, v237\n"
    "v_exp_f32 v239, v239\n"
    "s_nop 0\n"
    "v_cvt_pk_bf16_f32 v206, v237, v239\n"
    "v_fma_f32 v237, v190, s36, v220\n"
    "v_fma_f32 v239, v191, s36, v220\n"
    "v_exp_f32 v237, v237\n"
    "v_exp_f32 v239, v239\n"
    "s_nop 0\n"
    "v_cvt_pk_bf16_f32 v207, v237, v239\n"
    "s_nop 1\n"
    "v_mfma_f32_16x16x32_bf16 v[228:231], a[160:163], v[192:195], v[224:227]\n"
    "v_mfma_f32_16x16x32_bf16 v[228:231], a[164:167], v[196:199], v[228:231]\n"
    "v_mfma_f32_16x16x32_bf16 v[228:231], a[168:171], v[200:203], v[228:231]\n"
    "v_mfma_f32_16x16x32_bf16 v[228:231], a[172:175], v[204:207], v[228:231]\n"
    "v_fma_f32 v232, v136, s36, v212\n"
    "v_fma_f32 v233, v137, s36, v212\n"
    "v_fma_f32 v234, v138, s36, v212\n"
    "v_fma_f32 v235, v139, s36, v212\n"
    "v_exp_f32 v232, v232\n"
    "v_exp_f32 v233, v233\n"
    "v_exp_f32 v234, v234\n"
    "v_exp_f32 v235, v235\n"
    "s_nop 7\n"
    "s_nop 7\n"
    "s_add_u32 s86, s86, 4\n"
    "s_addc_u32 s87, s87, 0\n"
    "s_cmp_lg_u32 s34, 0\n"
    "s_setpc_b64 s[86:87]\n"
    "L_rare_1Pn_13_:\n"
    "s_cselect_b32 s34, 1, 0\n"
    "s_nop 7\n"
    "s_nop 7\n"
    "v_max3_f32 v236, v168, v169, v170\n"
    "v_max3_f32 v236, v236, v171, v172\n"
    "v_max3_f32 v236, v236, v173, v174\n"
    "v_max_f32 v236, v236, v175\n"
    "v_mov_b32 v237, v236\n"
    "s_nop 1\n"
    "v_permlane16_swap_b32 v236, v237\n"
    "v_max_f32 v236, v236, v237\n"
    "v_mov_b32 v237, v236\n"
    "s_nop 1\n"
    "v_permlane32_swap_b32 v236, v237\n"
    "v_max_f32 v236, v236, v237\n"
    "v_fma_f32 v236, v236, s36, v212\n"
    "v_log_f32 v237, v225\n"
    "s_nop 0\n"
    "v_add_f32 v237, 0xc1300000, v237\n"
    "v_max3_f32 v236, v236, v237, 0\n"
    "v_exp_f32 v238, -v236\n"
    "s_nop 0\n"
    "v_mul_f32 v225, v238, v225\n"
    "v_sub_f32 v212, v212, v236\n"
    "v_sub_f32 v213, v213, v236\n"
    "v_sub_f32 v214, v214, v236\n"
    "v_sub_f32 v215, v215, v236\n"
    "v_mul_f32 v32, v238, v32\n"
    "v_mul_f32 v33, v238, v33\n"
    "v_mul_f32 v34, v238, v34\n"
    "v_mul_f32 v35, v238, v35\n"
    "v_mul_f32 v36, v238, v36\n"
    "v_mul_f32 v37, v238, v37\n"
    "v_mul_f32 v38, v238, v38\n"
    "v_mul_f32 v39, v238, v39\n"
    "v_mul_f32 v40, v238, v40\n"
    "v_mul_f32 v41, v238, v41\n"
    "v_mul_f32 v42, v238, v42\n"
    "v_mul_f32 v43, v238, v43\n"
    "v_mul_f32 v44, v238, v44\n"
    "v_mul_f32 v45, v238, v45\n"
    "v_mul_f32 v46, v238, v46\n"
    "v_mul_f32 v47, v238, v47\n"
    "v_mul_f32 v48, v238, v48\n"
    "v_mul_f32 v49, v238, v49\n"
    "v_mul_f32 v50, v238, v50\n"
    "v_mul_f32 v51, v238, v51\n"
    "v_mul_f32 v52, v238, v52\n"
    "v_mul_f32 v53, v238, v53\n"
    "v_mul_f32 v54, v238, v54\n"
    "v_mul_f32 v55, v238, v55\n"
    "v_mul_f32 v56, v238, v56\n"
    "v_mul_f32 v57, v238, v57\n"
    "v_mul_f32 v58, v238, v58\n"
    "v_mul_f32 v59, v238, v59\n"
    "v_mul_f32 v60, v238, v60\n"
    "v_mul_f32 v61, v238, v61\n"
    "v_mul_f32 v62, v238, v62\n"
    "v_mul_f32 v63, v238, v63\n"
    "v_fma_f32 v237, v168, s36, v212\n"
    "v_fma_f32 v239, v169, s36, v212\n"
    "v_exp_f32 v237, v237\n"
    "v_exp_f32 v239, v239\n"
    "s_nop 0\n"
    "v_cvt_pk_bf16_f32 v196, v237, v239\n"
    "v_fma_f32 v237, v170, s36, v212\n"
    "v_fma_f32 v239, v171, s36, v212\n"
    "v_exp_f32 v237, v237\n"
    "v_exp_f32 v239, v239\n"
    "s_nop 0\n"
    "v_cvt_pk_bf16_f32 v197, v237, v239\n"
    "v_fma_f32 v237, v172, s36, v212\n"
    "v_fma_f32 v239, v173, s36, v212\n"
    "v_exp_f32 v237, v237\n"
    "v_exp_f32 v239, v239\n"
    "s_nop 0\n"
    "v_cvt_pk_bf16_f32 v198, v237, v239\n"
    "v_fma_f32 v237, v174, s36, v212\n"
    "v_fma_f32 v239, v175, s36, v212\n"
    "v_exp_f32 v237, v237\n"
    "v_exp_f32 v239, v239\n"
    "s_nop 0\n"
    "v_cvt_pk_bf16_f32 v199, v237, v239\n"
    "v_max3_f32 v236, v184, v185, v186\n"
    "v_max3_f32 v236, v236, v187, v188\n"
    "v_max3_f32 v236, v236, v189, v190\n"
    "v_max_f32 v236, v236, v191\n"
    "v_mov_b32 v237, v236\n"
    "s_nop 1\n"
    "v_permlane16_swap_b32 v236, v237\n"
    "v_max_f32 v236, v236, v237\n"
    "v_mov_b32 v237, v236\n"
    "s_nop 1\n"
    "v_permlane32_swap_b32 v236, v237\n"
    "v_max_f32 v236, v236, v237\n"
    "v_fma_f32 v236, v236, s36, v220\n"
    "v_log_f32 v237, v227\n"
    "s_nop 0\n"
    "v_add_f32 v237, 0xc1300000, v237\n"
    "v_max3_f32 v236, v236, v237, 0\n"
    "v_exp_f32 v238, -v236\n"
    "s_nop 0\n"
    "v_mul_f32 v227, v238, v227\n"
    "v_sub_f32 v220, v220, v236\n"
    "v_sub_f32 v221, v221, v236\n"
    "v_sub_f32 v222, v222, v236\n"
    "v_sub_f32 v223, v223, v236\n"
    "v_accvgpr_read_b32 v239, a208\n"
    "v_mul_f32 v239, v238, v239\n"
    "v_accvgpr_write_b32 a208, v239\n"
    "v_accvgpr_read_b32 v239, a209\n"
    "v_mul_f32 v239, v238, v239\n"
    "v_accvgpr_write_b32 a209, v239\n"
    "v_accvgpr_read_b32 v239, a210\n"
    "v_mul_f32 v239, v238, v239\n"
    "v_accvgpr_write_b32 a210, v239\n"
    "v_accvgpr_read_b32 v239, a211\n"
    "v_mul_f32 v239, v238, v239\n"
    "v_accvgpr_write_b32 a211, v239\n"
    "v_accvgpr_read_b32 v239, a212\n"
    "v_mul_f32 v239, v238, v239\n"
    "v_accvgpr_write_b32 a212, v239\n"
    "v_accvgpr_read_b32 v239, a213\n"
    "v_mul_f32 v239, v238, v239\n"
    "v_accvgpr_write_b32 a213, v239\n"
    "v_accvgpr_read_b32 v239, a214\n"
    "v_mul_f32 v239, v238, v239\n"
    "v_accvgpr_write_b32 a214, v239\n"
    "v_accvgpr_read_b32 v239, a215\n"
    "v_mul_f32 v239, v238, v239\n"
    "v_accvgpr_write_b32 a215, v239\n"
    "v_accvgpr_read_b32 v239, a216\n"
    "v_mul_f32 v239, v238, v239\n"
    "v_accvgpr_write_b32 a216, v239\n"
    "v_accvgpr_read_b32 v239, a217\n"
    "v_mul_f32 v239, v238, v239\n"
    "v_accvgpr_write_b32 a217, v239\n"
    "v_accvgpr_read_b32 v239, a218\n"
    "v_mul_f32 v239, v238, v239\n"
    "v_accvgpr_write_b32 a218, v239\n"
    "v_accvgpr_read_b32 v239, a219\n"
    "v_mul_f32 v239, v238, v239\n"
    "v_accvgpr_write_b32 a219, v239\n"
    "v_accvgpr_read_b32 v239, a220\n"
    "v_mul_f32 v239, v238, v239\n"
    "v_accvgpr_write_b32 a220, v239\n"
    "v_accvgpr_read_b32 v239, a221\n"
    "v_mul_f32 v239, v238, v239\n"
    "v_accvgpr_write_b32 a221, v239\n"
    "v_accvgpr_read_b32 v239, a222\n"
    "v_mul_f32 v239, v238, v239\n"
    "v_accvgpr_write_b32 a222, v239\n"
    "v_accvgpr_read_b32 v239, a223\n"
    "v_mul_f32 v239, v238, v239\n"
    "v_accvgpr_write_b32 a223, v239\n"
    "v_accvgpr_read_b32 v239, a224\n"
    "v_mul_f32 v239, v238, v239\n"
    "v_accvgpr_write_b32 a224, v239\n"
    "v_accvgpr_read_b32 v239, a225\n"
    "v_mul_f32 v239, v238, v239\n"
    "v_accvgpr_write_b32 a225, v239\n"
    "v_accvgpr_read_b32 v239, a226\n"
    "v_mul_f32 v239, v238, v239\n"
    "v_accvgpr_write_b32 a226, v239\n"
    "v_accvgpr_read_b32 v239, a227\n"
    "v_mul_f32 v239, v238, v239\n"
    "v_accvgpr_write_b32 a227, v239\n"
    "v_accvgpr_read_b32 v239, a228\n"
    "v_mul_f32 v239, v238, v239\n"
    "v_accvgpr_write_b32 a228, v239\n"
    "v_accvgpr_read_b32 v239, a229\n"
    "v_mul_f32 v239, v238, v239\n"
    "v_accvgpr_write_b32 a229, v239\n"
    "v_accvgpr_read_b32 v239, a230\n"
    "v_mul_f32 v239, v238, v239\n"
    "v_accvgpr_write_b32 a230, v239\n"
    "v_accvgpr_read_b32 v239, a231\n"
    "v_mul_f32 v239, v238, v239\n"
    "v_accvgpr_write_b32 a231, v239\n"
    "v_accvgpr_read_b32 v239, a232\n"
    "v_mul_f32 v239, v238, v239\n"
    "v_accvgpr_write_b32 a232, v239\n"
    "v_accvgpr_read_b32 v239, a233\n"
    "v_mul_f32 v239, v238, v239\n"
    "v_accvgpr_write_b32 a233, v239\n"
    "v_accvgpr_read_b32 v239, a234\n"
    "v_mul_f32 v239, v238, v239\n"
    "v_accvgpr_write_b32 a234, v239\n"
    "v_accvgpr_read_b32 v239, a235\n"
    "v_mul_f32 v239, v238, v239\n"
    "v_accvgpr_write_b32 a235, v239\n"
    "v_accvgpr_read_b32 v239, a236\n"
    "v_mul_f32 v239, v238, v239\n"
    "v_accvgpr_write_b32 a236, v239\n"
    "v_accvgpr_read_b32 v239, a237\n"
    "v_mul_f32 v239, v238, v239\n"
    "v_accvgpr_write_b32 a237, v239\n"
    "v_accvgpr_read_b32 v239, a238\n"
    "v_mul_f32 v239, v238, v239\n"
    "v_accvgpr_write_b32 a238, v239\n"
    "v_accvgpr_read_b32 v239, a239\n"
    "v_mul_f32 v239, v238, v239\n"
    "v_accvgpr_write_b32 a239, v239\n"
    "v_fma_f32 v237, v184, s36, v220\n"
    "v_fma_f32 v239, v185, s36, v220\n"
    "v_exp_f32 v237, v237\n"
    "v_exp_f32 v239, v239\n"
    "s_nop 0\n"
    "v_cvt_pk_bf16_f32 v204, v237, v239\n"
    "v_fma_f32 v237, v186, s36, v220\n"
    "v_fma_f32 v239, v187, s36, v220\n"
    "v_exp_f32 v237, v237\n"
    "v_exp_f32 v239, v239\n"
    "s_nop 0\n"
    "v_cvt_pk_bf16_f32 v205, v237, v239\n"
    "v_fma_f32 v237, v188, s36, v220\n"
    "v_fma_f32 v239, v189, s36, v220\n"
    "v_exp_f32 v237, v237\n"
    "v_exp_f32 v239, v239\n"
    "s_nop 0\n"
    "v_cvt_pk_bf16_f32 v206, v237, v239\n"
    "v_fma_f32 v237, v190, s36, v220\n"
    "v_fma_f32 v239, v191, s36, v220\n"
    "v_exp_f32 v237, v237\n"
    "v_exp_f32 v239, v239\n"
    "s_nop 0\n"
    "v_cvt_pk_bf16_f32 v207, v237, v239\n"
    "s_nop 1\n"
    "v_mfma_f32_16x16x32_bf16 v[228:231], a[164:167], v[196:199], v[224:227]\n"
    "v_mfma_f32_16x16x32_bf16 v[228:231], a[172:175], v[204:207], v[228:231]\n"
    "s_nop 7\n"
    "s_nop 7\n"
    "s_add_u32 s86, s86, 4\n"
    "s_addc_u32 s87, s87, 0\n"
    "s_cmp_lg_u32 s34, 0\n"
    "s_setpc_b64 s[86:87]\n"
    "L_rare_1SP_0123_:\n"
    "s_cselect_b32 s34, 1, 0\n"
    "s_nop 7\n"
    "s_nop 7\n"
    "v_max3_f32 v236, v160, v161, v162\n"
    "v_max3_f32 v236, v236, v163, v164\n"
    "v_max3_f32 v236, v236, v165, v166\n"
    "v_max_f32 v236, v236, v167\n"
    "v_mov_b32 v237, v236\n"
    "s_nop 1\n"
    "v_permlane16_swap_b32 v236, v237\n"
    "v_max_f32 v236, v236, v237\n"
    "v_mov_b32 v237, v236\n"
    "s_nop 1\n"
    "v_permlane32_swap_b32 v236, v237\n"
    "v_max_f32 v236, v236, v237\n"
    "v_fma_f32 v236, v236, s36, v208\n"
    "v_log_f32 v237, v224\n"
    "s_nop 0\n"
    "v_add_f32 v237, 0xc1300000, v237\n"
    "v_max3_f32 v236, v236, v237, 0\n"
    "v_exp_f32 v238, -v236\n"
    "s_nop 0\n"
    "v_mul_f32 v224, v238, v224\n"
    "v_sub_f32 v208, v208, v236\n"
    "v_sub_f32 v209, v209, v236\n"
    "v_sub_f32 v210, v210, v236\n"
    "v_sub_f32 v211, v211, v236\n"
    "v_mul_f32 v0, v238, v0\n"
    "v_mul_f32 v1, v238, v1\n"
    "v_mul_f32 v2, v238, v2\n"
    "v_mul_f32 v3, v238, v3\n"
    "v_mul_f32 v4, v238, v4\n"
    "v_mul_f32 v5, v238, v5\n"
    "v_mul_f32 v6, v238, v6\n"
    "v_mul_f32 v7, v238, v7\n"
    "v_mul_f32 v8, v238, v8\n"
    "v_mul_f32 v9, v238, v9\n"
    "v_mul_f32 v10, v238, v10\n"
    "v_mul_f32 v11, v238, v11\n"
    "v_mul_f32 v12, v238, v12\n"
    "v_mul_f32 v13, v238, v13\n"
    "v_mul_f32 v14, v238, v14\n"
    "v_mul_f32 v15, v238, v15\n"
    "v_mul_f32 v16, v238, v16\n"
    "v_mul_f32 v17, v238, v17\n"
    "v_mul_f32 v18, v238, v18\n"
    "v_mul_f32 v19, v238, v19\n"
    "v_mul_f32 v20, v238, v20\n"
    "v_mul_f32 v21, v238, v21\n"
    "v_mul_f32 v22, v238, v22\n"
    "v_mul_f32 v23, v238, v23\n"
    "v_mul_f32 v24, v238, v24\n"
    "v_mul_f32 v25, v238, v25\n"
    "v_mul_f32 v26, v238, v26\n"
    "v_mul_f32 v27, v238, v27\n"
    "v_mul_f32 v28, v238, v28\n"
    "v_mul_f32 v29, v238, v29\n"
    "v_mul_f32 v30, v238, v30\n"
    "v_mul_f32 v31, v238, v31\n"
    "v_fma_f32 v237, v160, s36, v208\n"
    "v_fma_f32 v239, v161, s36, v208\n"
    "v_exp_f32 v237, v237\n"
    "v_exp_f32 v239, v239\n"
    "s_nop 0\n"
    "v_cvt_pk_bf16_f32 v192, v237, v239\n"
    "v_fma_f32 v237, v162, s36, v208\n"
    "v_fma_f32 v239, v163, s36, v208\n"
    "v_exp_f32 v237, v237\n"
    "v_exp_f32 v239, v239\n"
    "s_nop 0\n"
    "v_cvt_pk_bf16_f32 v193, v237, v239\n"
    "v_fma_f32 v237, v164, s36, v208\n"
    "v_fma_f32 v239, v165, s36, v208\n"
    "v_exp_f32 v237, v237\n"
    "v_exp_f32 v239, v239\n"
    "s_nop 0\n"
    "v_cvt_pk_bf16_f32 v194, v237, v239\n"
    "v_fma_f32 v237, v166, s36, v208\n"
    "v_fma_f32 v239, v167, s36, v208\n"
    "v_exp_f32 v237, v237\n"
    "v_exp_f32 v239, v239\n"
    "s_nop 0\n"
    "v_cvt_pk_bf16_f32 v195, v237, v239\n"
    "v_max3_f32 v236, v168, v169, v170\n"
    "v_max3_f32 v236, v236, v171, v172\n"
    "v_max3_f32 v236, v236, v173, v174\n"
    "v_max_f32 v236, v236, v175\n"
    "v_mov_b32 v237, v236\n"
    "s_nop 1\n"
    "v_permlane16_swap_b32 v236, v237\n"
    "v_max_f32 v236, v236, v237\n"
    "v_mov_b32 v237, v236\n"
    "s_nop 1\n"
    "v_permlane32_swap_b32 v236, v237\n"
    "v_max_f32 v236, v236, v237\n"
    "v_fma_f32 v236, v236, s36, v212\n"
    "v_log_f32 v237, v225\n"
    "s_nop 0\n"
    "v_add_f32 v237, 0xc1300000, v237\n"
    "v_max3_f32 v236, v236, v237, 0\n"
    "v_exp_f32 v238, -v236\n"
    "s_nop 0\n"
    "v_mul_f32 v225, v238, v225\n"
    "v_sub_f32 v212, v212, v236\n"
    "v_sub_f32 v213, v213, v236\n"
    "v_sub_f32 v214, v214, v236\n"
    "v_sub_f32 v215, v215, v236\n"
    "v_mul_f32 v32, v238, v32\n"
    "v_mul_f32 v33, v238, v33\n"
    "v_mul_f32 v34, v238, v34\n"
    "v_mul_f32 v35, v238, v35\n"
    "v_mul_f32 v36, v238, v36\n"
    "v_mul_f32 v37, v238, v37\n"
    "v_mul_f32 v38, v238, v38\n"
    "v_mul_f32 v39, v238, v39\n"
    "v_mul_f32 v40, v238, v40\n"
    "v_mul_f32 v41, v238, v41\n"
    "v_mul_f32 v42, v238, v42\n"
    "v_mul_f32 v43, v238, v43\n"
    "v_mul_f32 v44, v238, v44\n"
    "v_mul_f32 v45, v238, v45\n"
    "v_mul_f32 v46, v238, v46\n"
    "v_mul_f32 v47, v238, v47\n"
    "v_mul_f32 v48, v238, v48\n"
    "v_mul_f32 v49, v238, v49\n"
    "v_mul_f32 v50, v238, v50\n"
    "v_mul_f32 v51, v238, v51\n"
    "v_mul_f32 v52, v238, v52\n"
    "v_mul_f32 v53, v238, v53\n"
    "v_mul_f32 v54, v238, v54\n"
    "v_mul_f32 v55, v238, v55\n"
    "v_mul_f32 v56, v238, v56\n"
    "v_mul_f32 v57, v238, v57\n"
    "v_mul_f32 v58, v238, v58\n"
    "v_mul_f32 v59, v238, v59\n"
    "v_mul_f32 v60, v238, v60\n"
    "v_mul_f32 v61, v238, v61\n"
    "v_mul_f32 v62, v238, v62\n"
    "v_mul_f32 v63, v238, v63\n"
    "v_fma_f32 v237, v168, s36, v212\n"
    "v_fma_f32 v239, v169, s36, v212\n"
    "v_exp_f32 v237, v237\n"
    "v_exp_f32 v239, v239\n"
    "s_nop 0\n"
    "v_cvt_pk_bf16_f32 v196, v237, v239\n"
    "v_fma_f32 v237, v170, s36, v212\n"
    "v_fma_f32 v239, v171, s36, v212\n"
    "v_exp_f32 v237, v237\n"
    "v_exp_f32 v239, v239\n"
    "s_nop 0\n"
    "v_cvt_pk_bf16_f32 v197, v237, v239\n"
    "v_fma_f32 v237, v172, s36, v212\n"
    "v_fma_f32 v239, v173, s36, v212\n"
    "v_exp_f32 v237, v237\n"
    "v_exp_f32 v239, v239\n"
    "s_nop 0\n"
    "v_cvt_pk_bf16_f32 v198, v237, v239\n"
    "v_fma_f32 v237, v174, s36, v212\n"
    "v_fma_f32 v239, v175, s36, v212\n"
    "v_exp_f32 v237, v237\n"
    "v_exp_f32 v239, v239\n"
    "s_nop 0\n"
    "v_cvt_pk_bf16_f32 v199, v237, v239\n"
    "v_max3_f32 v236, v176, v177, v178\n"
    "v_max3_f32 v236, v236, v179, v180\n"
    "v_max3_f32 v236, v236, v181, v182\n"
    "v_max_f32 v236, v236, v183\n"
    "v_mov_b32 v237, v236\n"
    "s_nop 1\n"
    "v_permlane16_swap_b32 v236, v237\n"
    "v_max_f32 v236, v236, v237\n"
    "v_mov_b32 v237, v236\n"
    "s_nop 1\n"
    "v_permlane32_swap_b32 v236, v237\n"
    "v_max_f32 v236, v236, v237\n"
    "v_fma_f32 v236, v236, s36, v244\n"
    "v_log_f32 v237, v226\n"
    "s_nop 0\n"
    "v_add_f32 v237, 0xc1300000, v237\n"
    "v_max3_f32 v236, v236, v237, 0\n"
    "v_exp_f32 v238, -v236\n"
    "s_nop 0\n"
    "v_mul_f32 v226, v238, v226\n"
    "v_sub_f32 v244, v244, v236\n"
    "v_mul_f32 v64, v238, v64\n"
    "v_mul_f32 v65, v238, v65\n"
    "v_mul_f32 v66, v238, v66\n"
    "v_mul_f32 v67, v238, v67\n"
    "v_mul_f32 v68, v238, v68\n"
    "v_mul_f32 v69, v238, v69\n"
    "v_mul_f32 v70, v238, v70\n"
    "v_mul_f32 v71, v238, v71\n"
    "v_mul_f32 v72, v238, v72\n"
    "v_mul_f32 v73, v238, v73\n"
    "v_mul_f32 v74, v238, v74\n"
    "v_mul_f32 v75, v238, v75\n"
    "v_mul_f32 v76, v238, v76\n"
    "v_mul_f32 v77, v238, v77\n"
    "v_mul_f32 v78, v238, v78\n"
    "v_mul_f32 v79, v238, v79\n"
    "v_mul_f32 v80, v238, v80\n"
    "v_mul_f32 v81, v238, v81\n"
    "v_mul_f32 v82, v238, v82\n"
    "v_mul_f32 v83, v238, v83\n"
    "v_mul_f32 v84, v238, v84\n"
    "v_mul_f32 v85, v238, v85\n"
    "v_mul_f32 v86, v238, v86\n"
    "v_mul_f32 v87, v238, v87\n"
    "v_mul_f32 v88, v238, v88\n"
    "v_mul_f32 v89, v238, v89\n"
    "v_mul_f32 v90, v238, v90\n"
    "v_mul_f32 v91, v238, v91\n"
    "v_mul_f32 v92, v238, v92\n"
    "v_mul_f32 v93, v238, v93\n"
    "v_mul_f32 v94, v238, v94\n"
    "v_mul_f32 v95, v238, v95\n"
    "v_fma_f32 v237, v176, s36, v244\n"
    "v_fma_f32 v239, v177, s36, v244\n"
    "v_exp_f32 v237, v237\n"
    "v_exp_f32 v239, v239\n"
    "s_nop 0\n"
    "v_cvt_pk_bf16_f32 v200, v237, v239\n"
    "v_fma_f32 v237, v178, s36, v244\n"
    "v_fma_f32 v239, v179, s36, v244\n"
    "v_exp_f32 v237, v237\n"
    "v_exp_f32 v239, v239\n"
    "s_nop 0\n"
    "v_cvt_pk_bf16_f32 v201, v237, v239\n"
    "v_fma_f32 v237, v180, s36, v244\n"
    "v_fma_f32 v239, v181, s36, v244\n"
    "v_exp_f32 v237, v237\n"
    "v_exp_f32 v239, v239\n"
    "s_nop 0\n"
    "v_cvt_pk_bf16_f32 v202, v237, v239\n"
    "v_fma_f32 v237, v182, s36, v244\n"
    "v_fma_f32 v239, v183, s36, v244\n"
    "v_exp_f32 v237, v237\n"
    "v_exp_f32 v239, v239\n"
    "s_nop 0\n"
    "v_cvt_pk_bf16_f32 v203, v237, v239\n"
    "v_max3_f32 v236, v184, v185, v186\n"
    "v_max3_f32 v236, v236, v187, v188\n"
    "v_max3_f32 v236, v236, v189, v190\n"
    "v_max_f32 v236, v236, v191\n"
    "v_mov_b32 v237, v236\n"
    "s_nop 1\n"
    "v_permlane16_swap_b32 v236, v237\n"
    "v_max_f32 v236, v236, v237\n"
    "v_mov_b32 v237, v236\n"
    "s_nop 1\n"
    "v_permlane32_swap_b32 v236, v237\n"
    "v_max_f32 v236, v236, v237\n"
    "v_fma_f32 v236, v236, s36, v245\n"
    "v_log_f32 v237, v227\n"
    "s_nop 0\n"
    "v_add_f32 v237, 0xc1300000, v237\n"
    "v_max3_f32 v236, v236, v237, 0\n"
    "v_exp_f32 v238, -v236\n"
    "s_nop 0\n"
    "v_mul_f32 v227, v238, v227\n"
    "v_sub_f32 v245, v245, v236\n"
    "v_mul_f32 v96, v238, v96\n"
    "v_mul_f32 v97, v238, v97\n"
    "v_mul_f32 v98, v238, v98\n"
    "v_mul_f32 v99, v238, v99\n"
    "v_mul_f32 v100, v238, v100\n"
    "v_mul_f32 v101, v238, v101\n"
    "v_mul_f32 v102, v238, v102\n"
    "v_mul_f32 v103, v238, v103\n"
    "v_mul_f32 v104, v238, v104\n"
    "v_mul_f32 v105, v238, v105\n"
    "v_mul_f32 v106, v238, v106\n"
    "v_mul_f32 v107, v238, v107\n"
    "v_mul_f32 v108, v238, v108\n"
    "v_mul_f32 v109, v238, v109\n"
    "v_mul_f32 v110, v238, v110\n"
    "v_mul_f32 v111, v238, v111\n"
    "v_mul_f32 v112, v238, v112\n"
    "v_mul_f32 v113, v238, v113\n"
    "v_mul_f32 v114, v238, v114\n"
    "v_mul_f32 v115, v238, v115\n"
    "v_mul_f32 v116, v238, v116\n"
    "v_mul_f32 v117, v238, v117\n"
    "v_mul_f32 v118, v238, v118\n"
    "v_mul_f32 v119, v238, v119\n"
    "v_mul_f32 v120, v238, v120\n"
    "v_mul_f32 v121, v238, v121\n"
    "v_mul_f32 v122, v238, v122\n"
    "v_mul_f32 v123, v238, v123\n"
    "v_mul_f32 v124, v238, v124\n"
    "v_mul_f32 v125, v238, v125\n"
    "v_mul_f32 v126, v238, v126\n"
    "v_mul_f32 v127, v238, v127\n"
    "v_fma_f32 v237, v184, s36, v245\n"
    "v_fma_f32 v239, v185, s36, v245\n"
    "v_exp_f32 v237, v237\n"
    "v_exp_f32 v239, v239\n"
    "s_nop 0\n"
    "v_cvt_pk_bf16_f32 v204, v237, v239\n"
    "v_fma_f32 v237, v186, s36, v245\n"
    "v_fma_f32 v239, v187, s36, v245\n"
    "v_exp_f32 v237, v237\n"
    "v_exp_f32 v239, v239\n"
    "s_nop 0\n"
    "v_cvt_pk_bf16_f32 v205, v237, v239\n"
    "v_fma_f32 v237, v188, s36, v245\n"
    "v_fma_f32 v239, v189, s36, v245\n"
    "v_exp_f32 v237, v237\n"
    "v_exp_f32 v239, v239\n"
    "s_nop 0\n"
    "v_cvt_pk_bf16_f32 v206, v237, v239\n"
    "v_fma_f32 v237, v190, s36, v245\n"
    "v_fma_f32 v239, v191, s36, v245\n"
    "v_exp_f32 v237, v237\n"
    "v_exp_f32 v239, v239\n"
    "s_nop 0\n"
    "v_cvt_pk_bf16_f32 v207, v237, v239\n"
    "s_nop 1\n"
    "v_mfma_f32_16x16x32_bf16 v[228:231], a[160:163], v[192:195], v[224:227]\n"
    "v_mfma_f32_16x16x32_bf16 v[228:231], a[164:167], v[196:199], v[228:231]\n"
    "v_mfma_f32_16x16x32_bf16 v[228:231], a[168:171], v[200:203], v[228:231]\n"
    "v_mfma_f32_16x16x32_bf16 v[228:231], a[172:175], v[204:207], v[228:231]\n"
    "v_fma_f32 v232, v136, s36, v212\n"
    "v_fma_f32 v233, v137, s36, v212\n"
    "v_fma_f32 v234, v138, s36, v212\n"
    "v_fma_f32 v235, v139, s36, v212\n"
    "v_exp_f32 v232, v232\n"
    "v_exp_f32 v233, v233\n"
    "v_exp_f32 v234, v234\n"
    "v_exp_f32 v235, v235\n"
    "s_nop 7\n"
    "s_nop 7\n"
    "s_add_u32 s86, s86, 4\n"
    "s_addc_u32 s87, s87, 0\n"
    "s_cmp_lg_u32 s34, 0\n"
    "s_setpc_b64 s[86:87]\n"
    "L_rare_1SP_013_:\n"
    "s_cselect_b32 s34, 1, 0\n"
    "s_nop 7\n"
    "s_nop 7\n"
    "v_max3_f32 v236, v160, v161, v162\n"
    "v_max3_f32 v236, v236, v163, v164\n"
    "v_max3_f32 v236, v236, v165, v166\n"
    "v_max_f32 v236, v236, v167\n"
    "v_mov_b32 v237, v236\n"
    "s_nop 1\n"
    "v_permlane16_swap_b32 v236, v237\n"
    "v_max_f32 v236, v236, v237\n"
    "v_mov_b32 v237, v236\n"
    "s_nop 1\n"
    "v_permlane32_swap_b32 v236, v237\n"
    "v_max_f32 v236, v236, v237\n"
    "v_fma_f32 v236, v236, s36, v208\n"
    "v_log_f32 v237, v224\n"
    "s_nop 0\n"
    "v_add_f32 v237, 0xc1300000, v237\n"
    "v_max3_f32 v236, v236, v237, 0\n"
    "v_exp_f32 v238, -v236\n"
    "s_nop 0\n"
    "v_mul_f32 v224, v238, v224\n"
    "v_sub_f32 v208, v208, v236\n"
    "v_sub_f32 v209, v209, v236\n"
    "v_sub_f32 v210, v210, v236\n"
    "v_sub_f32 v211, v211, v236\n"
    "v_mul_f32 v0, v238, v0\n"
    "v_mul_f32 v1, v238, v1\n"
    "v_mul_f32 v2, v238, v2\n"
    "v_mul_f32 v3, v238, v3\n"
    "v_mul_f32 v4, v238, v4\n"
    "v_mul_f32 v5, v238, v5\n"
    "v_mul_f32 v6, v238, v6\n"
    "v_mul_f32 v7, v238, v7\n"
    "v_mul_f32 v8, v238, v8\n"
    "v_mul_f32 v9, v238, v9\n"
    "v_mul_f32 v10, v238, v10\n"
    "v_mul_f32 v11, v238, v11\n"
    "v_mul_f32 v12, v238, v12\n"
    "v_mul_f32 v13, v238, v13\n"
    "v_mul_f32 v14, v238, v14\n"
    "v_mul_f32 v15, v238, v15\n"
    "v_mul_f32 v16, v238, v16\n"
    "v_mul_f32 v17, v238, v17\n"
    "v_mul_f32 v18, v238, v18\n"
    "v_mul_f32 v19, v238, v19\n"
    "v_mul_f32 v20, v238, v20\n"
    "v_mul_f32 v21, v238, v21\n"
    "v_mul_f32 v22, v238, v22\n"
    "v_mul_f32 v23, v238, v23\n"
    "v_mul_f32 v24, v238, v24\n"
    "v_mul_f32 v25, v238, v25\n"
    "v_mul_f32 v26, v238, v26\n"
    "v_mul_f32 v27, v238, v27\n"
    "v_mul_f32 v28, v238, v28\n"
    "v_mul_f32 v29, v238, v29\n"
    "v_mul_f32 v30, v238, v30\n"
    "v_mul_f32 v31, v238, v31\n"
    "v_fma_f32 v237, v160, s36, v208\n"
    "v_fma_f32 v239, v161, s36, v208\n"
    "v_exp_f32 v237, v237\n"
    "v_exp_f32 v239, v239\n"
    "s_nop 0\n"
    "v_cvt_pk_bf16_f32 v192, v237, v239\n"
    "v_fma_f32 v237, v162, s36, v208\n"
    "v_fma_f32 v239, v163, s36, v208\n"
    "v_exp_f32 v237, v237\n"
    "v_exp_f32 v239, v239\n"
    "s_nop 0\n"
    "v_cvt_pk_bf16_f32 v193, v237, v239\n"
    "v_fma_f32 v237, v164, s36, v208\n"
    "v_fma_f32 v239, v165, s36, v208\n"
    "v_exp_f32 v237, v237\n"
    "v_exp_f32 v239, v239\n"
    "s_nop 0\n"
    "v_cvt_pk_bf16_f32 v194, v237, v239\n"
    "v_fma_f32 v237, v166, s36, v208\n"
    "v_fma_f32 v239, v167, s36, v208\n"
    "v_exp_f32 v237, v237\n"
    "v_exp_f32 v239, v239\n"
    "s_nop 0\n"
    "v_cvt_pk_bf16_f32 v195, v237, v239\n"
    "v_max3_f32 v236, v168, v169, v170\n"
    "v_max3_f32 v236, v236, v171, v172\n"
    "v_max3_f32 v236, v236, v173, v174\n"
    "v_max_f32 v236, v236, v175\n"
    "v_mov_b32 v237, v236\n"
    "s_nop 1\n"
    "v_permlane16_swap_b32 v236, v237\n"
    "v_max_f32 v236, v236, v237\n"
    "v_mov_b32 v237, v236\n"
    "s_nop 1\n"
    "v_permlane32_swap_b32 v236, v237\n"
    "v_max_f32 v236, v236, v237\n"
    "v_fma_f32 v236, v236, s36, v212\n"
    "v_log_f32 v237, v225\n"
    "s_nop 0\n"
    "v_add_f32 v237, 0xc1300000, v237\n"
    "v_max3_f32 v236, v236, v237, 0\n"
    "v_exp_f32 v238, -v236\n"
    "s_nop 0\n"
    "v_mul_f32 v225, v238, v225\n"
    "v_sub_f32 v212, v212, v236\n"
    "v_sub_f32 v213, v213, v236\n"
    "v_sub_f32 v214, v214, v236\n"
    "v_sub_f32 v215, v215, v236\n"
    "v_mul_f32 v32, v238, v32\n"
    "v_mul_f32 v33, v238, v33\n"
    "v_mul_f32 v34, v238, v34\n"
    "v_mul_f32 v35, v238, v35\n"
    "v_mul_f32 v36, v238, v36\n"
    "v_mul_f32 v37, v238, v37\n"
    "v_mul_f32 v38, v238, v38\n"
    "v_mul_f32 v39, v238, v39\n"
    "v_mul_f32 v40, v238, v40\n"
    "v_mul_f32 v41, v238, v41\n"
    "v_mul_f32 v42, v238, v42\n"
    "v_mul_f32 v43, v238, v43\n"
    "v_mul_f32 v44, v238, v44\n"
    "v_mul_f32 v45, v238, v45\n"
    "v_mul_f32 v46, v238, v46\n"
    "v_mul_f32 v47, v238, v47\n"
    "v_mul_f32 v48, v238, v48\n"
    "v_mul_f32 v49, v238, v49\n"
    "v_mul_f32 v50, v238, v50\n"
    "v_mul_f32 v51, v238, v51\n"
    "v_mul_f32 v52, v238, v52\n"
    "v_mul_f32 v53, v238, v53\n"
    "v_mul_f32 v54, v238, v54\n"
    "v_mul_f32 v55, v238, v55\n"
    "v_mul_f32 v56, v238, v56\n"
    "v_mul_f32 v57, v238, v57\n"
    "v_mul_f32 v58, v238, v58\n"
    "v_mul_f32 v59, v238, v59\n"
    "v_mul_f32 v60, v238, v60\n"
    "v_mul_f32 v61, v238, v61\n"
    "v_mul_f32 v62, v238, v62\n"
    "v_mul_f32 v63, v238, v63\n"
    "v_fma_f32 v237, v168, s36, v212\n"
    "v_fma_f32 v239, v169, s36, v212\n"
    "v_exp_f32 v237, v237\n"
    "v_exp_f32 v239, v239\n"
    "s_nop 0\n"
    "v_cvt_pk_bf16_f32 v196, v237, v239\n"
    "v_fma_f32 v237, v170, s36, v212\n"
    "v_fma_f32 v239, v171, s36, v212\n"
    "v_exp_f32 v237, v237\n"
    "v_exp_f32 v239, v239\n"
    "s_nop 0\n"
    "v_cvt_pk_bf16_f32 v197, v237, v239\n"
    "v_fma_f32 v237, v172, s36, v212\n"
    "v_fma_f32 v239, v173, s36, v212\n"
    "v_exp_f32 v237, v237\n"
    "v_exp_f32 v239, v239\n"
    "s_nop 0\n"
    "v_cvt_pk_bf16_f32 v198, v237, v239\n"
    "v_fma_f32 v237, v174, s36, v212\n"
    "v_fma_f32 v239, v175, s36, v212\n"
    "v_exp_f32 v237, v237\n"
    "v_exp_f32 v239, v239\n"
    "s_nop 0\n"
    "v_cvt_pk_bf16_f32 v199, v237, v239\n"
    "v_max3_f32 v236, v184, v185, v186\n"
    "v_max3_f32 v236, v236, v187, v188\n"
    "v_max3_f32 v236, v236, v189, v190\n"
    "v_max_f32 v236, v236, v191\n"
    "v_mov_b32 v237, v236\n"
    "s_nop 1\n"
    "v_permlane16_swap_b32 v236, v237\n"
    "v_max_f32 v236, v236, v237\n"
    "v_mov_b32 v237, v236\n"
    "s_nop 1\n"
    "v_permlane32_swap_b32 v236, v237\n"
    "v_max_f32 v236, v236, v237\n"
    "v_fma_f32 v236, v236, s36, v245\n"
    "v_log_f32 v237, v227\n"
    "s_nop 0\n"
    "v_add_f32 v237, 0xc1300000, v237\n"
    "v_max3_f32 v236, v236, v237, 0\n"
    "v_exp_f32 v238, -v236\n"
    "s_nop 0\n"
    "v_mul_f32 v227, v238, v227\n"
    "v_sub_f32 v245, v245, v236\n"
    "v_mul_f32 v96, v238, v96\n"
    "v_mul_f32 v97, v238, v97\n"
    "v_mul_f32 v98, v238, v98\n"
    "v_mul_f32 v99, v238, v99\n"
    "v_mul_f32 v100, v238, v100\n"
    "v_mul_f32 v101, v238, v101\n"
    "v_mul_f32 v102, v238, v102\n"
    "v_mul_f32 v103, v238, v103\n"
    "v_mul_f32 v104, v238, v104\n"
    "v_mul_f32 v105, v238, v105\n"
    "v_mul_f32 v106, v238, v106\n"
    "v_mul_f32 v107, v238, v107\n"
    "v_mul_f32 v108, v238, v108\n"
    "v_mul_f32 v109, v238, v109\n"
    "v_mul_f32 v110, v238, v110\n"
    "v_mul_f32 v111, v238, v111\n"
    "v_mul_f32 v112, v238, v112\n"
    "v_mul_f32 v113, v238, v113\n"
    "v_mul_f32 v114, v238, v114\n"
    "v_mul_f32 v115, v238, v115\n"
    "v_mul_f32 v116, v238, v116\n"
    "v_mul_f32 v117, v238, v117\n"
    "v_mul_f32 v118, v238, v118\n"
    "v_mul_f32 v119, v238, v119\n"
    "v_mul_f32 v120, v238, v120\n"
    "v_mul_f32 v121, v238, v121\n"
    "v_mul_f32 v122, v238, v122\n"
    "v_mul_f32 v123, v238, v123\n"
    "v_mul_f32 v124, v238, v124\n"
    "v_mul_f32 v125, v238, v125\n"
    "v_mul_f32 v126, v238, v126\n"
    "v_mul_f32 v127, v238, v127\n"
    "v_fma_f32 v237, v184, s36, v245\n"
    "v_fma_f32 v239, v185, s36, v245\n"
    "v_exp_f32 v237, v237\n"
    "v_exp_f32 v239, v239\n"
    "s_nop 0\n"
    "v_cvt_pk_bf16_f32 v204, v237, v239\n"
    "v_fma_f32 v237, v186, s36, v245\n"
    "v_fma_f32 v239, v187, s36, v245\n"
    "v_exp_f32 v237, v237\n"
    "v_exp_f32 v239, v239\n"
    "s_nop 0\n"
    "v_cvt_pk_bf16_f32 v205, v237, v239\n"
    "v_fma_f32 v237, v188, s36, v245\n"
    "v_fma_f32 v239, v189, s36, v245\n"
    "v_exp_f32 v237, v237\n"
    "v_exp_f32 v239, v239\n"
    "s_nop 0\n"
    "v_cvt_pk_bf16_f32 v206, v237, v239\n"
    "v_fma_f32 v237, v190, s36, v245\n"
    "v_fma_f32 v239, v191, s36, v245\n"
    "v_exp_f32 v237, v237\n"
    "v_exp_f32 v239, v239\n"
    "s_nop 0\n"
    "v_cvt_pk_bf16_f32 v207, v237, v239\n"
    "s_nop 1\n"
    "v_mfma_f32_16x16x32_bf16 v[228:231], a[160:163], v[192:195], v[224:227]\n"
    "v_mfma_f32_16x16x32_bf16 v[228:231], a[164:167], v[196:199], v[228:231]\n"
    "v_mfma_f32_16x16x32_bf16 v[228:231], a[172:175], v[204:207], v[228:231]\n"
    "v_fma_f32 v232, v136, s36, v212\n"
    "v_fma_f32 v233, v137, s36, v212\n"
    "v_fma_f32 v234, v138, s36, v212\n"
    "v_fma_f32 v235, v139, s36, v212\n"
    "v_exp_f32 v232, v232\n"
    "v_exp_f32 v233, v233\n"
    "v_exp_f32 v234, v234\n"
    "v_exp_f32 v235, v235\n"
    "s_nop 7\n"
    "s_nop 7\n"
    "s_add_u32 s86, s86, 4\n"
    "s_addc_u32 s87, s87, 0\n"
    "s_cmp_lg_u32 s34, 0\n"
    "s_setpc_b64 s[86:87]\n"
    "L_rare_1Sn_013_:\n"
    "s_cselect_b32 s34, 1, 0\n"
    "s_nop 7\n"
    "s_nop 7\n"
    "v_max3_f32 v236, v160, v161, v162\n"
    "v_max3_f32 v236, v236, v163, v164\n"
    "v_max3_f32 v236, v236, v165, v166\n"
    "v_max_f32 v236, v236, v167\n"
    "v_mov_b32 v237, v236\n"
    "s_nop 1\n"
    "v_permlane16_swap_b32 v236, v237\n"
    "v_max_f32 v236, v236, v237\n"
    "v_mov_b32 v237, v236\n"
    "s_nop 1\n"
    "v_permlane32_swap_b32 v236, v237\n"
    "v_max_f32 v236, v236, v237\n"
    "v_fma_f32 v236, v236, s36, v208\n"
    "v_log_f32 v237, v224\n"
    "s_nop 0\n"
    "v_add_f32 v237, 0xc1300000, v237\n"
    "v_max3_f32 v236, v236, v237, 0\n"
    "v_exp_f32 v238, -v236\n"
    "s_nop 0\n"
    "v_mul_f32 v224, v238, v224\n"
    "v_sub_f32 v208, v208, v236\n"
    "v_sub_f32 v209, v209, v236\n"
    "v_sub_f32 v210, v210, v236\n"
    "v_sub_f32 v211, v211, v236\n"
    "v_mul_f32 v0, v238, v0\n"
    "v_mul_f32 v1, v238, v1\n"
    "v_mul_f32 v2, v238, v2\n"
    "v_mul_f32 v3, v238, v3\n"
    "v_mul_f32 v4, v238, v4\n"
    "v_mul_f32 v5, v238, v5\n"
    "v_mul_f32 v6, v238, v6\n"
    "v_mul_f32 v7, v238, v7\n"
    "v_mul_f32 v8, v238, v8\n"
    "v_mul_f32 v9, v238, v9\n"
    "v_mul_f32 v10, v238, v10\n"
    "v_mul_f32 v11, v238, v11\n"
    "v_mul_f32 v12, v238, v12\n"
    "v_mul_f32 v13, v238, v13\n"
    "v_mul_f32 v14, v238, v14\n"
    "v_mul_f32 v15, v238, v15\n"
    "v_mul_f32 v16, v238, v16\n"
    "v_mul_f32 v17, v238, v17\n"
    "v_mul_f32 v18, v238, v18\n"
    "v_mul_f32 v19, v238, v19\n"
    "v_mul_f32 v20, v238, v20\n"
    "v_mul_f32 v21, v238, v21\n"
    "v_mul_f32 v22, v238, v22\n"
    "v_mul_f32 v23, v238, v23\n"
    "v_mul_f32 v24, v238, v24\n"
    "v_mul_f32 v25, v238, v25\n"
    "v_mul_f32 v26, v238, v26\n"
    "v_mul_f32 v27, v238, v27\n"
    "v_mul_f32 v28, v238, v28\n"
    "v_mul_f32 v29, v238, v29\n"
    "v_mul_f32 v30, v238, v30\n"
    "v_mul_f32 v31, v238, v31\n"
    "v_fma_f32 v237, v160, s36, v208\n"
    "v_fma_f32 v239, v161, s36, v208\n"
    "v_exp_f32 v237, v237\n"
    "v_exp_f32 v239, v239\n"
    "s_nop 0\n"
    "v_cvt_pk_bf16_f32 v192, v237, v239\n"
    "v_fma_f32 v237, v162, s36, v208\n"
    "v_fma_f32 v239, v163, s36, v208\n"
    "v_exp_f32 v237, v237\n"
    "v_exp_f32 v239, v239\n"
    "s_nop 0\n"
    "v_cvt_pk_bf16_f32 v193, v237, v239\n"
    "v_fma_f32 v237, v164, s36, v208\n"
    "v_fma_f32 v239, v165, s36, v208\n"
    "v_exp_f32 v237, v237\n"
    "v_exp_f32 v239, v239\n"
    "s_nop 0\n"
    "v_cvt_pk_bf16_f32 v194, v237, v239\n"
    "v_fma_f32 v237, v166, s36, v208\n"
    "v_fma_f32 v239, v167, s36, v208\n"
    "v_exp_f32 v237, v237\n"
    "v_exp_f32 v239, v239\n"
    "s_nop 0\n"
    "v_cvt_pk_bf16_f32 v195, v237, v239\n"
    "v_max3_f32 v236, v168, v169, v170\n"
    "v_max3_f32 v236, v236, v171, v172\n"
    "v_max3_f32 v236, v236, v173, v174\n"
    "v_max_f32 v236, v236, v175\n"
    "v_mov_b32 v237, v236\n"
    "s_nop 1\n"
    "v_permlane16_swap_b32 v236, v237\n"
    "v_max_f32 v236, v236, v237\n"
    "v_mov_b32 v237, v236\n"
    "s_nop 1\n"
    "v_permlane32_swap_b32 v236, v237\n"
    "v_max_f32 v236, v236, v237\n"
    "v_fma_f32 v236, v236, s36, v212\n"
    "v_log_f32 v237, v225\n"
    "s_nop 0\n"
    "v_add_f32 v237, 0xc1300000, v237\n"
    "v_max3_f32 v236, v236, v237, 0\n"
    "v_exp_f32 v238, -v236\n"
    "s_nop 0\n"
    "v_mul_f32 v225, v238, v225\n"
    "v_sub_f32 v212, v212, v236\n"
    "v_sub_f32 v213, v213, v236\n"
    "v_sub_f32 v214, v214, v236\n"
    "v_sub_f32 v215, v215, v236\n"
    "v_mul_f32 v32, v238, v32\n"
    "v_mul_f32 v33, v238, v33\n"
    "v_mul_f32 v34, v238, v34\n"
    "v_mul_f32 v35, v238, v35\n"
    "v_mul_f32 v36, v238, v36\n"
    "v_mul_f32 v37, v238, v37\n"
    "v_mul_f32 v38, v238, v38\n"
    "v_mul_f32 v39, v238, v39\n"
    "v_mul_f32 v40, v238, v40\n"
    "v_mul_f32 v41, v238, v41\n"
    "v_mul_f32 v42, v238, v42\n"
    "v_mul_f32 v43, v238, v43\n"
    "v_mul_f32 v44, v238, v44\n"
    "v_mul_f32 v45, v238, v45\n"
    "v_mul_f32 v46, v238, v46\n"
    "v_mul_f32 v47, v238, v47\n"
    "v_mul_f32 v48, v238, v48\n"
    "v_mul_f32 v49, v238, v49\n"
    "v_mul_f32 v50, v238, v50\n"
    "v_mul_f32 v51, v238, v51\n"
    "v_mul_f32 v52, v238, v52\n"
    "v_mul_f32 v53, v238, v53\n"
    "v_mul_f32 v54, v238, v54\n"
    "v_mul_f32 v55, v238, v55\n"
    "v_mul_f32 v56, v238, v56\n"
    "v_mul_f32 v57, v238, v57\n"
    "v_mul_f32 v58, v238, v58\n"
    "v_mul_f32 v59, v238, v59\n"
    "v_mul_f32 v60, v238, v60\n"
    "v_mul_f32 v61, v238, v61\n"
    "v_mul_f32 v62, v238, v62\n"
    "v_mul_f32 v63, v238, v63\n"
    "v_fma_f32 v237, v168, s36, v212\n"
    "v_fma_f32 v239, v169, s36, v212\n"
    "v_exp_f32 v237, v237\n"
    "v_exp_f32 v239, v239\n"
    "s_nop 0\n"
    "v_cvt_pk_bf16_f32 v196, v237, v239\n"
    "v_fma_f32 v237, v170, s36, v212\n"
    "v_fma_f32 v239, v171, s36, v212\n"
    "v_exp_f32 v237, v237\n"
    "v_exp_f32 v239, v239\n"
    "s_nop 0\n"
    "v_cvt_pk_bf16_f32 v197, v237, v239\n"
    "v_fma_f32 v237, v172, s36, v212\n"
    "v_fma_f32 v239, v173, s36, v212\n"
    "v_exp_f32 v237, v237\n"
    "v_exp_f32 v239, v239\n"
    "s_nop 0\n"
    "v_cvt_pk_bf16_f32 v198, v237, v239\n"
    "v_fma_f32 v237, v174, s36, v212\n"
    "v_fma_f32 v239, v175, s36, v212\n"
    "v_exp_f32 v237, v237\n"
    "v_exp_f32 v239, v239\n"
    "s_nop 0\n"
    "v_cvt_pk_bf16_f32 v199, v237, v239\n"
    "v_max3_f32 v236, v184, v185, v186\n"
    "v_max3_f32 v236, v236, v187, v188\n"
    "v_max3_f32 v236, v236, v189, v190\n"
    "v_max_f32 v236, v236, v191\n"
    "v_mov_b32 v237, v236\n"
    "s_nop 1\n"
    "v_permlane16_swap_b32 v236, v237\n"
    "v_max_f32 v236, v236, v237\n"
    "v_mov_b32 v237, v236\n"
    "s_nop 1\n"
    "v_permlane32_swap_b32 v236, v237\n"
    "v_max_f32 v236, v236, v237\n"
    "v_fma_f32 v236, v236, s36, v245\n"
    "v_log_f32 v237, v227\n"
    "s_nop 0\n"
    "v_add_f32 v237, 0xc1300000, v237\n"
    "v_max3_f32 v236, v236, v237, 0\n"
    "v_exp_f32 v238, -v236\n"
    "s_nop 0\n"
    "v_mul_f32 v227, v238, v227\n"
    "v_sub_f32 v245, v245, v236\n"
    "v_mul_f32 v96, v238, v96\n"
    "v_mul_f32 v97, v238, v97\n"
    "v_mul_f32 v98, v238, v98\n"
    "v_mul_f32 v99, v238, v99\n"
    "v_mul_f32 v100, v238, v100\n"
    "v_mul_f32 v101, v238, v101\n"
    "v_mul_f32 v102, v238, v102\n"
    "v_mul_f32 v103, v238, v103\n"
    "v_mul_f32 v104, v238, v104\n"
    "v_mul_f32 v105, v238, v105\n"
    "v_mul_f32 v106, v238, v106\n"
    "v_mul_f32 v107, v238, v107\n"
    "v_mul_f32 v108, v238, v108\n"
    "v_mul_f32 v109, v238, v109\n"
    "v_mul_f32 v110, v238, v110\n"
    "v_mul_f32 v111, v238, v111\n"
    "v_mul_f32 v112, v238, v112\n"
    "v_mul_f32 v113, v238, v113\n"
    "v_mul_f32 v114, v238, v114\n"
    "v_mul_f32 v115, v238, v115\n"
    "v_mul_f32 v116, v238, v116\n"
    "v_mul_f32 v117, v238, v117\n"
    "v_mul_f32 v118, v238, v118\n"
    "v_mul_f32 v119, v238, v119\n"
    "v_mul_f32 v120, v238, v120\n"
    "v_mul_f32 v121, v238, v121\n"
    "v_mul_f32 v122, v238, v122\n"
    "v_mul_f32 v123, v238, v123\n"
    "v_mul_f32 v124, v238, v124\n"
    "v_mul_f32 v125, v238, v125\n"
    "v_mul_f32 v126, v238, v126\n"
    "v_mul_f32 v127, v238, v127\n"
    "v_fma_f32 v237, v184, s36, v245\n"
    "v_fma_f32 v239, v185, s36, v245\n"
    "v_exp_f32 v237, v237\n"
    "v_exp_f32 v239, v239\n"
    "s_nop 0\n"
    "v_cvt_pk_bf16_f32 v204, v237, v239\n"
    "v_fma_f32 v237, v186, s36, v245\n"
    "v_fma_f32 v239, v187, s36, v245\n"
    "v_exp_f32 v237, v237\n"
    "v_exp_f32 v239, v239\n"
    "s_nop 0\n"
    "v_cvt_pk_bf16_f32 v205, v237, v239\n"
    "v_fma_f32 v237, v188, s36, v245\n"
    "v_fma_f32 v239, v189, s36, v245\n"
    "v_exp_f32 v237, v237\n"
    "v_exp_f32 v239, v239\n"
    "s_nop 0\n"
    "v_cvt_pk_bf16_f32 v206, v237, v239\n"
    "v_fma_f32 v237, v190, s36, v245\n"
    "v_fma_f32 v239, v191, s36, v245\n"
    "v_exp_f32 v237, v237\n"
    "v_exp_f32 v239, v239\n"
    "s_nop 0\n"
    "v_cvt_pk_bf16_f32 v207, v237, v239\n"
    "s_nop 1\n"
    "v_mfma_f32_16x16x32_bf16 v[228:231], a[160:163], v[192:195], v[224:227]\n"
    "v_mfma_f32_16x16x32_bf16 v[228:231], a[164:167], v[196:199], v[228:231]\n"
    "v_mfma_f32_16x16x32_bf16 v[228:231], a[172:175], v[204:207], v[228:231]\n"
    "s_nop 7\n"
    "s_nop 7\n"
    "s_add_u32 s86, s86, 4\n"
    "s_addc_u32 s87, s87, 0\n"
    "s_cmp_lg_u32 s34, 0\n"
    "s_setpc_b64 s[86:87]\n"
    "L_end:\n"
    :: "s"(__builtin_amdgcn_kernarg_segment_ptr()), "s"(w + blockIdx.x * 4), "s"((unsigned)(size_t)lds)
    : "v0","v1","v2","v3","v4","v5","v6","v7","v8","v9","v10","v11","v12","v13","v14","v15","v16","v17","v18","v19","v20","v21","v22","v23","v24","v25","v26","v27","v28","v29","v30","v31","v32","v33","v34","v35","v36","v37","v38","v39","v40","v41","v42","v43","v44","v45","v46","v47","v48","v49","v50","v51","v52","v53","v54","v55","v56","v57","v58","v59","v60","v61","v62","v63","v64","v65","v66","v67","v68","v69","v70","v71","v72","v73","v74","v75","v76","v77","v78","v79","v80","v81","v82","v83","v84","v85","v86","v87","v88","v89","v90","v91","v92","v93","v94","v95","v96","v97","v98","v99","v100","v101","v102","v103","v104","v105","v106","v107","v108","v109","v110","v111","v112","v113","v114","v115","v116","v117","v118","v119","v120","v121","v122","v123","v124","v125","v126","v127","v128","v129","v130","v131","v132","v133","v134","v135","v136","v137","v138","v139","v140","v141","v142","v143","v144","v145","v146","v147","v148","v149","v150","v151","v152","v153","v154","v155","v156","v157","v158","v159","v160","v161","v162","v163","v164","v165","v166","v167","v168","v169","v170","v171","v172","v173","v174","v175","v176","v177","v178","v179","v180","v181","v182","v183","v184","v185","v186","v187","v188","v189","v190","v191","v192","v193","v194","v195","v196","v197","v198","v199","v200","v201","v202","v203","v204","v205","v206","v207","v208","v209","v210","v211","v212","v213","v214","v215","v216","v217","v218","v219","v220","v221","v222","v223","v224","v225","v226","v227","v228","v229","v230","v231","v232","v233","v234","v235","v236","v237","v238","v239","v240","v241","v242","v243","v244","v245","v246","v247","v248","v249","v250","v251","v252","v253","v254","v255","a0","a1","a2","a3","a4","a5","a6","a7","a8","a9","a10","a11","a12","a13","a14","a15","a16","a17","a18","a19","a20","a21","a22","a23","a24","a25","a26","a27","a28","a29","a30","a31","a32","a33","a34","a35","a36","a37","a38","a39","a40","a41","a42","a43","a44","a45","a46","a47","a48","a49","a50","a51","a52","a53","a54","a55","a56","a57","a58","a59","a60","a61","a62","a63","a64","a65","a66","a67","a68","a69","a70","a71","a72","a73","a74","a75","a76","a77","a78","a79","a80","a81","a82","a83","a84","a85","a86","a87","a88","a89","a90","a91","a92","a93","a94","a95","a96","a97","a98","a99","a100","a101","a102","a103","a104","a105","a106","a107","a108","a109","a110","a111","a112","a113","a114","a115","a116","a117","a118","a119","a120","a121","a122","a123","a124","a125","a126","a127","a128","a129","a130","a131","a132","a133","a134","a135","a136","a137","a138","a139","a140","a141","a142","a143","a144","a145","a146","a147","a148","a149","a150","a151","a152","a153","a154","a155","a156","a157","a158","a159","a160","a161","a162","a163","a164","a165","a166","a167","a168","a169","a170","a171","a172","a173","a174","a175","a176","a177","a178","a179","a180","a181","a182","a183","a184","a185","a186","a187","a188","a189","a190","a191","a192","a193","a194","a195","a196","a197","a198","a199","a200","a201","a202","a203","a204","a205","a206","a207","a208","a209","a210","a211","a212","a213","a214","a215","a216","a217","a218","a219","a220","a221","a222","a223","a224","a225","a226","a227","a228","a229","a230","a231","a232","a233","a234","a235","a236","a237","a238","a239","a240","a241","a242","a243","a244","a245","a246","a247","a248","a249","a250","a251","a252","a253","a254","a255","s14","s15","s16","s17","s18","s19","s20","s21","s22","s23","s33","s34","s35","s36","s37","s38","s39","s40","s41","s42","s43","s44","s45","s46","s47","s48","s49","s50","s51","s52","s53","s54","s55","s56","s57","s58","s59","s60","s61","s62","s63","s64","s65","s66","s67","s68","s69","s70","s71","s72","s73","s74","s75","s76","s77","s78","s79","s80","s81","s82","s83","s84","s85","s86","s87","s88","s89","s90","s91","s92","s93","s94","s95","s96","s97","s98","s99","vcc","scc","memory");
}
extern "C" void kernel_launch(void* const* d_in, const int* in_sizes, int n_in, void* d_out, int out_size, void* d_ws, size_t ws_size, hipStream_t stream) {
  attn_causal_kernel<<<256, 256, 0, stream>>>((const char*)d_in[0], (const char*)d_in[1], (const char*)d_in[2], (char*)d_out);
}
```
